```python
import torch

def _softmax(x, axis=-1):
    return torch.softmax(x, dim=axis)
def _gelu(x):
    return torch.nn.functional.gelu(x, approximate="tanh")
def _silu(x):
    return torch.nn.functional.silu(x)
def _sigmoid(x):
    return torch.sigmoid(x)
def _layernorm(x, axis=-1, eps=1e-5):
    m = x.mean(dim=axis, keepdim=True)
    v = x.var(dim=axis, keepdim=True, unbiased=False)
    return (x - m) / torch.sqrt(v + eps)
def _scatter_add(tgt, idx, src, axis=0):
    return tgt.clone().index_add_(axis, idx, src)
def _segment_sum(src, seg, n):
    out = torch.zeros((n,) + src.shape[1:], dtype=src.dtype)
    return out.index_add_(0, seg, src)
def _pad(x, pw):
    flat = tuple(v for pair in reversed(pw) for v in pair)
    return torch.nn.functional.pad(x, flat)

kernel_name = 'standalone__mha_block_cached__near_ridge'

def setup_inputs():

    _w = (lambda a: (lambda t: t.to(torch.bfloat16) if t.is_floating_point() else t)(torch.as_tensor(a)))
    B, Sq, D = 8, 224, 448
    H, Sk, Dh = 16, 1024, 256
    return {
        "x": _w((lambda *s: torch.randn(*s, dtype=torch.bfloat16) * 0.8)(B, Sq, D)),
        "k_cache": _w((lambda *s: torch.randn(*s, dtype=torch.bfloat16) * 0.8)(B, H, Sk, Dh)),
        "v_cache": _w((lambda *s: torch.randn(*s, dtype=torch.bfloat16) * 0.05)(B, H, Sk, Dh)),
        "wq": _w((lambda *s: torch.randn(*s, dtype=torch.bfloat16) * 0.8)(D, H * Dh) * (D ** -0.5)),
        "wo": _w((lambda *s: torch.randn(*s, dtype=torch.bfloat16) * 0.05)(H * Dh, D) * ((H * Dh) ** -0.5)),
    }

def reference(x, k_cache, v_cache, wq, wo):
    B, Sq, D = x.shape
    H, Dh = 16, 256
    q = (lambda x, *d: x.permute(*d))((x @ wq).reshape(B, Sq, H, Dh), 0, 2, 1, 3)
    scores = torch.einsum("bhqd,bhkd->bhqk", q, k_cache) * (Dh ** -0.5)
    p = _softmax(scores, axis=-1)
    o = torch.einsum("bhqk,bhkd->bhqd", p, v_cache)
    return (lambda x, *d: x.permute(*d))(o, 0, 2, 1, 3).reshape(B, Sq, H * Dh) @ wo
```

```cpp
#include <hip/hip_runtime.h>
#include <hip/hip_bf16.h>
#include <stdint.h>

typedef short  bf16x8 __attribute__((ext_vector_type(8)));
typedef short  s16x4  __attribute__((ext_vector_type(4)));
typedef float  f32x16 __attribute__((ext_vector_type(16)));
typedef unsigned u32x4 __attribute__((ext_vector_type(4)));
typedef unsigned u32x2 __attribute__((ext_vector_type(2)));
typedef float  f32x2  __attribute__((ext_vector_type(2)));
typedef __bf16 bf2    __attribute__((ext_vector_type(2)));
#define LDS3 __attribute__((address_space(3)))
#define MFMA(a, b, c) __builtin_amdgcn_mfma_f32_32x32x16_bf16(__builtin_bit_cast(bf8_t, a), __builtin_bit_cast(bf8_t, b), c, 0, 0, 0)
typedef __bf16 bf8_t __attribute__((ext_vector_type(8)));

constexpr int B = 8, SQ = 224, DM = 448, H = 16, SK = 1024, DH = 256, HD = H * DH;

__device__ __forceinline__ unsigned cvtpk(float lo, float hi) {
  f32x2 v = {lo, hi}; bf2 b = __builtin_convertvector(v, bf2); return __builtin_bit_cast(unsigned, b);
}
__device__ __forceinline__ s16x4 trread(const char* p) {
  return __builtin_amdgcn_ds_read_tr16_b64_v4i16((LDS3 s16x4*)p);
}
__device__ __forceinline__ bf16x8 cat(s16x4 a, s16x4 b) { return __builtin_shufflevector(a, b, 0, 1, 2, 3, 4, 5, 6, 7); }

__global__ __launch_bounds__(256, 1) void attn_kernel(const unsigned short* __restrict__ x, const unsigned short* __restrict__ kc,
                                                      const unsigned short* __restrict__ vc, const unsigned short* __restrict__ wq,
                                                      unsigned short* __restrict__ og) {
  __shared__ __attribute__((aligned(16))) char lds[65536];
  const int tid = threadIdx.x, lane = tid & 63, w = tid >> 6, r = lane & 31, h = lane >> 5;
  const int blk = (lane >> 4) & 1, q4 = (lane & 15) >> 2, p4 = lane & 3;
  const int bid = blockIdx.x;
  const int b = bid & 7, rest = bid >> 3, hd = rest >> 1, qh = rest & 1;
  const int row0 = qh * 112;
  f32x16 acc[8];
  for (int i = 0; i < 8; ++i) for (int j = 0; j < 16; ++j) acc[i][j] = 0.f;
  char* xs = lds;
  char* wsm = lds + 16384;
  for (int kc0 = 0; kc0 < DM; kc0 += 64) {
    __syncthreads();
    for (int i = tid; i < 128 * 8; i += 256) { int row = i >> 3, ch = i & 7; int gr = row < 112 ? row : 111;
      u32x4 v = *(const u32x4*)(x + ((size_t)(b * SQ + row0 + gr) * DM + kc0 + ch * 8));
      *(u32x4*)(xs + row * 128 + ((ch ^ ((row >> 1) & 7)) << 4)) = v; }
    for (int i = tid; i < 64 * 32; i += 256) { int row = i >> 5, ch = i & 31;
      u32x4 v = *(const u32x4*)(wq + ((size_t)(kc0 + row) * HD + hd * DH + ch * 8));
      *(u32x4*)(wsm + row * 512 + ((ch ^ ((row & 3) << 2)) << 4)) = v; }
    __syncthreads();
    #pragma unroll
    for (int ks = 0; ks < 4; ++ks) {
      const int xr = w * 32 + r;
      bf16x8 bx = *(const bf16x8*)(xs + xr * 128 + (((2 * ks + h) ^ ((xr >> 1) & 7)) << 4));
      const int pp = ((p4 & 1) << 1) | (p4 >> 1);
      #pragma unroll
      for (int db = 0; db < 8; ++db) {
        s16x4 t[2];
        #pragma unroll
        for (int tt = 0; tt < 2; ++tt) { int row = 16 * ks + 8 * h + 4 * tt + q4; int col = 32 * db + 16 * blk + 4 * pp;
          int ch = col >> 3; t[tt] = trread(wsm + row * 512 + ((ch ^ ((row & 3) << 2)) << 4) + (col & 7) * 2); }
        acc[db] = MFMA(cat(t[0], t[1]), bx, acc[db]);
      }
    }
  }
  bf16x8 qf[16];
  #pragma unroll
  for (int db = 0; db < 8; ++db)
    #pragma unroll
    for (int s = 0; s < 2; ++s) { u32x4 u; for (int e = 0; e < 4; ++e) u[e] = cvtpk(acc[db][8 * s + 2 * e], acc[db][8 * s + 2 * e + 1]);
      qf[2 * db + s] = __builtin_bit_cast(bf16x8, u); }
  f32x16 o[8];
  for (int i = 0; i < 8; ++i) for (int j = 0; j < 16; ++j) o[i][j] = 0.f;
  float m = -1e30f, l = 0.f;
  const float C = 0.0625f * 1.4426950408889634f;
  const unsigned short* kb = kc + (size_t)(b * H + hd) * SK * DH;
  const unsigned short* vb = vc + (size_t)(b * H + hd) * SK * DH;
  char* ks_ = lds; char* vs_ = lds + 32768;
  for (int t = 0; t < SK / 64; ++t) {
    __syncthreads();
    for (int i = tid; i < 64 * 32; i += 256) { int row = i >> 5, ch = i & 31;
      u32x4 v = *(const u32x4*)(kb + (size_t)(t * 64 + row) * DH + ch * 8);
      *(u32x4*)(ks_ + row * 512 + ((ch ^ (row & 15)) << 4)) = v;
      u32x4 u = *(const u32x4*)(vb + (size_t)(t * 64 + row) * DH + ch * 8);
      *(u32x4*)(vs_ + row * 512 + ((ch ^ ((row & 3) << 2)) << 4)) = u; }
    __syncthreads();
    f32x16 s[2];
    #pragma unroll
    for (int kb2 = 0; kb2 < 2; ++kb2) { for (int j = 0; j < 16; ++j) s[kb2][j] = 0.f;
      #pragma unroll
      for (int st = 0; st < 16; ++st) { int row = kb2 * 32 + r;
        bf16x8 a = *(const bf16x8*)(ks_ + row * 512 + (((2 * st + h) ^ (row & 15)) << 4));
        s[kb2] = MFMA(a, qf[st], s[kb2]); } }
    float mx = s[0][0];
    for (int j = 0; j < 16; ++j) { mx = fmaxf(mx, s[0][j]); mx = fmaxf(mx, s[1][j]); }
    mx = fmaxf(mx, __shfl_xor(mx, 32));
    float mn = fmaxf(m, mx * C); float al = __builtin_amdgcn_exp2f(m - mn); m = mn;
    float rs = 0.f;
    #pragma unroll
    for (int kb2 = 0; kb2 < 2; ++kb2) for (int j = 0; j < 16; ++j) { float pv = __builtin_amdgcn_exp2f(s[kb2][j] * C - m); s[kb2][j] = pv; rs += pv; }
    rs += __shfl_xor(rs, 32);
    l = l * al + rs;
    #pragma unroll
    for (int db = 0; db < 8; ++db) for (int j = 0; j < 16; ++j) o[db][j] *= al;
    #pragma unroll
    for (int st = 0; st < 4; ++st) { u32x4 u; const int kb2 = st >> 1, s0 = (st & 1) * 8;
      for (int e = 0; e < 4; ++e) u[e] = cvtpk(s[kb2][s0 + 2 * e], s[kb2][s0 + 2 * e + 1]);
      bf16x8 pf = __builtin_bit_cast(bf16x8, u);
      #pragma unroll
      for (int db = 0; db < 8; ++db) { s16x4 t2[2];
        #pragma unroll
        for (int tt = 0; tt < 2; ++tt) { int row = 16 * st + 8 * tt + 4 * h + q4; int col = 32 * db + 16 * blk + 4 * p4; int ch = col >> 3;
          t2[tt] = trread(vs_ + row * 512 + ((ch ^ ((row & 3) << 2)) << 4) + (col & 7) * 2); }
        o[db] = MFMA(cat(t2[0], t2[1]), pf, o[db]); } }
  }
  const float il = 1.f / l;
  const int qrow = w * 32 + r;
  if (qrow < 112) {
    unsigned short* op = og + (size_t)(b * SQ + row0 + qrow) * HD + hd * DH;
    #pragma unroll
    for (int db = 0; db < 8; ++db)
      #pragma unroll
      for (int g = 0; g < 4; ++g) { u32x2 u = {cvtpk(o[db][4 * g] * il, o[db][4 * g + 1] * il), cvtpk(o[db][4 * g + 2] * il, o[db][4 * g + 3] * il)};
        *(u32x2*)(op + 32 * db + 8 * g + 4 * h) = u; }
  }
}

__global__ __launch_bounds__(256) void oproj_kernel(const unsigned short* __restrict__ og, const unsigned short* __restrict__ wo, unsigned short* __restrict__ out) {
  __shared__ __attribute__((aligned(16))) char lds[16384];
  const int tid = threadIdx.x, lane = tid & 63, w = tid >> 6, r = lane & 31, h = lane >> 5;
  const int blk = (lane >> 4) & 1, q4 = (lane & 15) >> 2, p4 = lane & 3;
  const int m0 = (blockIdx.x / 7) * 64, n0 = (blockIdx.x % 7) * 64;
  const int wm = w >> 1, wn = w & 1;
  char* os = lds;
  char* wsm = lds + 8192;
  f32x16 acc; for (int j = 0; j < 16; ++j) acc[j] = 0.f;
  for (int k0 = 0; k0 < HD; k0 += 64) {
    __syncthreads();
    for (int i = tid; i < 64 * 8; i += 256) { int row = i >> 3, ch = i & 7;
      u32x4 v = *(const u32x4*)(og + (size_t)(m0 + row) * HD + k0 + ch * 8);
      *(u32x4*)(os + row * 128 + ((ch ^ ((row >> 1) & 7)) << 4)) = v;
      u32x4 u = *(const u32x4*)(wo + (size_t)(k0 + row) * DM + n0 + ch * 8);
      *(u32x4*)(wsm + row * 128 + (ch << 4)) = u; }
    __syncthreads();
    #pragma unroll
    for (int ks = 0; ks < 4; ++ks) { const int xr = wm * 32 + r;
      bf16x8 bx = *(const bf16x8*)(os + xr * 128 + (((2 * ks + h) ^ ((xr >> 1) & 7)) << 4));
      s16x4 t[2];
      for (int tt = 0; tt < 2; ++tt) { int row = 16 * ks + 8 * h + 4 * tt + q4; int col = 32 * wn + 16 * blk + 4 * p4;
        t[tt] = trread(wsm + row * 128 + col * 2); }
      acc = MFMA(cat(t[0], t[1]), bx, acc); }
  }
  unsigned short* op = out + (size_t)(m0 + wm * 32 + r) * DM + n0 + wn * 32;
  for (int g = 0; g < 4; ++g) { u32x2 u = {cvtpk(acc[4 * g], acc[4 * g + 1]), cvtpk(acc[4 * g + 2], acc[4 * g + 3])};
    *(u32x2*)(op + 8 * g + 4 * h) = u; }
}

extern "C" void kernel_launch(void* const* d_in, const int* in_sizes, int n_in, void* d_out, int out_size, void* d_ws, size_t ws_size, hipStream_t stream) {
  const unsigned short* x = (const unsigned short*)d_in[0];
  const unsigned short* kc = (const unsigned short*)d_in[1];
  const unsigned short* vc = (const unsigned short*)d_in[2];
  const unsigned short* wq = (const unsigned short*)d_in[3];
  const unsigned short* wo = (const unsigned short*)d_in[4];
  unsigned short* og = (unsigned short*)d_ws;
  attn_kernel<<<256, 256, 0, stream>>>(x, kc, vc, wq, og);
  oproj_kernel<<<28 * 7, 256, 0, stream>>>(og, wo, (unsigned short*)d_out);
}
```

```cpp
#include <hip/hip_runtime.h>
#include <hip/hip_bf16.h>
#include <stdint.h>

typedef short  bf16x8 __attribute__((ext_vector_type(8)));
typedef short  s16x4  __attribute__((ext_vector_type(4)));
typedef float  f32x16 __attribute__((ext_vector_type(16)));
typedef unsigned u32x4 __attribute__((ext_vector_type(4)));
typedef unsigned u32x2 __attribute__((ext_vector_type(2)));
typedef float  f32x2  __attribute__((ext_vector_type(2)));
typedef __bf16 bf2    __attribute__((ext_vector_type(2)));
#define LDS3 __attribute__((address_space(3)))
#define MFMA(a, b, c) __builtin_amdgcn_mfma_f32_32x32x16_bf16(__builtin_bit_cast(bf8_t, a), __builtin_bit_cast(bf8_t, b), c, 0, 0, 0)
typedef __bf16 bf8_t __attribute__((ext_vector_type(8)));

constexpr int B = 8, SQ = 224, DM = 448, H = 16, SK = 1024, DH = 256, HD = H * DH;

__device__ __forceinline__ unsigned cvtpk(float lo, float hi) {
  f32x2 v = {lo, hi}; bf2 b = __builtin_convertvector(v, bf2); return __builtin_bit_cast(unsigned, b);
}
__device__ __forceinline__ s16x4 trread(const char* p) {
  return __builtin_amdgcn_ds_read_tr16_b64_v4i16((LDS3 s16x4*)p);
}
__device__ __forceinline__ bf16x8 cat(s16x4 a, s16x4 b) { return __builtin_shufflevector(a, b, 0, 1, 2, 3, 4, 5, 6, 7); }

__global__ __launch_bounds__(256, 1) void attn_kernel(const unsigned short* __restrict__ x, const unsigned short* __restrict__ kc,
                                                      const unsigned short* __restrict__ vc, const unsigned short* __restrict__ wq,
                                                      unsigned short* __restrict__ og) {
  __shared__ __attribute__((aligned(16))) char lds[65536];
  const int tid = threadIdx.x, lane = tid & 63, w = tid >> 6, r = lane & 31, h = lane >> 5;
  const int blk = (lane >> 4) & 1, q4 = (lane & 15) >> 2, p4 = lane & 3;
  const int bid = blockIdx.x;
  const int b = bid & 7, rest = bid >> 3, hd = rest >> 1, qh = rest & 1;
  const int row0 = qh * 112;
  f32x16 acc[8];
  for (int i = 0; i < 8; ++i) for (int j = 0; j < 16; ++j) acc[i][j] = 0.f;
  char* xs = lds;
  char* wsm = lds + 16384;
  for (int kc0 = 0; kc0 < DM; kc0 += 64) {
    __syncthreads();
    for (int i = tid; i < 128 * 8; i += 256) { int row = i >> 3, ch = i & 7; int gr = row < 112 ? row : 111;
      u32x4 v = *(const u32x4*)(x + ((size_t)(b * SQ + row0 + gr) * DM + kc0 + ch * 8));
      *(u32x4*)(xs + row * 128 + ((ch ^ ((row >> 1) & 7)) << 4)) = v; }
    for (int i = tid; i < 64 * 32; i += 256) { int row = i >> 5, ch = i & 31;
      u32x4 v = *(const u32x4*)(wq + ((size_t)(kc0 + row) * HD + hd * DH + ch * 8));
      *(u32x4*)(wsm + row * 512 + ((ch ^ ((row & 3) << 2)) << 4)) = v; }
    __syncthreads();
    #pragma unroll
    for (int ks = 0; ks < 4; ++ks) {
      const int xr = w * 32 + r;
      bf16x8 bx = *(const bf16x8*)(xs + xr * 128 + (((2 * ks + h) ^ ((xr >> 1) & 7)) << 4));
      const int pp = ((p4 & 1) << 1) | (p4 >> 1);
      #pragma unroll
      for (int db = 0; db < 8; ++db) {
        s16x4 t[2];
        #pragma unroll
        for (int tt = 0; tt < 2; ++tt) { int row = 16 * ks + 8 * h + 4 * tt + q4; int col = 32 * db + 16 * blk + 4 * pp;
          int ch = col >> 3; t[tt] = trread(wsm + row * 512 + ((ch ^ ((row & 3) << 2)) << 4) + (col & 7) * 2); }
        acc[db] = MFMA(cat(t[0], t[1]), bx, acc[db]);
      }
    }
  }
  bf16x8 qf[16];
  #pragma unroll
  for (int db = 0; db < 8; ++db)
    #pragma unroll
    for (int s = 0; s < 2; ++s) { u32x4 u; for (int e = 0; e < 4; ++e) u[e] = cvtpk(acc[db][8 * s + 2 * e], acc[db][8 * s + 2 * e + 1]);
      qf[2 * db + s] = __builtin_bit_cast(bf16x8, u); }
  f32x16 o[8];
  for (int i = 0; i < 8; ++i) for (int j = 0; j < 16; ++j) o[i][j] = 0.f;
  float m = -1e30f, l = 0.f;
  const float C = 0.0625f * 1.4426950408889634f;
  const unsigned short* kb = kc + (size_t)(b * H + hd) * SK * DH;
  const unsigned short* vb = vc + (size_t)(b * H + hd) * SK * DH;
  char* ks_ = lds; char* vs_ = lds + 32768;
  for (int t = 0; t < SK / 64; ++t) {
    __syncthreads();
    for (int i = tid; i < 64 * 32; i += 256) { int row = i >> 5, ch = i & 31;
      u32x4 v = *(const u32x4*)(kb + (size_t)(t * 64 + row) * DH + ch * 8);
      *(u32x4*)(ks_ + row * 512 + ((ch ^ (row & 15)) << 4)) = v;
      u32x4 u = *(const u32x4*)(vb + (size_t)(t * 64 + row) * DH + ch * 8);
      *(u32x4*)(vs_ + row * 512 + ((ch ^ ((row & 3) << 2)) << 4)) = u; }
    __syncthreads();
    f32x16 s[2];
    #pragma unroll
    for (int kb2 = 0; kb2 < 2; ++kb2) { for (int j = 0; j < 16; ++j) s[kb2][j] = 0.f;
      #pragma unroll
      for (int st = 0; st < 16; ++st) { int row = kb2 * 32 + r;
        bf16x8 a = *(const bf16x8*)(ks_ + row * 512 + (((2 * st + h) ^ (row & 15)) << 4));
        s[kb2] = MFMA(a, qf[st], s[kb2]); } }
    float mx = s[0][0];
    for (int j = 0; j < 16; ++j) { mx = fmaxf(mx, s[0][j]); mx = fmaxf(mx, s[1][j]); }
    mx = fmaxf(mx, __shfl_xor(mx, 32));
    float mn = fmaxf(m, mx * C); float al = __builtin_amdgcn_exp2f(m - mn); m = mn;
    float rs = 0.f;
    #pragma unroll
    for (int kb2 = 0; kb2 < 2; ++kb2) for (int j = 0; j < 16; ++j) { float pv = __builtin_amdgcn_exp2f(s[kb2][j] * C - m); s[kb2][j] = pv; rs += pv; }
    rs += __shfl_xor(rs, 32);
    l = l * al + rs;
    #pragma unroll
    for (int db = 0; db < 8; ++db) for (int j = 0; j < 16; ++j) o[db][j] *= al;
    #pragma unroll
    for (int st = 0; st < 4; ++st) { u32x4 u; const int kb2 = st >> 1, s0 = (st & 1) * 8;
      for (int e = 0; e < 4; ++e) u[e] = cvtpk(s[kb2][s0 + 2 * e], s[kb2][s0 + 2 * e + 1]);
      bf16x8 pf = __builtin_bit_cast(bf16x8, u);
      #pragma unroll
      for (int db = 0; db < 8; ++db) { s16x4 t2[2];
        #pragma unroll
        for (int tt = 0; tt < 2; ++tt) { int row = 16 * st + 8 * tt + 4 * h + q4; int col = 32 * db + 16 * blk + 4 * p4; int ch = col >> 3;
          t2[tt] = trread(vs_ + row * 512 + ((ch ^ ((row & 3) << 2)) << 4) + (col & 7) * 2); }
        o[db] = MFMA(cat(t2[0], t2[1]), pf, o[db]); } }
  }
  const float il = 1.f / l;
  const int qrow = w * 32 + r;
  if (qrow < 112) {
    unsigned short* op = og + (size_t)(b * SQ + row0 + qrow) * HD + hd * DH;
    #pragma unroll
    for (int db = 0; db < 8; ++db)
      #pragma unroll
      for (int g = 0; g < 4; ++g) { u32x2 u = {cvtpk(o[db][4 * g] * il, o[db][4 * g + 1] * il), cvtpk(o[db][4 * g + 2] * il, o[db][4 * g + 3] * il)};
        *(u32x2*)(op + 32 * db + 8 * g + 4 * h) = u; }
  }
}

__global__ __launch_bounds__(256) void oproj_kernel(const unsigned short* __restrict__ og, const unsigned short* __restrict__ wo, unsigned short* __restrict__ out) {
  __shared__ __attribute__((aligned(16))) char lds[16384];
  const int tid = threadIdx.x, lane = tid & 63, w = tid >> 6, r = lane & 31, h = lane >> 5;
  const int blk = (lane >> 4) & 1, q4 = (lane & 15) >> 2, p4 = lane & 3;
  const int m0 = (blockIdx.x / 7) * 64, n0 = (blockIdx.x % 7) * 64;
  const int wm = w >> 1, wn = w & 1;
  char* os = lds;
  char* wsm = lds + 8192;
  f32x16 acc; for (int j = 0; j < 16; ++j) acc[j] = 0.f;
  for (int k0 = 0; k0 < HD; k0 += 64) {
    __syncthreads();
    for (int i = tid; i < 64 * 8; i += 256) { int row = i >> 3, ch = i & 7;
      u32x4 v = *(const u32x4*)(og + (size_t)(m0 + row) * HD + k0 + ch * 8);
      *(u32x4*)(os + row * 128 + ((ch ^ ((row >> 1) & 7)) << 4)) = v;
      u32x4 u = *(const u32x4*)(wo + (size_t)(k0 + row) * DM + n0 + ch * 8);
      *(u32x4*)(wsm + row * 128 + (ch << 4)) = u; }
    __syncthreads();
    #pragma unroll
    for (int ks = 0; ks < 4; ++ks) { const int xr = wm * 32 + r;
      bf16x8 bx = *(const bf16x8*)(os + xr * 128 + (((2 * ks + h) ^ ((xr >> 1) & 7)) << 4));
      s16x4 t[2];
      for (int tt = 0; tt < 2; ++tt) { int row = 16 * ks + 8 * h + 4 * tt + q4; int col = 32 * wn + 16 * blk + 4 * p4;
        t[tt] = trread(wsm + row * 128 + col * 2); }
      acc = MFMA(cat(t[0], t[1]), bx, acc); }
  }
  unsigned short* op = out + (size_t)(m0 + wm * 32 + r) * DM + n0 + wn * 32;
  for (int g = 0; g < 4; ++g) { u32x2 u = {cvtpk(acc[4 * g], acc[4 * g + 1]), cvtpk(acc[4 * g + 2], acc[4 * g + 3])};
    *(u32x2*)(op + 8 * g + 4 * h) = u; }
}


constexpr int SHARE = 1, SLOT = 32768, NS = 4, WAVES = 4;
__global__ __launch_bounds__(256) void probe(const char* __restrict__ k, const char* __restrict__ v) {
  __shared__ __attribute__((aligned(16))) char lds[SLOT * NS];
  const int tid = threadIdx.x, lane = tid & 63;
  const int w = __builtin_amdgcn_readfirstlane(tid >> 6);
  unsigned long long t0 = __builtin_amdgcn_s_memrealtime();
  const int bid = blockIdx.x;
  size_t base; int total;
  if (SHARE == 1) { base = (size_t)bid * 262144; total = 262144; }
  else { int b = bid & 7, rest = bid >> 3, hd = rest >> 1; base = (size_t)(b * 16 + hd) * 524288; total = 524288; }
  constexpr int PIECES = SLOT / 1024 / WAVES;
  const int nslots = 2 * total / SLOT;
  auto rsK = __builtin_amdgcn_make_buffer_rsrc((void*)(k + base), (short)0, total, 0x00020000);
  auto rsV = __builtin_amdgcn_make_buffer_rsrc((void*)(v + base), (short)0, total, 0x00020000);
  const unsigned voff = lane * 16;
  const unsigned ldsb = (unsigned)(size_t)lds;
  auto issue = [&](int s) {
    const unsigned so = (unsigned)(s >> 1) * SLOT + w * PIECES * 1024;
    const unsigned m0 = ldsb + (unsigned)(s % NS) * SLOT + w * PIECES * 1024;
    #pragma unroll
    for (int p = 0; p < PIECES; ++p) {
      if (s & 1) asm volatile("s_mov_b32 m0, %0\n\ts_nop 0\n\tbuffer_load_dwordx4 %1, %2, %3 offen  lds" :: "s"(m0 + p * 1024), "v"(voff), "s"(rsV), "s"(so + p * 1024) : "memory");
      else       asm volatile("s_mov_b32 m0, %0\n\ts_nop 0\n\tbuffer_load_dwordx4 %1, %2, %3 offen  lds" :: "s"(m0 + p * 1024), "v"(voff), "s"(rsK), "s"(so + p * 1024) : "memory");
    }
  };
  constexpr int DEPTH = NS - 1;
  for (int s = 0; s < DEPTH; ++s) issue(s);
  for (int s = 0; s < nslots; ++s) {
    if (s + DEPTH <= nslots) asm volatile("s_waitcnt vmcnt(%0)" :: "i"((DEPTH - 1) * PIECES > 63 ? 63 : (DEPTH - 1) * PIECES) : "memory");
    else asm volatile("s_waitcnt vmcnt(0)" ::: "memory");
    asm volatile("s_barrier" ::: "memory");
    if (s + DEPTH < nslots) issue(s + DEPTH);
  }
  unsigned long long t1 = __builtin_amdgcn_s_memrealtime();
  unsigned long long dt = t1 - t0;
  while (__builtin_amdgcn_s_memrealtime() - t0 < dt * 10) __builtin_amdgcn_s_sleep(8);
}
extern "C" void kernel_launch(void* const* d_in, const int* in_sizes, int n_in, void* d_out, int out_size, void* d_ws, size_t ws_size, hipStream_t stream) {
  const unsigned short* x = (const unsigned short*)d_in[0];
  const unsigned short* kc = (const unsigned short*)d_in[1];
  const unsigned short* vc = (const unsigned short*)d_in[2];
  const unsigned short* wq = (const unsigned short*)d_in[3];
  const unsigned short* wo = (const unsigned short*)d_in[4];
  unsigned short* og = (unsigned short*)d_ws;
probe<<<256, 256, 0, stream>>>((const char*)kc, (const char*)vc);
  attn_kernel<<<256, 256, 0, stream>>>(x, kc, vc, wq, og);
  oproj_kernel<<<28 * 7, 256, 0, stream>>>(og, wo, (unsigned short*)d_out);
}
```

```cpp
#include <hip/hip_runtime.h>
#include <hip/hip_bf16.h>
#include <stdint.h>

typedef short  bf16x8 __attribute__((ext_vector_type(8)));
typedef short  s16x4  __attribute__((ext_vector_type(4)));
typedef float  f32x16 __attribute__((ext_vector_type(16)));
typedef unsigned u32x4 __attribute__((ext_vector_type(4)));
typedef unsigned u32x2 __attribute__((ext_vector_type(2)));
typedef float  f32x2  __attribute__((ext_vector_type(2)));
typedef __bf16 bf2    __attribute__((ext_vector_type(2)));
#define LDS3 __attribute__((address_space(3)))
#define MFMA(a, b, c) __builtin_amdgcn_mfma_f32_32x32x16_bf16(__builtin_bit_cast(bf8_t, a), __builtin_bit_cast(bf8_t, b), c, 0, 0, 0)
typedef __bf16 bf8_t __attribute__((ext_vector_type(8)));

constexpr int B = 8, SQ = 224, DM = 448, H = 16, SK = 1024, DH = 256, HD = H * DH;

__device__ __forceinline__ unsigned cvtpk(float lo, float hi) {
  f32x2 v = {lo, hi}; bf2 b = __builtin_convertvector(v, bf2); return __builtin_bit_cast(unsigned, b);
}
__device__ __forceinline__ s16x4 trread(const char* p) {
  return __builtin_amdgcn_ds_read_tr16_b64_v4i16((LDS3 s16x4*)p);
}
__device__ __forceinline__ bf16x8 cat(s16x4 a, s16x4 b) { return __builtin_shufflevector(a, b, 0, 1, 2, 3, 4, 5, 6, 7); }

__global__ __launch_bounds__(256, 1) void attn_kernel(const unsigned short* __restrict__ x, const unsigned short* __restrict__ kc,
                                                      const unsigned short* __restrict__ vc, const unsigned short* __restrict__ wq,
                                                      unsigned short* __restrict__ og) {
  __shared__ __attribute__((aligned(16))) char lds[65536];
  const int tid = threadIdx.x, lane = tid & 63, w = tid >> 6, r = lane & 31, h = lane >> 5;
  const int blk = (lane >> 4) & 1, q4 = (lane & 15) >> 2, p4 = lane & 3;
  const int bid = blockIdx.x;
  const int b = bid & 7, rest = bid >> 3, hd = rest >> 1, qh = rest & 1;
  const int row0 = qh * 112;
  f32x16 acc[8];
  for (int i = 0; i < 8; ++i) for (int j = 0; j < 16; ++j) acc[i][j] = 0.f;
  char* xs = lds;
  char* wsm = lds + 16384;
  for (int kc0 = 0; kc0 < DM; kc0 += 64) {
    __syncthreads();
    for (int i = tid; i < 128 * 8; i += 256) { int row = i >> 3, ch = i & 7; int gr = row < 112 ? row : 111;
      u32x4 v = *(const u32x4*)(x + ((size_t)(b * SQ + row0 + gr) * DM + kc0 + ch * 8));
      *(u32x4*)(xs + row * 128 + ((ch ^ ((row >> 1) & 7)) << 4)) = v; }
    for (int i = tid; i < 64 * 32; i += 256) { int row = i >> 5, ch = i & 31;
      u32x4 v = *(const u32x4*)(wq + ((size_t)(kc0 + row) * HD + hd * DH + ch * 8));
      *(u32x4*)(wsm + row * 512 + ((ch ^ ((row & 3) << 2)) << 4)) = v; }
    __syncthreads();
    #pragma unroll
    for (int ks = 0; ks < 4; ++ks) {
      const int xr = w * 32 + r;
      bf16x8 bx = *(const bf16x8*)(xs + xr * 128 + (((2 * ks + h) ^ ((xr >> 1) & 7)) << 4));
      const int pp = ((p4 & 1) << 1) | (p4 >> 1);
      #pragma unroll
      for (int db = 0; db < 8; ++db) {
        s16x4 t[2];
        #pragma unroll
        for (int tt = 0; tt < 2; ++tt) { int row = 16 * ks + 8 * h + 4 * tt + q4; int col = 32 * db + 16 * blk + 4 * pp;
          int ch = col >> 3; t[tt] = trread(wsm + row * 512 + ((ch ^ ((row & 3) << 2)) << 4) + (col & 7) * 2); }
        acc[db] = MFMA(cat(t[0], t[1]), bx, acc[db]);
      }
    }
  }
  bf16x8 qf[16];
  #pragma unroll
  for (int db = 0; db < 8; ++db)
    #pragma unroll
    for (int s = 0; s < 2; ++s) { u32x4 u; for (int e = 0; e < 4; ++e) u[e] = cvtpk(acc[db][8 * s + 2 * e], acc[db][8 * s + 2 * e + 1]);
      qf[2 * db + s] = __builtin_bit_cast(bf16x8, u); }
  f32x16 o[8];
  for (int i = 0; i < 8; ++i) for (int j = 0; j < 16; ++j) o[i][j] = 0.f;
  float m = -1e30f, l = 0.f;
  const float C = 0.0625f * 1.4426950408889634f;
  const unsigned short* kb = kc + (size_t)(b * H + hd) * SK * DH;
  const unsigned short* vb = vc + (size_t)(b * H + hd) * SK * DH;
  char* ks_ = lds; char* vs_ = lds + 32768;
  for (int t = 0; t < SK / 64; ++t) {
    __syncthreads();
    for (int i = tid; i < 64 * 32; i += 256) { int row = i >> 5, ch = i & 31;
      u32x4 v = *(const u32x4*)(kb + (size_t)(t * 64 + row) * DH + ch * 8);
      *(u32x4*)(ks_ + row * 512 + ((ch ^ (row & 15)) << 4)) = v;
      u32x4 u = *(const u32x4*)(vb + (size_t)(t * 64 + row) * DH + ch * 8);
      *(u32x4*)(vs_ + row * 512 + ((ch ^ ((row & 3) << 2)) << 4)) = u; }
    __syncthreads();
    f32x16 s[2];
    #pragma unroll
    for (int kb2 = 0; kb2 < 2; ++kb2) { for (int j = 0; j < 16; ++j) s[kb2][j] = 0.f;
      #pragma unroll
      for (int st = 0; st < 16; ++st) { int row = kb2 * 32 + r;
        bf16x8 a = *(const bf16x8*)(ks_ + row * 512 + (((2 * st + h) ^ (row & 15)) << 4));
        s[kb2] = MFMA(a, qf[st], s[kb2]); } }
    float mx = s[0][0];
    for (int j = 0; j < 16; ++j) { mx = fmaxf(mx, s[0][j]); mx = fmaxf(mx, s[1][j]); }
    mx = fmaxf(mx, __shfl_xor(mx, 32));
    float mn = fmaxf(m, mx * C); float al = __builtin_amdgcn_exp2f(m - mn); m = mn;
    float rs = 0.f;
    #pragma unroll
    for (int kb2 = 0; kb2 < 2; ++kb2) for (int j = 0; j < 16; ++j) { float pv = __builtin_amdgcn_exp2f(s[kb2][j] * C - m); s[kb2][j] = pv; rs += pv; }
    rs += __shfl_xor(rs, 32);
    l = l * al + rs;
    #pragma unroll
    for (int db = 0; db < 8; ++db) for (int j = 0; j < 16; ++j) o[db][j] *= al;
    #pragma unroll
    for (int st = 0; st < 4; ++st) { u32x4 u; const int kb2 = st >> 1, s0 = (st & 1) * 8;
      for (int e = 0; e < 4; ++e) u[e] = cvtpk(s[kb2][s0 + 2 * e], s[kb2][s0 + 2 * e + 1]);
      bf16x8 pf = __builtin_bit_cast(bf16x8, u);
      #pragma unroll
      for (int db = 0; db < 8; ++db) { s16x4 t2[2];
        #pragma unroll
        for (int tt = 0; tt < 2; ++tt) { int row = 16 * st + 8 * tt + 4 * h + q4; int col = 32 * db + 16 * blk + 4 * p4; int ch = col >> 3;
          t2[tt] = trread(vs_ + row * 512 + ((ch ^ ((row & 3) << 2)) << 4) + (col & 7) * 2); }
        o[db] = MFMA(cat(t2[0], t2[1]), pf, o[db]); } }
  }
  const float il = 1.f / l;
  const int qrow = w * 32 + r;
  if (qrow < 112) {
    unsigned short* op = og + (size_t)(b * SQ + row0 + qrow) * HD + hd * DH;
    #pragma unroll
    for (int db = 0; db < 8; ++db)
      #pragma unroll
      for (int g = 0; g < 4; ++g) { u32x2 u = {cvtpk(o[db][4 * g] * il, o[db][4 * g + 1] * il), cvtpk(o[db][4 * g + 2] * il, o[db][4 * g + 3] * il)};
        *(u32x2*)(op + 32 * db + 8 * g + 4 * h) = u; }
  }
}

__global__ __launch_bounds__(256) void oproj_kernel(const unsigned short* __restrict__ og, const unsigned short* __restrict__ wo, unsigned short* __restrict__ out) {
  __shared__ __attribute__((aligned(16))) char lds[16384];
  const int tid = threadIdx.x, lane = tid & 63, w = tid >> 6, r = lane & 31, h = lane >> 5;
  const int blk = (lane >> 4) & 1, q4 = (lane & 15) >> 2, p4 = lane & 3;
  const int m0 = (blockIdx.x / 7) * 64, n0 = (blockIdx.x % 7) * 64;
  const int wm = w >> 1, wn = w & 1;
  char* os = lds;
  char* wsm = lds + 8192;
  f32x16 acc; for (int j = 0; j < 16; ++j) acc[j] = 0.f;
  for (int k0 = 0; k0 < HD; k0 += 64) {
    __syncthreads();
    for (int i = tid; i < 64 * 8; i += 256) { int row = i >> 3, ch = i & 7;
      u32x4 v = *(const u32x4*)(og + (size_t)(m0 + row) * HD + k0 + ch * 8);
      *(u32x4*)(os + row * 128 + ((ch ^ ((row >> 1) & 7)) << 4)) = v;
      u32x4 u = *(const u32x4*)(wo + (size_t)(k0 + row) * DM + n0 + ch * 8);
      *(u32x4*)(wsm + row * 128 + (ch << 4)) = u; }
    __syncthreads();
    #pragma unroll
    for (int ks = 0; ks < 4; ++ks) { const int xr = wm * 32 + r;
      bf16x8 bx = *(const bf16x8*)(os + xr * 128 + (((2 * ks + h) ^ ((xr >> 1) & 7)) << 4));
      s16x4 t[2];
      for (int tt = 0; tt < 2; ++tt) { int row = 16 * ks + 8 * h + 4 * tt + q4; int col = 32 * wn + 16 * blk + 4 * p4;
        t[tt] = trread(wsm + row * 128 + col * 2); }
      acc = MFMA(cat(t[0], t[1]), bx, acc); }
  }
  unsigned short* op = out + (size_t)(m0 + wm * 32 + r) * DM + n0 + wn * 32;
  for (int g = 0; g < 4; ++g) { u32x2 u = {cvtpk(acc[4 * g], acc[4 * g + 1]), cvtpk(acc[4 * g + 2], acc[4 * g + 3])};
    *(u32x2*)(op + 8 * g + 4 * h) = u; }
}


__global__ __launch_bounds__(256, 1) void probe() {
  asm volatile("s_mov_b32 s24, 8\n\ts_memrealtime s[40:41]\n\ts_waitcnt lgkmcnt(0)\n\tLOOP%=:\n\tv_mfma_f32_32x32x16_bf16 a[0:15], v[128:131], v[160:163], a[0:15]\n\tv_fma_f32 v200, v200, v230, v231\n\tv_fma_f32 v201, v201, v230, v231\n\tv_fma_f32 v202, v202, v230, v231\n\tv_fma_f32 v203, v203, v230, v231\n\tv_mfma_f32_32x32x16_bf16 a[16:31], v[132:135], v[160:163], a[16:31]\n\tv_fma_f32 v204, v204, v230, v231\n\tv_fma_f32 v205, v205, v230, v231\n\tv_fma_f32 v206, v206, v230, v231\n\tv_fma_f32 v207, v207, v230, v231\n\tv_mfma_f32_32x32x16_bf16 a[32:47], v[136:139], v[160:163], a[32:47]\n\tv_fma_f32 v208, v208, v230, v231\n\tv_fma_f32 v209, v209, v230, v231\n\tv_fma_f32 v210, v210, v230, v231\n\tv_fma_f32 v211, v211, v230, v231\n\tv_mfma_f32_32x32x16_bf16 a[48:63], v[140:143], v[160:163], a[48:63]\n\tv_fma_f32 v212, v212, v230, v231\n\tv_fma_f32 v213, v213, v230, v231\n\tv_fma_f32 v214, v214, v230, v231\n\tv_fma_f32 v215, v215, v230, v231\n\tv_mfma_f32_32x32x16_bf16 a[64:79], v[128:131], v[160:163], a[64:79]\n\tv_fma_f32 v200, v200, v230, v231\n\tv_fma_f32 v201, v201, v230, v231\n\tv_fma_f32 v202, v202, v230, v231\n\tv_fma_f32 v203, v203, v230, v231\n\tv_mfma_f32_32x32x16_bf16 a[80:95], v[132:135], v[160:163], a[80:95]\n\tv_fma_f32 v204, v204, v230, v231\n\tv_fma_f32 v205, v205, v230, v231\n\tv_fma_f32 v206, v206, v230, v231\n\tv_fma_f32 v207, v207, v230, v231\n\tv_mfma_f32_32x32x16_bf16 a[96:111], v[136:139], v[160:163], a[96:111]\n\tv_fma_f32 v208, v208, v230, v231\n\tv_fma_f32 v209, v209, v230, v231\n\tv_fma_f32 v210, v210, v230, v231\n\tv_fma_f32 v211, v211, v230, v231\n\tv_mfma_f32_32x32x16_bf16 a[112:127], v[140:143], v[160:163], a[112:127]\n\tv_fma_f32 v212, v212, v230, v231\n\tv_fma_f32 v213, v213, v230, v231\n\tv_fma_f32 v214, v214, v230, v231\n\tv_fma_f32 v215, v215, v230, v231\n\tv_mfma_f32_32x32x16_bf16 a[0:15], v[128:131], v[160:163], a[0:15]\n\tv_fma_f32 v200, v200, v230, v231\n\tv_fma_f32 v201, v201, v230, v231\n\tv_fma_f32 v202, v202, v230, v231\n\tv_fma_f32 v203, v203, v230, v231\n\tv_mfma_f32_32x32x16_bf16 a[16:31], v[132:135], v[160:163], a[16:31]\n\tv_fma_f32 v204, v204, v230, v231\n\tv_fma_f32 v205, v205, v230, v231\n\tv_fma_f32 v206, v206, v230, v231\n\tv_fma_f32 v207, v207, v230, v231\n\tv_mfma_f32_32x32x16_bf16 a[32:47], v[136:139], v[160:163], a[32:47]\n\tv_fma_f32 v208, v208, v230, v231\n\tv_fma_f32 v209, v209, v230, v231\n\tv_fma_f32 v210, v210, v230, v231\n\tv_fma_f32 v211, v211, v230, v231\n\tv_mfma_f32_32x32x16_bf16 a[48:63], v[140:143], v[160:163], a[48:63]\n\tv_fma_f32 v212, v212, v230, v231\n\tv_fma_f32 v213, v213, v230, v231\n\tv_fma_f32 v214, v214, v230, v231\n\tv_fma_f32 v215, v215, v230, v231\n\tv_mfma_f32_32x32x16_bf16 a[64:79], v[128:131], v[160:163], a[64:79]\n\tv_fma_f32 v200, v200, v230, v231\n\tv_fma_f32 v201, v201, v230, v231\n\tv_fma_f32 v202, v202, v230, v231\n\tv_fma_f32 v203, v203, v230, v231\n\tv_mfma_f32_32x32x16_bf16 a[80:95], v[132:135], v[160:163], a[80:95]\n\tv_fma_f32 v204, v204, v230, v231\n\tv_fma_f32 v205, v205, v230, v231\n\tv_fma_f32 v206, v206, v230, v231\n\tv_fma_f32 v207, v207, v230, v231\n\tv_mfma_f32_32x32x16_bf16 a[96:111], v[136:139], v[160:163], a[96:111]\n\tv_fma_f32 v208, v208, v230, v231\n\tv_fma_f32 v209, v209, v230, v231\n\tv_fma_f32 v210, v210, v230, v231\n\tv_fma_f32 v211, v211, v230, v231\n\tv_mfma_f32_32x32x16_bf16 a[112:127], v[140:143], v[160:163], a[112:127]\n\tv_fma_f32 v212, v212, v230, v231\n\tv_fma_f32 v213, v213, v230, v231\n\tv_fma_f32 v214, v214, v230, v231\n\tv_fma_f32 v215, v215, v230, v231\n\tv_mfma_f32_32x32x16_bf16 a[0:15], v[128:131], v[160:163], a[0:15]\n\tv_fma_f32 v200, v200, v230, v231\n\tv_fma_f32 v201, v201, v230, v231\n\tv_fma_f32 v202, v202, v230, v231\n\tv_fma_f32 v203, v203, v230, v231\n\tv_mfma_f32_32x32x16_bf16 a[16:31], v[132:135], v[160:163], a[16:31]\n\tv_fma_f32 v204, v204, v230, v231\n\tv_fma_f32 v205, v205, v230, v231\n\tv_fma_f32 v206, v206, v230, v231\n\tv_fma_f32 v207, v207, v230, v231\n\tv_mfma_f32_32x32x16_bf16 a[32:47], v[136:139], v[160:163], a[32:47]\n\tv_fma_f32 v208, v208, v230, v231\n\tv_fma_f32 v209, v209, v230, v231\n\tv_fma_f32 v210, v210, v230, v231\n\tv_fma_f32 v211, v211, v230, v231\n\tv_mfma_f32_32x32x16_bf16 a[48:63], v[140:143], v[160:163], a[48:63]\n\tv_fma_f32 v212, v212, v230, v231\n\tv_fma_f32 v213, v213, v230, v231\n\tv_fma_f32 v214, v214, v230, v231\n\tv_fma_f32 v215, v215, v230, v231\n\tv_mfma_f32_32x32x16_bf16 a[64:79], v[128:131], v[160:163], a[64:79]\n\tv_fma_f32 v200, v200, v230, v231\n\tv_fma_f32 v201, v201, v230, v231\n\tv_fma_f32 v202, v202, v230, v231\n\tv_fma_f32 v203, v203, v230, v231\n\tv_mfma_f32_32x32x16_bf16 a[80:95], v[132:135], v[160:163], a[80:95]\n\tv_fma_f32 v204, v204, v230, v231\n\tv_fma_f32 v205, v205, v230, v231\n\tv_fma_f32 v206, v206, v230, v231\n\tv_fma_f32 v207, v207, v230, v231\n\tv_mfma_f32_32x32x16_bf16 a[96:111], v[136:139], v[160:163], a[96:111]\n\tv_fma_f32 v208, v208, v230, v231\n\tv_fma_f32 v209, v209, v230, v231\n\tv_fma_f32 v210, v210, v230, v231\n\tv_fma_f32 v211, v211, v230, v231\n\tv_mfma_f32_32x32x16_bf16 a[112:127], v[140:143], v[160:163], a[112:127]\n\tv_fma_f32 v212, v212, v230, v231\n\tv_fma_f32 v213, v213, v230, v231\n\tv_fma_f32 v214, v214, v230, v231\n\tv_fma_f32 v215, v215, v230, v231\n\tv_mfma_f32_32x32x16_bf16 a[0:15], v[128:131], v[160:163], a[0:15]\n\tv_fma_f32 v200, v200, v230, v231\n\tv_fma_f32 v201, v201, v230, v231\n\tv_fma_f32 v202, v202, v230, v231\n\tv_fma_f32 v203, v203, v230, v231\n\tv_mfma_f32_32x32x16_bf16 a[16:31], v[132:135], v[160:163], a[16:31]\n\tv_fma_f32 v204, v204, v230, v231\n\tv_fma_f32 v205, v205, v230, v231\n\tv_fma_f32 v206, v206, v230, v231\n\tv_fma_f32 v207, v207, v230, v231\n\tv_mfma_f32_32x32x16_bf16 a[32:47], v[136:139], v[160:163], a[32:47]\n\tv_fma_f32 v208, v208, v230, v231\n\tv_fma_f32 v209, v209, v230, v231\n\tv_fma_f32 v210, v210, v230, v231\n\tv_fma_f32 v211, v211, v230, v231\n\tv_mfma_f32_32x32x16_bf16 a[48:63], v[140:143], v[160:163], a[48:63]\n\tv_fma_f32 v212, v212, v230, v231\n\tv_fma_f32 v213, v213, v230, v231\n\tv_fma_f32 v214, v214, v230, v231\n\tv_fma_f32 v215, v215, v230, v231\n\tv_mfma_f32_32x32x16_bf16 a[64:79], v[128:131], v[160:163], a[64:79]\n\tv_fma_f32 v200, v200, v230, v231\n\tv_fma_f32 v201, v201, v230, v231\n\tv_fma_f32 v202, v202, v230, v231\n\tv_fma_f32 v203, v203, v230, v231\n\tv_mfma_f32_32x32x16_bf16 a[80:95], v[132:135], v[160:163], a[80:95]\n\tv_fma_f32 v204, v204, v230, v231\n\tv_fma_f32 v205, v205, v230, v231\n\tv_fma_f32 v206, v206, v230, v231\n\tv_fma_f32 v207, v207, v230, v231\n\tv_mfma_f32_32x32x16_bf16 a[96:111], v[136:139], v[160:163], a[96:111]\n\tv_fma_f32 v208, v208, v230, v231\n\tv_fma_f32 v209, v209, v230, v231\n\tv_fma_f32 v210, v210, v230, v231\n\tv_fma_f32 v211, v211, v230, v231\n\tv_mfma_f32_32x32x16_bf16 a[112:127], v[140:143], v[160:163], a[112:127]\n\tv_fma_f32 v212, v212, v230, v231\n\tv_fma_f32 v213, v213, v230, v231\n\tv_fma_f32 v214, v214, v230, v231\n\tv_fma_f32 v215, v215, v230, v231\n\tv_mfma_f32_32x32x16_bf16 a[0:15], v[128:131], v[160:163], a[0:15]\n\tv_fma_f32 v200, v200, v230, v231\n\tv_fma_f32 v201, v201, v230, v231\n\tv_fma_f32 v202, v202, v230, v231\n\tv_fma_f32 v203, v203, v230, v231\n\tv_mfma_f32_32x32x16_bf16 a[16:31], v[132:135], v[160:163], a[16:31]\n\tv_fma_f32 v204, v204, v230, v231\n\tv_fma_f32 v205, v205, v230, v231\n\tv_fma_f32 v206, v206, v230, v231\n\tv_fma_f32 v207, v207, v230, v231\n\tv_mfma_f32_32x32x16_bf16 a[32:47], v[136:139], v[160:163], a[32:47]\n\tv_fma_f32 v208, v208, v230, v231\n\tv_fma_f32 v209, v209, v230, v231\n\tv_fma_f32 v210, v210, v230, v231\n\tv_fma_f32 v211, v211, v230, v231\n\tv_mfma_f32_32x32x16_bf16 a[48:63], v[140:143], v[160:163], a[48:63]\n\tv_fma_f32 v212, v212, v230, v231\n\tv_fma_f32 v213, v213, v230, v231\n\tv_fma_f32 v214, v214, v230, v231\n\tv_fma_f32 v215, v215, v230, v231\n\tv_mfma_f32_32x32x16_bf16 a[64:79], v[128:131], v[160:163], a[64:79]\n\tv_fma_f32 v200, v200, v230, v231\n\tv_fma_f32 v201, v201, v230, v231\n\tv_fma_f32 v202, v202, v230, v231\n\tv_fma_f32 v203, v203, v230, v231\n\tv_mfma_f32_32x32x16_bf16 a[80:95], v[132:135], v[160:163], a[80:95]\n\tv_fma_f32 v204, v204, v230, v231\n\tv_fma_f32 v205, v205, v230, v231\n\tv_fma_f32 v206, v206, v230, v231\n\tv_fma_f32 v207, v207, v230, v231\n\tv_mfma_f32_32x32x16_bf16 a[96:111], v[136:139], v[160:163], a[96:111]\n\tv_fma_f32 v208, v208, v230, v231\n\tv_fma_f32 v209, v209, v230, v231\n\tv_fma_f32 v210, v210, v230, v231\n\tv_fma_f32 v211, v211, v230, v231\n\tv_mfma_f32_32x32x16_bf16 a[112:127], v[140:143], v[160:163], a[112:127]\n\tv_fma_f32 v212, v212, v230, v231\n\tv_fma_f32 v213, v213, v230, v231\n\tv_fma_f32 v214, v214, v230, v231\n\tv_fma_f32 v215, v215, v230, v231\n\tv_mfma_f32_32x32x16_bf16 a[0:15], v[128:131], v[160:163], a[0:15]\n\tv_fma_f32 v200, v200, v230, v231\n\tv_fma_f32 v201, v201, v230, v231\n\tv_fma_f32 v202, v202, v230, v231\n\tv_fma_f32 v203, v203, v230, v231\n\tv_mfma_f32_32x32x16_bf16 a[16:31], v[132:135], v[160:163], a[16:31]\n\tv_fma_f32 v204, v204, v230, v231\n\tv_fma_f32 v205, v205, v230, v231\n\tv_fma_f32 v206, v206, v230, v231\n\tv_fma_f32 v207, v207, v230, v231\n\tv_mfma_f32_32x32x16_bf16 a[32:47], v[136:139], v[160:163], a[32:47]\n\tv_fma_f32 v208, v208, v230, v231\n\tv_fma_f32 v209, v209, v230, v231\n\tv_fma_f32 v210, v210, v230, v231\n\tv_fma_f32 v211, v211, v230, v231\n\tv_mfma_f32_32x32x16_bf16 a[48:63], v[140:143], v[160:163], a[48:63]\n\tv_fma_f32 v212, v212, v230, v231\n\tv_fma_f32 v213, v213, v230, v231\n\tv_fma_f32 v214, v214, v230, v231\n\tv_fma_f32 v215, v215, v230, v231\n\tv_mfma_f32_32x32x16_bf16 a[64:79], v[128:131], v[160:163], a[64:79]\n\tv_fma_f32 v200, v200, v230, v231\n\tv_fma_f32 v201, v201, v230, v231\n\tv_fma_f32 v202, v202, v230, v231\n\tv_fma_f32 v203, v203, v230, v231\n\tv_mfma_f32_32x32x16_bf16 a[80:95], v[132:135], v[160:163], a[80:95]\n\tv_fma_f32 v204, v204, v230, v231\n\tv_fma_f32 v205, v205, v230, v231\n\tv_fma_f32 v206, v206, v230, v231\n\tv_fma_f32 v207, v207, v230, v231\n\tv_mfma_f32_32x32x16_bf16 a[96:111], v[136:139], v[160:163], a[96:111]\n\tv_fma_f32 v208, v208, v230, v231\n\tv_fma_f32 v209, v209, v230, v231\n\tv_fma_f32 v210, v210, v230, v231\n\tv_fma_f32 v211, v211, v230, v231\n\tv_mfma_f32_32x32x16_bf16 a[112:127], v[140:143], v[160:163], a[112:127]\n\tv_fma_f32 v212, v212, v230, v231\n\tv_fma_f32 v213, v213, v230, v231\n\tv_fma_f32 v214, v214, v230, v231\n\tv_fma_f32 v215, v215, v230, v231\n\tv_mfma_f32_32x32x16_bf16 a[0:15], v[128:131], v[160:163], a[0:15]\n\tv_fma_f32 v200, v200, v230, v231\n\tv_fma_f32 v201, v201, v230, v231\n\tv_fma_f32 v202, v202, v230, v231\n\tv_fma_f32 v203, v203, v230, v231\n\tv_mfma_f32_32x32x16_bf16 a[16:31], v[132:135], v[160:163], a[16:31]\n\tv_fma_f32 v204, v204, v230, v231\n\tv_fma_f32 v205, v205, v230, v231\n\tv_fma_f32 v206, v206, v230, v231\n\tv_fma_f32 v207, v207, v230, v231\n\tv_mfma_f32_32x32x16_bf16 a[32:47], v[136:139], v[160:163], a[32:47]\n\tv_fma_f32 v208, v208, v230, v231\n\tv_fma_f32 v209, v209, v230, v231\n\tv_fma_f32 v210, v210, v230, v231\n\tv_fma_f32 v211, v211, v230, v231\n\tv_mfma_f32_32x32x16_bf16 a[48:63], v[140:143], v[160:163], a[48:63]\n\tv_fma_f32 v212, v212, v230, v231\n\tv_fma_f32 v213, v213, v230, v231\n\tv_fma_f32 v214, v214, v230, v231\n\tv_fma_f32 v215, v215, v230, v231\n\tv_mfma_f32_32x32x16_bf16 a[64:79], v[128:131], v[160:163], a[64:79]\n\tv_fma_f32 v200, v200, v230, v231\n\tv_fma_f32 v201, v201, v230, v231\n\tv_fma_f32 v202, v202, v230, v231\n\tv_fma_f32 v203, v203, v230, v231\n\tv_mfma_f32_32x32x16_bf16 a[80:95], v[132:135], v[160:163], a[80:95]\n\tv_fma_f32 v204, v204, v230, v231\n\tv_fma_f32 v205, v205, v230, v231\n\tv_fma_f32 v206, v206, v230, v231\n\tv_fma_f32 v207, v207, v230, v231\n\tv_mfma_f32_32x32x16_bf16 a[96:111], v[136:139], v[160:163], a[96:111]\n\tv_fma_f32 v208, v208, v230, v231\n\tv_fma_f32 v209, v209, v230, v231\n\tv_fma_f32 v210, v210, v230, v231\n\tv_fma_f32 v211, v211, v230, v231\n\tv_mfma_f32_32x32x16_bf16 a[112:127], v[140:143], v[160:163], a[112:127]\n\tv_fma_f32 v212, v212, v230, v231\n\tv_fma_f32 v213, v213, v230, v231\n\tv_fma_f32 v214, v214, v230, v231\n\tv_fma_f32 v215, v215, v230, v231\n\tv_mfma_f32_32x32x16_bf16 a[0:15], v[128:131], v[160:163], a[0:15]\n\tv_fma_f32 v200, v200, v230, v231\n\tv_fma_f32 v201, v201, v230, v231\n\tv_fma_f32 v202, v202, v230, v231\n\tv_fma_f32 v203, v203, v230, v231\n\tv_mfma_f32_32x32x16_bf16 a[16:31], v[132:135], v[160:163], a[16:31]\n\tv_fma_f32 v204, v204, v230, v231\n\tv_fma_f32 v205, v205, v230, v231\n\tv_fma_f32 v206, v206, v230, v231\n\tv_fma_f32 v207, v207, v230, v231\n\tv_mfma_f32_32x32x16_bf16 a[32:47], v[136:139], v[160:163], a[32:47]\n\tv_fma_f32 v208, v208, v230, v231\n\tv_fma_f32 v209, v209, v230, v231\n\tv_fma_f32 v210, v210, v230, v231\n\tv_fma_f32 v211, v211, v230, v231\n\tv_mfma_f32_32x32x16_bf16 a[48:63], v[140:143], v[160:163], a[48:63]\n\tv_fma_f32 v212, v212, v230, v231\n\tv_fma_f32 v213, v213, v230, v231\n\tv_fma_f32 v214, v214, v230, v231\n\tv_fma_f32 v215, v215, v230, v231\n\tv_mfma_f32_32x32x16_bf16 a[64:79], v[128:131], v[160:163], a[64:79]\n\tv_fma_f32 v200, v200, v230, v231\n\tv_fma_f32 v201, v201, v230, v231\n\tv_fma_f32 v202, v202, v230, v231\n\tv_fma_f32 v203, v203, v230, v231\n\tv_mfma_f32_32x32x16_bf16 a[80:95], v[132:135], v[160:163], a[80:95]\n\tv_fma_f32 v204, v204, v230, v231\n\tv_fma_f32 v205, v205, v230, v231\n\tv_fma_f32 v206, v206, v230, v231\n\tv_fma_f32 v207, v207, v230, v231\n\tv_mfma_f32_32x32x16_bf16 a[96:111], v[136:139], v[160:163], a[96:111]\n\tv_fma_f32 v208, v208, v230, v231\n\tv_fma_f32 v209, v209, v230, v231\n\tv_fma_f32 v210, v210, v230, v231\n\tv_fma_f32 v211, v211, v230, v231\n\tv_mfma_f32_32x32x16_bf16 a[112:127], v[140:143], v[160:163], a[112:127]\n\tv_fma_f32 v212, v212, v230, v231\n\tv_fma_f32 v213, v213, v230, v231\n\tv_fma_f32 v214, v214, v230, v231\n\tv_fma_f32 v215, v215, v230, v231\n\tv_mfma_f32_32x32x16_bf16 a[0:15], v[128:131], v[160:163], a[0:15]\n\tv_fma_f32 v200, v200, v230, v231\n\tv_fma_f32 v201, v201, v230, v231\n\tv_fma_f32 v202, v202, v230, v231\n\tv_fma_f32 v203, v203, v230, v231\n\tv_mfma_f32_32x32x16_bf16 a[16:31], v[132:135], v[160:163], a[16:31]\n\tv_fma_f32 v204, v204, v230, v231\n\tv_fma_f32 v205, v205, v230, v231\n\tv_fma_f32 v206, v206, v230, v231\n\tv_fma_f32 v207, v207, v230, v231\n\tv_mfma_f32_32x32x16_bf16 a[32:47], v[136:139], v[160:163], a[32:47]\n\tv_fma_f32 v208, v208, v230, v231\n\tv_fma_f32 v209, v209, v230, v231\n\tv_fma_f32 v210, v210, v230, v231\n\tv_fma_f32 v211, v211, v230, v231\n\tv_mfma_f32_32x32x16_bf16 a[48:63], v[140:143], v[160:163], a[48:63]\n\tv_fma_f32 v212, v212, v230, v231\n\tv_fma_f32 v213, v213, v230, v231\n\tv_fma_f32 v214, v214, v230, v231\n\tv_fma_f32 v215, v215, v230, v231\n\tv_mfma_f32_32x32x16_bf16 a[64:79], v[128:131], v[160:163], a[64:79]\n\tv_fma_f32 v200, v200, v230, v231\n\tv_fma_f32 v201, v201, v230, v231\n\tv_fma_f32 v202, v202, v230, v231\n\tv_fma_f32 v203, v203, v230, v231\n\tv_mfma_f32_32x32x16_bf16 a[80:95], v[132:135], v[160:163], a[80:95]\n\tv_fma_f32 v204, v204, v230, v231\n\tv_fma_f32 v205, v205, v230, v231\n\tv_fma_f32 v206, v206, v230, v231\n\tv_fma_f32 v207, v207, v230, v231\n\tv_mfma_f32_32x32x16_bf16 a[96:111], v[136:139], v[160:163], a[96:111]\n\tv_fma_f32 v208, v208, v230, v231\n\tv_fma_f32 v209, v209, v230, v231\n\tv_fma_f32 v210, v210, v230, v231\n\tv_fma_f32 v211, v211, v230, v231\n\tv_mfma_f32_32x32x16_bf16 a[112:127], v[140:143], v[160:163], a[112:127]\n\tv_fma_f32 v212, v212, v230, v231\n\tv_fma_f32 v213, v213, v230, v231\n\tv_fma_f32 v214, v214, v230, v231\n\tv_fma_f32 v215, v215, v230, v231\n\tv_mfma_f32_32x32x16_bf16 a[0:15], v[128:131], v[160:163], a[0:15]\n\tv_fma_f32 v200, v200, v230, v231\n\tv_fma_f32 v201, v201, v230, v231\n\tv_fma_f32 v202, v202, v230, v231\n\tv_fma_f32 v203, v203, v230, v231\n\tv_mfma_f32_32x32x16_bf16 a[16:31], v[132:135], v[160:163], a[16:31]\n\tv_fma_f32 v204, v204, v230, v231\n\tv_fma_f32 v205, v205, v230, v231\n\tv_fma_f32 v206, v206, v230, v231\n\tv_fma_f32 v207, v207, v230, v231\n\tv_mfma_f32_32x32x16_bf16 a[32:47], v[136:139], v[160:163], a[32:47]\n\tv_fma_f32 v208, v208, v230, v231\n\tv_fma_f32 v209, v209, v230, v231\n\tv_fma_f32 v210, v210, v230, v231\n\tv_fma_f32 v211, v211, v230, v231\n\tv_mfma_f32_32x32x16_bf16 a[48:63], v[140:143], v[160:163], a[48:63]\n\tv_fma_f32 v212, v212, v230, v231\n\tv_fma_f32 v213, v213, v230, v231\n\tv_fma_f32 v214, v214, v230, v231\n\tv_fma_f32 v215, v215, v230, v231\n\tv_mfma_f32_32x32x16_bf16 a[64:79], v[128:131], v[160:163], a[64:79]\n\tv_fma_f32 v200, v200, v230, v231\n\tv_fma_f32 v201, v201, v230, v231\n\tv_fma_f32 v202, v202, v230, v231\n\tv_fma_f32 v203, v203, v230, v231\n\tv_mfma_f32_32x32x16_bf16 a[80:95], v[132:135], v[160:163], a[80:95]\n\tv_fma_f32 v204, v204, v230, v231\n\tv_fma_f32 v205, v205, v230, v231\n\tv_fma_f32 v206, v206, v230, v231\n\tv_fma_f32 v207, v207, v230, v231\n\tv_mfma_f32_32x32x16_bf16 a[96:111], v[136:139], v[160:163], a[96:111]\n\tv_fma_f32 v208, v208, v230, v231\n\tv_fma_f32 v209, v209, v230, v231\n\tv_fma_f32 v210, v210, v230, v231\n\tv_fma_f32 v211, v211, v230, v231\n\tv_mfma_f32_32x32x16_bf16 a[112:127], v[140:143], v[160:163], a[112:127]\n\tv_fma_f32 v212, v212, v230, v231\n\tv_fma_f32 v213, v213, v230, v231\n\tv_fma_f32 v214, v214, v230, v231\n\tv_fma_f32 v215, v215, v230, v231\n\tv_mfma_f32_32x32x16_bf16 a[0:15], v[128:131], v[160:163], a[0:15]\n\tv_fma_f32 v200, v200, v230, v231\n\tv_fma_f32 v201, v201, v230, v231\n\tv_fma_f32 v202, v202, v230, v231\n\tv_fma_f32 v203, v203, v230, v231\n\tv_mfma_f32_32x32x16_bf16 a[16:31], v[132:135], v[160:163], a[16:31]\n\tv_fma_f32 v204, v204, v230, v231\n\tv_fma_f32 v205, v205, v230, v231\n\tv_fma_f32 v206, v206, v230, v231\n\tv_fma_f32 v207, v207, v230, v231\n\tv_mfma_f32_32x32x16_bf16 a[32:47], v[136:139], v[160:163], a[32:47]\n\tv_fma_f32 v208, v208, v230, v231\n\tv_fma_f32 v209, v209, v230, v231\n\tv_fma_f32 v210, v210, v230, v231\n\tv_fma_f32 v211, v211, v230, v231\n\tv_mfma_f32_32x32x16_bf16 a[48:63], v[140:143], v[160:163], a[48:63]\n\tv_fma_f32 v212, v212, v230, v231\n\tv_fma_f32 v213, v213, v230, v231\n\tv_fma_f32 v214, v214, v230, v231\n\tv_fma_f32 v215, v215, v230, v231\n\tv_mfma_f32_32x32x16_bf16 a[64:79], v[128:131], v[160:163], a[64:79]\n\tv_fma_f32 v200, v200, v230, v231\n\tv_fma_f32 v201, v201, v230, v231\n\tv_fma_f32 v202, v202, v230, v231\n\tv_fma_f32 v203, v203, v230, v231\n\tv_mfma_f32_32x32x16_bf16 a[80:95], v[132:135], v[160:163], a[80:95]\n\tv_fma_f32 v204, v204, v230, v231\n\tv_fma_f32 v205, v205, v230, v231\n\tv_fma_f32 v206, v206, v230, v231\n\tv_fma_f32 v207, v207, v230, v231\n\tv_mfma_f32_32x32x16_bf16 a[96:111], v[136:139], v[160:163], a[96:111]\n\tv_fma_f32 v208, v208, v230, v231\n\tv_fma_f32 v209, v209, v230, v231\n\tv_fma_f32 v210, v210, v230, v231\n\tv_fma_f32 v211, v211, v230, v231\n\tv_mfma_f32_32x32x16_bf16 a[112:127], v[140:143], v[160:163], a[112:127]\n\tv_fma_f32 v212, v212, v230, v231\n\tv_fma_f32 v213, v213, v230, v231\n\tv_fma_f32 v214, v214, v230, v231\n\tv_fma_f32 v215, v215, v230, v231\n\tv_mfma_f32_32x32x16_bf16 a[0:15], v[128:131], v[160:163], a[0:15]\n\tv_fma_f32 v200, v200, v230, v231\n\tv_fma_f32 v201, v201, v230, v231\n\tv_fma_f32 v202, v202, v230, v231\n\tv_fma_f32 v203, v203, v230, v231\n\tv_mfma_f32_32x32x16_bf16 a[16:31], v[132:135], v[160:163], a[16:31]\n\tv_fma_f32 v204, v204, v230, v231\n\tv_fma_f32 v205, v205, v230, v231\n\tv_fma_f32 v206, v206, v230, v231\n\tv_fma_f32 v207, v207, v230, v231\n\tv_mfma_f32_32x32x16_bf16 a[32:47], v[136:139], v[160:163], a[32:47]\n\tv_fma_f32 v208, v208, v230, v231\n\tv_fma_f32 v209, v209, v230, v231\n\tv_fma_f32 v210, v210, v230, v231\n\tv_fma_f32 v211, v211, v230, v231\n\tv_mfma_f32_32x32x16_bf16 a[48:63], v[140:143], v[160:163], a[48:63]\n\tv_fma_f32 v212, v212, v230, v231\n\tv_fma_f32 v213, v213, v230, v231\n\tv_fma_f32 v214, v214, v230, v231\n\tv_fma_f32 v215, v215, v230, v231\n\tv_mfma_f32_32x32x16_bf16 a[64:79], v[128:131], v[160:163], a[64:79]\n\tv_fma_f32 v200, v200, v230, v231\n\tv_fma_f32 v201, v201, v230, v231\n\tv_fma_f32 v202, v202, v230, v231\n\tv_fma_f32 v203, v203, v230, v231\n\tv_mfma_f32_32x32x16_bf16 a[80:95], v[132:135], v[160:163], a[80:95]\n\tv_fma_f32 v204, v204, v230, v231\n\tv_fma_f32 v205, v205, v230, v231\n\tv_fma_f32 v206, v206, v230, v231\n\tv_fma_f32 v207, v207, v230, v231\n\tv_mfma_f32_32x32x16_bf16 a[96:111], v[136:139], v[160:163], a[96:111]\n\tv_fma_f32 v208, v208, v230, v231\n\tv_fma_f32 v209, v209, v230, v231\n\tv_fma_f32 v210, v210, v230, v231\n\tv_fma_f32 v211, v211, v230, v231\n\tv_mfma_f32_32x32x16_bf16 a[112:127], v[140:143], v[160:163], a[112:127]\n\tv_fma_f32 v212, v212, v230, v231\n\tv_fma_f32 v213, v213, v230, v231\n\tv_fma_f32 v214, v214, v230, v231\n\tv_fma_f32 v215, v215, v230, v231\n\tv_mfma_f32_32x32x16_bf16 a[0:15], v[128:131], v[160:163], a[0:15]\n\tv_fma_f32 v200, v200, v230, v231\n\tv_fma_f32 v201, v201, v230, v231\n\tv_fma_f32 v202, v202, v230, v231\n\tv_fma_f32 v203, v203, v230, v231\n\tv_mfma_f32_32x32x16_bf16 a[16:31], v[132:135], v[160:163], a[16:31]\n\tv_fma_f32 v204, v204, v230, v231\n\tv_fma_f32 v205, v205, v230, v231\n\tv_fma_f32 v206, v206, v230, v231\n\tv_fma_f32 v207, v207, v230, v231\n\tv_mfma_f32_32x32x16_bf16 a[32:47], v[136:139], v[160:163], a[32:47]\n\tv_fma_f32 v208, v208, v230, v231\n\tv_fma_f32 v209, v209, v230, v231\n\tv_fma_f32 v210, v210, v230, v231\n\tv_fma_f32 v211, v211, v230, v231\n\tv_mfma_f32_32x32x16_bf16 a[48:63], v[140:143], v[160:163], a[48:63]\n\tv_fma_f32 v212, v212, v230, v231\n\tv_fma_f32 v213, v213, v230, v231\n\tv_fma_f32 v214, v214, v230, v231\n\tv_fma_f32 v215, v215, v230, v231\n\tv_mfma_f32_32x32x16_bf16 a[64:79], v[128:131], v[160:163], a[64:79]\n\tv_fma_f32 v200, v200, v230, v231\n\tv_fma_f32 v201, v201, v230, v231\n\tv_fma_f32 v202, v202, v230, v231\n\tv_fma_f32 v203, v203, v230, v231\n\tv_mfma_f32_32x32x16_bf16 a[80:95], v[132:135], v[160:163], a[80:95]\n\tv_fma_f32 v204, v204, v230, v231\n\tv_fma_f32 v205, v205, v230, v231\n\tv_fma_f32 v206, v206, v230, v231\n\tv_fma_f32 v207, v207, v230, v231\n\tv_mfma_f32_32x32x16_bf16 a[96:111], v[136:139], v[160:163], a[96:111]\n\tv_fma_f32 v208, v208, v230, v231\n\tv_fma_f32 v209, v209, v230, v231\n\tv_fma_f32 v210, v210, v230, v231\n\tv_fma_f32 v211, v211, v230, v231\n\tv_mfma_f32_32x32x16_bf16 a[112:127], v[140:143], v[160:163], a[112:127]\n\tv_fma_f32 v212, v212, v230, v231\n\tv_fma_f32 v213, v213, v230, v231\n\tv_fma_f32 v214, v214, v230, v231\n\tv_fma_f32 v215, v215, v230, v231\n\tv_mfma_f32_32x32x16_bf16 a[0:15], v[128:131], v[160:163], a[0:15]\n\tv_fma_f32 v200, v200, v230, v231\n\tv_fma_f32 v201, v201, v230, v231\n\tv_fma_f32 v202, v202, v230, v231\n\tv_fma_f32 v203, v203, v230, v231\n\tv_mfma_f32_32x32x16_bf16 a[16:31], v[132:135], v[160:163], a[16:31]\n\tv_fma_f32 v204, v204, v230, v231\n\tv_fma_f32 v205, v205, v230, v231\n\tv_fma_f32 v206, v206, v230, v231\n\tv_fma_f32 v207, v207, v230, v231\n\tv_mfma_f32_32x32x16_bf16 a[32:47], v[136:139], v[160:163], a[32:47]\n\tv_fma_f32 v208, v208, v230, v231\n\tv_fma_f32 v209, v209, v230, v231\n\tv_fma_f32 v210, v210, v230, v231\n\tv_fma_f32 v211, v211, v230, v231\n\tv_mfma_f32_32x32x16_bf16 a[48:63], v[140:143], v[160:163], a[48:63]\n\tv_fma_f32 v212, v212, v230, v231\n\tv_fma_f32 v213, v213, v230, v231\n\tv_fma_f32 v214, v214, v230, v231\n\tv_fma_f32 v215, v215, v230, v231\n\tv_mfma_f32_32x32x16_bf16 a[64:79], v[128:131], v[160:163], a[64:79]\n\tv_fma_f32 v200, v200, v230, v231\n\tv_fma_f32 v201, v201, v230, v231\n\tv_fma_f32 v202, v202, v230, v231\n\tv_fma_f32 v203, v203, v230, v231\n\tv_mfma_f32_32x32x16_bf16 a[80:95], v[132:135], v[160:163], a[80:95]\n\tv_fma_f32 v204, v204, v230, v231\n\tv_fma_f32 v205, v205, v230, v231\n\tv_fma_f32 v206, v206, v230, v231\n\tv_fma_f32 v207, v207, v230, v231\n\tv_mfma_f32_32x32x16_bf16 a[96:111], v[136:139], v[160:163], a[96:111]\n\tv_fma_f32 v208, v208, v230, v231\n\tv_fma_f32 v209, v209, v230, v231\n\tv_fma_f32 v210, v210, v230, v231\n\tv_fma_f32 v211, v211, v230, v231\n\tv_mfma_f32_32x32x16_bf16 a[112:127], v[140:143], v[160:163], a[112:127]\n\tv_fma_f32 v212, v212, v230, v231\n\tv_fma_f32 v213, v213, v230, v231\n\tv_fma_f32 v214, v214, v230, v231\n\tv_fma_f32 v215, v215, v230, v231\n\tv_mfma_f32_32x32x16_bf16 a[0:15], v[128:131], v[160:163], a[0:15]\n\tv_fma_f32 v200, v200, v230, v231\n\tv_fma_f32 v201, v201, v230, v231\n\tv_fma_f32 v202, v202, v230, v231\n\tv_fma_f32 v203, v203, v230, v231\n\tv_mfma_f32_32x32x16_bf16 a[16:31], v[132:135], v[160:163], a[16:31]\n\tv_fma_f32 v204, v204, v230, v231\n\tv_fma_f32 v205, v205, v230, v231\n\tv_fma_f32 v206, v206, v230, v231\n\tv_fma_f32 v207, v207, v230, v231\n\tv_mfma_f32_32x32x16_bf16 a[32:47], v[136:139], v[160:163], a[32:47]\n\tv_fma_f32 v208, v208, v230, v231\n\tv_fma_f32 v209, v209, v230, v231\n\tv_fma_f32 v210, v210, v230, v231\n\tv_fma_f32 v211, v211, v230, v231\n\tv_mfma_f32_32x32x16_bf16 a[48:63], v[140:143], v[160:163], a[48:63]\n\tv_fma_f32 v212, v212, v230, v231\n\tv_fma_f32 v213, v213, v230, v231\n\tv_fma_f32 v214, v214, v230, v231\n\tv_fma_f32 v215, v215, v230, v231\n\tv_mfma_f32_32x32x16_bf16 a[64:79], v[128:131], v[160:163], a[64:79]\n\tv_fma_f32 v200, v200, v230, v231\n\tv_fma_f32 v201, v201, v230, v231\n\tv_fma_f32 v202, v202, v230, v231\n\tv_fma_f32 v203, v203, v230, v231\n\tv_mfma_f32_32x32x16_bf16 a[80:95], v[132:135], v[160:163], a[80:95]\n\tv_fma_f32 v204, v204, v230, v231\n\tv_fma_f32 v205, v205, v230, v231\n\tv_fma_f32 v206, v206, v230, v231\n\tv_fma_f32 v207, v207, v230, v231\n\tv_mfma_f32_32x32x16_bf16 a[96:111], v[136:139], v[160:163], a[96:111]\n\tv_fma_f32 v208, v208, v230, v231\n\tv_fma_f32 v209, v209, v230, v231\n\tv_fma_f32 v210, v210, v230, v231\n\tv_fma_f32 v211, v211, v230, v231\n\tv_mfma_f32_32x32x16_bf16 a[112:127], v[140:143], v[160:163], a[112:127]\n\tv_fma_f32 v212, v212, v230, v231\n\tv_fma_f32 v213, v213, v230, v231\n\tv_fma_f32 v214, v214, v230, v231\n\tv_fma_f32 v215, v215, v230, v231\n\tv_mfma_f32_32x32x16_bf16 a[0:15], v[128:131], v[160:163], a[0:15]\n\tv_fma_f32 v200, v200, v230, v231\n\tv_fma_f32 v201, v201, v230, v231\n\tv_fma_f32 v202, v202, v230, v231\n\tv_fma_f32 v203, v203, v230, v231\n\tv_mfma_f32_32x32x16_bf16 a[16:31], v[132:135], v[160:163], a[16:31]\n\tv_fma_f32 v204, v204, v230, v231\n\tv_fma_f32 v205, v205, v230, v231\n\tv_fma_f32 v206, v206, v230, v231\n\tv_fma_f32 v207, v207, v230, v231\n\tv_mfma_f32_32x32x16_bf16 a[32:47], v[136:139], v[160:163], a[32:47]\n\tv_fma_f32 v208, v208, v230, v231\n\tv_fma_f32 v209, v209, v230, v231\n\tv_fma_f32 v210, v210, v230, v231\n\tv_fma_f32 v211, v211, v230, v231\n\tv_mfma_f32_32x32x16_bf16 a[48:63], v[140:143], v[160:163], a[48:63]\n\tv_fma_f32 v212, v212, v230, v231\n\tv_fma_f32 v213, v213, v230, v231\n\tv_fma_f32 v214, v214, v230, v231\n\tv_fma_f32 v215, v215, v230, v231\n\tv_mfma_f32_32x32x16_bf16 a[64:79], v[128:131], v[160:163], a[64:79]\n\tv_fma_f32 v200, v200, v230, v231\n\tv_fma_f32 v201, v201, v230, v231\n\tv_fma_f32 v202, v202, v230, v231\n\tv_fma_f32 v203, v203, v230, v231\n\tv_mfma_f32_32x32x16_bf16 a[80:95], v[132:135], v[160:163], a[80:95]\n\tv_fma_f32 v204, v204, v230, v231\n\tv_fma_f32 v205, v205, v230, v231\n\tv_fma_f32 v206, v206, v230, v231\n\tv_fma_f32 v207, v207, v230, v231\n\tv_mfma_f32_32x32x16_bf16 a[96:111], v[136:139], v[160:163], a[96:111]\n\tv_fma_f32 v208, v208, v230, v231\n\tv_fma_f32 v209, v209, v230, v231\n\tv_fma_f32 v210, v210, v230, v231\n\tv_fma_f32 v211, v211, v230, v231\n\tv_mfma_f32_32x32x16_bf16 a[112:127], v[140:143], v[160:163], a[112:127]\n\tv_fma_f32 v212, v212, v230, v231\n\tv_fma_f32 v213, v213, v230, v231\n\tv_fma_f32 v214, v214, v230, v231\n\tv_fma_f32 v215, v215, v230, v231\n\ts_sub_u32 s24, s24, 1\n\ts_cmp_lg_u32 s24, 0\n\ts_cbranch_scc1 LOOP%=\n\ts_memrealtime s[42:43]\n\ts_waitcnt lgkmcnt(0)\n\ts_sub_u32 s44, s42, s40\n\ts_mul_i32 s44, s44, 10\n\tSPIN%=:\n\ts_sleep 4\n\ts_memrealtime s[42:43]\n\ts_waitcnt lgkmcnt(0)\n\ts_sub_u32 s45, s42, s40\n\ts_cmp_lt_u32 s45, s44\n\ts_cbranch_scc1 SPIN%=" ::: "v0", "v1", "v2", "v3", "v4", "v5", "v6", "v7", "v8", "v9", "v10", "v11", "v12", "v13", "v14", "v15", "v16", "v17", "v18", "v19", "v20", "v21", "v22", "v23", "v24", "v25", "v26", "v27", "v28", "v29", "v30", "v31", "v32", "v33", "v34", "v35", "v36", "v37", "v38", "v39", "v40", "v41", "v42", "v43", "v44", "v45", "v46", "v47", "v48", "v49", "v50", "v51", "v52", "v53", "v54", "v55", "v56", "v57", "v58", "v59", "v60", "v61", "v62", "v63", "v64", "v65", "v66", "v67", "v68", "v69", "v70", "v71", "v72", "v73", "v74", "v75", "v76", "v77", "v78", "v79", "v80", "v81", "v82", "v83", "v84", "v85", "v86", "v87", "v88", "v89", "v90", "v91", "v92", "v93", "v94", "v95", "v96", "v97", "v98", "v99", "v100", "v101", "v102", "v103", "v104", "v105", "v106", "v107", "v108", "v109", "v110", "v111", "v112", "v113", "v114", "v115", "v116", "v117", "v118", "v119", "v120", "v121", "v122", "v123", "v124", "v125", "v126", "v127", "v128", "v129", "v130", "v131", "v132", "v133", "v134", "v135", "v136", "v137", "v138", "v139", "v140", "v141", "v142", "v143", "v144", "v145", "v146", "v147", "v148", "v149", "v150", "v151", "v152", "v153", "v154", "v155", "v156", "v157", "v158", "v159", "v160", "v161", "v162", "v163", "v164", "v165", "v166", "v167", "v168", "v169", "v170", "v171", "v172", "v173", "v174", "v175", "v176", "v177", "v178", "v179", "v180", "v181", "v182", "v183", "v184", "v185", "v186", "v187", "v188", "v189", "v190", "v191", "v192", "v193", "v194", "v195", "v196", "v197", "v198", "v199", "v200", "v201", "v202", "v203", "v204", "v205", "v206", "v207", "v208", "v209", "v210", "v211", "v212", "v213", "v214", "v215", "v216", "v217", "v218", "v219", "v220", "v221", "v222", "v223", "v224", "v225", "v226", "v227", "v228", "v229", "v230", "v231", "v232", "v233", "v234", "v235", "v236", "v237", "v238", "v239", "v240", "v241", "v242", "v243", "v244", "v245", "v246", "v247", "v248", "v249", "v250", "v251", "a0", "a1", "a2", "a3", "a4", "a5", "a6", "a7", "a8", "a9", "a10", "a11", "a12", "a13", "a14", "a15", "a16", "a17", "a18", "a19", "a20", "a21", "a22", "a23", "a24", "a25", "a26", "a27", "a28", "a29", "a30", "a31", "a32", "a33", "a34", "a35", "a36", "a37", "a38", "a39", "a40", "a41", "a42", "a43", "a44", "a45", "a46", "a47", "a48", "a49", "a50", "a51", "a52", "a53", "a54", "a55", "a56", "a57", "a58", "a59", "a60", "a61", "a62", "a63", "a64", "a65", "a66", "a67", "a68", "a69", "a70", "a71", "a72", "a73", "a74", "a75", "a76", "a77", "a78", "a79", "a80", "a81", "a82", "a83", "a84", "a85", "a86", "a87", "a88", "a89", "a90", "a91", "a92", "a93", "a94", "a95", "a96", "a97", "a98", "a99", "a100", "a101", "a102", "a103", "a104", "a105", "a106", "a107", "a108", "a109", "a110", "a111", "a112", "a113", "a114", "a115", "a116", "a117", "a118", "a119", "a120", "a121", "a122", "a123", "a124", "a125", "a126", "a127", "a128", "a129", "a130", "a131", "a132", "a133", "a134", "a135", "a136", "a137", "a138", "a139", "a140", "a141", "a142", "a143", "a144", "a145", "a146", "a147", "a148", "a149", "a150", "a151", "a152", "a153", "a154", "a155", "a156", "a157", "a158", "a159", "a160", "a161", "a162", "a163", "a164", "a165", "a166", "a167", "a168", "a169", "a170", "a171", "a172", "a173", "a174", "a175", "a176", "a177", "a178", "a179", "a180", "a181", "a182", "a183", "a184", "a185", "a186", "a187", "a188", "a189", "a190", "a191", "a192", "a193", "a194", "a195", "a196", "a197", "a198", "a199", "a200", "a201", "a202", "a203", "a204", "a205", "a206", "a207", "a208", "a209", "a210", "a211", "a212", "a213", "a214", "a215", "a216", "a217", "a218", "a219", "a220", "a221", "a222", "a223", "a224", "a225", "a226", "a227", "a228", "a229", "a230", "a231", "a232", "a233", "a234", "a235", "a236", "a237", "a238", "a239", "a240", "a241", "a242", "a243", "a244", "a245", "a246", "a247", "a248", "a249", "a250", "a251", "a252", "a253", "a254", "a255", "s20", "s21", "s22", "s23", "s24", "s25", "s26", "s27", "s28", "s29", "s30", "s31", "s36", "s37", "s38", "s39", "s40", "s41", "s42", "s43", "s44", "s45", "s46", "s47", "s48", "s49", "s50", "s51", "s52", "s53", "s54", "s55", "s56", "s57", "s58", "s59", "s60", "s61", "s62", "s63", "s64", "s65", "s66", "s67", "s68", "s69", "s70", "s71", "s72", "s73", "s74", "s75", "s76", "s77", "s78", "s79", "s80", "s81", "s82", "s83", "s84", "s85", "s86", "s87", "s88", "s89", "s90", "s91", "s92", "s93", "s94", "s95", "s96", "s97", "s98", "s99", "vcc", "scc", "memory");
}
extern "C" void kernel_launch(void* const* d_in, const int* in_sizes, int n_in, void* d_out, int out_size, void* d_ws, size_t ws_size, hipStream_t stream) {
  const unsigned short* x = (const unsigned short*)d_in[0];
  const unsigned short* kc = (const unsigned short*)d_in[1];
  const unsigned short* vc = (const unsigned short*)d_in[2];
  const unsigned short* wq = (const unsigned short*)d_in[3];
  const unsigned short* wo = (const unsigned short*)d_in[4];
  unsigned short* og = (unsigned short*)d_ws;
probe<<<256, 256, 0, stream>>>();
  attn_kernel<<<256, 256, 0, stream>>>(x, kc, vc, wq, og);
  oproj_kernel<<<28 * 7, 256, 0, stream>>>(og, wo, (unsigned short*)d_out);
}
```

```cpp
#include <hip/hip_runtime.h>
#include <hip/hip_bf16.h>
#include <stdint.h>

__global__ __launch_bounds__(256, 1) void attn_fused(const char* __restrict__ x, const char* __restrict__ kc, const char* __restrict__ vc,
                                                     const char* __restrict__ wq, const char* __restrict__ wo, char* __restrict__ ws, char* __restrict__ out) {
  asm volatile(
      "s_load_dwordx8 s[8:15], s[0:1], 0x0\n\t"
      "s_load_dwordx4 s[16:19], s[0:1], 0x20\n\t"
      "s_load_dwordx2 s[20:21], s[0:1], 0x30\n\t"
      "v_readfirstlane_b32 s4, v0\n\t"
      "s_nop 0\n\t"
      "s_lshr_b32 s4, s4, 6\n\t"
      "s_lshr_b32 s94, s2, 3\n\t"
      "s_and_b32 s98, s94, 1\n\t"
      "s_bfe_u32 s96, s94, 0x20001\n\t"
      "s_lshr_b32 s97, s94, 3\n\t"
      "s_bfe_u32 s94, s2, 0x20001\n\t"
      "s_lshl_b32 s94, s94, 2\n\t"
      "s_add_u32 s7, s94, s96\n\t"
      "s_and_b32 s94, s2, 1\n\t"
      "s_lshl_b32 s94, s94, 2\n\t"
      "s_add_u32 s3, s94, s97\n\t"
      "s_lshl_b32 s94, s97, 1\n\t"
      "s_add_u32 s6, s94, s98\n\t"
      "s_mul_i32 s94, s96, 3\n\t"
      "s_add_u32 s5, s6, s94\n\t"
      "s_sub_u32 s5, s5, s98\n\t"
      "s_cmp_ge_u32 s6, 7\n\t"
      "s_cselect_b32 s94, 7, 0\n\t"
      "s_sub_u32 s6, s6, s94\n\t"
      "s_cmp_ge_u32 s5, 7\n\t"
      "s_cselect_b32 s94, 7, 0\n\t"
      "s_sub_u32 s5, s5, s94\n\t"
      "s_cmp_ge_u32 s5, 7\n\t"
      "s_cselect_b32 s94, 7, 0\n\t"
      "s_sub_u32 s5, s5, s94\n\t"
      "s_lshl_b32 s94, s3, 1\n\t"
      "s_add_u32 s30, s94, s98\n\t"
      "s_mul_i32 s94, s3, 224\n\t"
      "s_mul_i32 s95, s98, 112\n\t"
      "s_add_u32 s94, s94, s95\n\t"
      "s_mul_i32 s31, s94, 896\n\t"
      "s_sub_u32 s24, 1605632, s31\n\t"
      "s_lshl_b32 s94, s3, 4\n\t"
      "s_add_u32 s94, s94, s7\n\t"
      "s_lshl_b32 s3, s94, 19\n\t"
      "s_mov_b32 s88, s4\n\t"
      "s_lshl_b32 s89, s88, 13\n\t"
      "s_lshl_b32 s90, s88, 12\n\t"
      "s_mov_b32 s92, s5\n\t"
      "s_mov_b32 s93, s5\n\t"
      "s_mul_i32 s94, s88, 28672\n\t"
      "s_add_u32 s84, s94, 0\n\t"
      "s_add_u32 s85, s94, 6144\n\t"
      "s_add_u32 s86, s94, 12288\n\t"
      "s_add_u32 s87, s94, 18432\n\t"
      "s_cmp_eq_u32 s88, 3\n\t"
      "s_cselect_b32 s86, 0x40000000, s86\n\t"
      "s_cselect_b32 s87, 0x40000000, s87\n\t"
      "s_lshl_b32 s94, s88, 7\n\t"
      "s_add_u32 s76, s94, 0\n\t"
      "s_add_u32 s77, s94, 130048\n\t"
      "s_add_u32 s78, s94, 30720\n\t"
      "s_add_u32 s79, s94, 160768\n\t"
      "s_add_u32 s80, s94, 262144\n\t"
      "s_add_u32 s81, s94, 392192\n\t"
      "s_add_u32 s82, s94, 292864\n\t"
      "s_add_u32 s83, s94, 422912\n\t"
      "v_mbcnt_lo_u32_b32 v0, -1, 0\n\t"
      "v_mbcnt_hi_u32_b32 v0, -1, v0\n\t"
      "v_and_b32 v1, 15, v0\n\t"
      "v_lshrrev_b32 v2, 4, v0\n\t"
      "v_lshrrev_b32 v248, 3, v0\n\t"
      "v_and_b32 v249, 7, v0\n\t"
      "v_xor_b32 v3, v249, v2\n\t"
      "v_lshlrev_b32 v3, 4, v3\n\t"
      "v_mul_u32_u24 v15, 896, v248\n\t"
      "v_add_u32 v46, v15, v3\n\t"
      "v_xor_b32 v47, 64, v46\n\t"
      "v_lshlrev_b32 v3, 1, v2\n\t"
      "v_xor_b32 v3, v3, v249\n\t"
      "v_lshlrev_b32 v250, 4, v3\n\t"
      "v_lshrrev_b32 v15, 5, v0\n\t"
      "v_lshlrev_b32 v15, 3, v15\n\t"
      "v_and_b32 v3, 3, v248\n\t"
      "v_add_u32 v15, v15, v3\n\t"
      "v_lshlrev_b32 v15, 13, v15\n\t"
      "v_add_u32 v48, v15, v250\n\t"
      "s_waitcnt lgkmcnt(0)\n\t"
      "s_waitcnt lgkmcnt(0)\n\t"
      "s_add_u32 s8, s8, s31\n\t"
      "s_addc_u32 s9, s9, 0\n\t"
      "s_lshl_b32 s94, s7, 9\n\t"
      "s_add_u32 s14, s14, s94\n\t"
      "s_addc_u32 s15, s15, 0\n\t"
      "s_mov_b32 s44, s8\n\t"
      "s_and_b32 s45, s9, 0xffff\n\t"
      "s_mov_b32 s46, s24\n\t"
      "s_mov_b32 s47, 0x20000\n\t"
      "s_mov_b32 s48, s14\n\t"
      "s_and_b32 s49, s15, 0xffff\n\t"
      "s_mov_b32 s50, 524288\n\t"
      "s_mov_b32 s51, 0x20000\n\t"
      "s_mov_b32 s97, s14\n\t"
      "s_and_b32 s98, s15, 0xffff\n\t"
      "s_lshl_b32 s95, s92, 7\n\t"
      "s_add_u32 s96, s84, s95\n\t"
      "s_add_u32 m0, s90, 131072\n\t"
      "s_nop 0\n\t"
      "buffer_load_dwordx4 v46, s[44:47], s96 offen offset:0 lds\n\t"
      "s_add_u32 s96, s85, s95\n\t"
      "buffer_load_dwordx4 v47, s[44:47], s96 offen offset:1024 lds\n\t"
      "s_add_u32 s96, s86, s95\n\t"
      "buffer_load_dwordx4 v46, s[44:47], s96 offen offset:2048 lds\n\t"
      "s_add_u32 s96, s87, s95\n\t"
      "buffer_load_dwordx4 v47, s[44:47], s96 offen offset:3072 lds\n\t"
      "s_add_u32 s92, s92, 1\n\t"
      "s_cmp_eq_u32 s92, 7\n\t"
      "s_cselect_b32 s92, 0, s92\n\t"
      "s_mul_i32 s94, s93, 524288\n\t"
      "s_add_u32 s48, s97, s94\n\t"
      "s_addc_u32 s49, s98, 0\n\t"
      "s_add_u32 m0, s89, 65536\n\t"
      "s_nop 0\n\t"
      "buffer_load_dwordx4 v48, s[48:51], s76 offen offset:0 lds\n\t"
      "buffer_load_dwordx4 v48, s[48:51], s77 offen offset:1024 lds\n\t"
      "buffer_load_dwordx4 v48, s[48:51], s78 offen offset:2048 lds\n\t"
      "buffer_load_dwordx4 v48, s[48:51], s79 offen offset:3072 lds\n\t"
      "s_lshl_b32 s94, s88, 7\n\t"
      "s_add_u32 s68, s94, 0\n\t"
      "s_add_u32 s69, s94, 3072\n\t"
      "s_add_u32 s70, s94, 6144\n\t"
      "s_add_u32 s71, s94, 9216\n\t"
      "s_add_u32 s72, s94, 16384\n\t"
      "s_add_u32 s73, s94, 19456\n\t"
      "s_add_u32 s74, s94, 22528\n\t"
      "s_add_u32 s75, s94, 25600\n\t"
      "s_add_u32 s60, s89, 2048\n\t"
      "s_add_u32 s61, s89, 5120\n\t"
      "s_add_u32 s62, s89, 128\n\t"
      "s_add_u32 s63, s89, 3200\n\t"
      "s_add_u32 s64, s89, 2304\n\t"
      "s_add_u32 s65, s89, 5376\n\t"
      "s_add_u32 s66, s89, 384\n\t"
      "s_add_u32 s67, s89, 3456\n\t"
      "s_mov_b32 s100, s6\n\t"
      "s_mov_b32 s101, s6\n\t"
      "s_mov_b32 s99, 0x3db8aa3b\n\t"
      "v_xor_b32 v3, v249, v2\n\t"
      "v_lshlrev_b32 v3, 4, v3\n\t"
      "v_lshlrev_b32 v15, 9, v248\n\t"
      "v_add_u32 v12, v15, v3\n\t"
      "v_xor_b32 v13, 64, v12\n\t"
      "v_add_u32 v14, v15, v250\n\t"
      "v_lshrrev_b32 v3, 3, v1\n\t"
      "v_lshlrev_b32 v4, 10, v3\n\t"
      "v_and_b32 v3, 7, v1\n\t"
      "v_lshlrev_b32 v3, 7, v3\n\t"
      "v_add_u32 v4, v4, v3\n\t"
      "v_lshrrev_b32 v3, 1, v1\n\t"
      "v_xor_b32 v3, v3, v2\n\t"
      "v_lshlrev_b32 v3, 4, v3\n\t"
      "v_add_u32 v4, v4, v3\n\t"
      "v_xor_b32 v5, 64, v4\n\t"
      "v_add_u32 v44, 98304, v4\n\t"
      "v_add_u32 v45, 98304, v5\n\t"
      "v_add_u32 v4, s89, v4\n\t"
      "v_add_u32 v5, s89, v5\n\t"
      "v_lshrrev_b32 v15, 2, v1\n\t"
      "v_and_b32 v251, 3, v0\n\t"
      "v_lshlrev_b32 v3, 2, v2\n\t"
      "v_add_u32 v3, v3, v15\n\t"
      "v_lshlrev_b32 v253, 7, v3\n\t"
      "v_lshrrev_b32 v252, 1, v3\n\t"
      "v_and_b32 v252, 3, v252\n\t"
      "v_lshlrev_b32 v252, 1, v252\n\t"
      "s_add_u32 s94, s89, 65536\n\t"
      "v_add_u32 v3, 0, v251\n\t"
      "v_xor_b32 v3, v3, v252\n\t"
      "v_lshlrev_b32 v3, 4, v3\n\t"
      "v_add_u32 v3, v253, v3\n\t"
      "v_add_u32 v52, s94, v3\n\t"
      "v_add_u32 v3, 4, v251\n\t"
      "v_xor_b32 v3, v3, v252\n\t"
      "v_lshlrev_b32 v3, 4, v3\n\t"
      "v_add_u32 v3, v253, v3\n\t"
      "v_add_u32 v53, s94, v3\n\t"
      "v_and_b32 v3, 1, v251\n\t"
      "v_lshlrev_b32 v3, 3, v3\n\t"
      "v_add_u32 v253, v253, v3\n\t"
      "v_lshrrev_b32 v251, 1, v251\n\t"
      "v_add_u32 v3, 0, v251\n\t"
      "v_xor_b32 v3, v3, v252\n\t"
      "v_lshlrev_b32 v3, 4, v3\n\t"
      "v_add_u32 v3, v253, v3\n\t"
      "v_add_u32 v6, s94, v3\n\t"
      "v_add_u32 v3, 2, v251\n\t"
      "v_xor_b32 v3, v3, v252\n\t"
      "v_lshlrev_b32 v3, 4, v3\n\t"
      "v_add_u32 v3, v253, v3\n\t"
      "v_add_u32 v7, s94, v3\n\t"
      "v_add_u32 v3, 4, v251\n\t"
      "v_xor_b32 v3, v3, v252\n\t"
      "v_lshlrev_b32 v3, 4, v3\n\t"
      "v_add_u32 v3, v253, v3\n\t"
      "v_add_u32 v8, s94, v3\n\t"
      "v_add_u32 v3, 6, v251\n\t"
      "v_xor_b32 v3, v3, v252\n\t"
      "v_lshlrev_b32 v3, 4, v3\n\t"
      "v_add_u32 v3, v253, v3\n\t"
      "v_add_u32 v9, s94, v3\n\t"
      "v_lshlrev_b32 v3, 4, v0\n\t"
      "v_add_u32 v10, 131072, v3\n\t"
      "v_add_u32 v50, 65536, v3\n\t"
      "s_lshr_b32 s94, s88, 1\n\t"
      "s_lshl_b32 s94, s94, 10\n\t"
      "s_and_b32 s95, s88, 1\n\t"
      "s_lshl_b32 s95, s95, 3\n\t"
      "s_add_u32 s94, s94, s95\n\t"
      "v_add_u32 v11, s94, v10\n\t"
      "s_lshl_b32 s94, s88, 11\n\t"
      "v_add_u32 v49, s94, v50\n\t"
      "v_add_u32 v51, 65536, v49\n\t"
      "v_lshlrev_b32 v80, 4, v1\n\t"
      "v_add_u32 v80, 145408, v80\n\t"
      "s_lshl_b32 s94, s88, 2\n\t"
      "v_add_u32 v79, s94, v80\n\t"
      "s_add_u32 m0, s89, 69632\n\t"
      "s_nop 0\n\t"
      "buffer_load_dwordx4 v48, s[48:51], s80 offen offset:0 lds\n\t"
      "buffer_load_dwordx4 v48, s[48:51], s81 offen offset:1024 lds\n\t"
      "buffer_load_dwordx4 v48, s[48:51], s82 offen offset:2048 lds\n\t"
      "buffer_load_dwordx4 v48, s[48:51], s83 offen offset:3072 lds\n\t"
      "s_add_u32 s93, s93, 1\n\t"
      "s_cmp_eq_u32 s93, 7\n\t"
      "s_cselect_b32 s93, 0, s93\n\t"
      "s_lshl_b32 s95, s92, 7\n\t"
      "s_add_u32 s96, s84, s95\n\t"
      "s_add_u32 m0, s90, 147456\n\t"
      "s_nop 0\n\t"
      "buffer_load_dwordx4 v46, s[44:47], s96 offen offset:0 lds\n\t"
      "s_add_u32 s96, s85, s95\n\t"
      "buffer_load_dwordx4 v47, s[44:47], s96 offen offset:1024 lds\n\t"
      "s_add_u32 s96, s86, s95\n\t"
      "buffer_load_dwordx4 v46, s[44:47], s96 offen offset:2048 lds\n\t"
      "s_add_u32 s96, s87, s95\n\t"
      "buffer_load_dwordx4 v47, s[44:47], s96 offen offset:3072 lds\n\t"
      "s_add_u32 s92, s92, 1\n\t"
      "s_cmp_eq_u32 s92, 7\n\t"
      "s_cselect_b32 s92, 0, s92\n\t"
      "s_getpc_b64 s[52:53]\n\t"
      "s_and_b32 s53, s53, 0xffff\n\t"
      "s_mov_b32 s54, 40960\n\t"
      "s_mov_b32 s55, 0x20000\n\t"
      "s_lshr_b32 s94, s2, 3\n\t"
      "s_lshl_b32 s94, s94, 11\n\t"
      "v_and_b32 v15, 15, v0\n\t"
      "v_lshlrev_b32 v15, 7, v15\n\t"
      "buffer_load_dword v16, v15, s[52:55], s94 offen offset:0\n\t"
      "s_add_u32 s10, s10, s3\n\t"
      "s_addc_u32 s11, s11, 0\n\t"
      "s_sub_u32 s10, s10, 2048\n\t"
      "s_subb_u32 s11, s11, 0\n\t"
      "s_add_u32 s12, s12, s3\n\t"
      "s_addc_u32 s13, s13, 0\n\t"
      "s_mul_i32 s94, s7, 229376\n\t"
      "s_add_u32 s16, s16, s94\n\t"
      "s_addc_u32 s17, s17, 0\n\t"
      "s_add_u32 s20, s20, s31\n\t"
      "s_addc_u32 s21, s21, 0\n\t"
      "s_lshl_b32 s94, s30, 12\n\t"
      "s_add_u32 s94, s94, 33554432\n\t"
      "s_add_u32 s22, s18, s94\n\t"
      "s_addc_u32 s23, s19, 0\n\t"
      "s_mul_i32 s94, s30, 1835008\n\t"
      "s_add_u32 s18, s18, s94\n\t"
      "s_addc_u32 s19, s19, 0\n\t"
      "s_mov_b32 s36, s10\n\t"
      "s_and_b32 s37, s11, 0xffff\n\t"
      "s_mov_b32 s38, 34816\n\t"
      "s_mov_b32 s39, 0x20000\n\t"
      "s_mov_b32 s40, s12\n\t"
      "s_and_b32 s41, s13, 0xffff\n\t"
      "s_mov_b32 s42, 32768\n\t"
      "s_mov_b32 s43, 0x20000\n\t"
      "s_mul_i32 s94, s7, 114688\n\t"
      "s_add_u32 s56, s18, s94\n\t"
      "s_addc_u32 s57, s19, 0\n\t"
      "s_and_b32 s57, s57, 0xffff\n\t"
      "s_mov_b32 s58, 114688\n\t"
      "s_mov_b32 s59, 0x20000\n\t"
      "s_mov_b32 s52, s16\n\t"
      "s_and_b32 s53, s17, 0xffff\n\t"
      "s_mov_b32 s54, 228608\n\t"
      "s_mov_b32 s55, 0x20000\n\t"
      "s_waitcnt vmcnt(5)\n\t"
      "s_barrier\n\t"
      "ds_read_b64_tr_b16 a[0:1], v52 offset:0\n\t"
      "ds_read_b64_tr_b16 a[2:3], v52 offset:2048\n\t"
      "ds_read_b64_tr_b16 a[4:5], v52 offset:4096\n\t"
      "ds_read_b64_tr_b16 a[6:7], v52 offset:6144\n\t"
      "ds_read_b64_tr_b16 a[8:9], v52 offset:8\n\t"
      "ds_read_b64_tr_b16 a[10:11], v52 offset:2056\n\t"
      "ds_read_b64_tr_b16 a[12:13], v52 offset:4104\n\t"
      "ds_read_b64_tr_b16 a[14:15], v52 offset:6152\n\t"
      "ds_read_b64_tr_b16 a[16:17], v53 offset:0\n\t"
      "ds_read_b64_tr_b16 a[18:19], v53 offset:2048\n\t"
      "ds_read_b64_tr_b16 a[20:21], v53 offset:4096\n\t"
      "ds_read_b64_tr_b16 a[22:23], v53 offset:6144\n\t"
      "ds_read_b64_tr_b16 a[24:25], v53 offset:8\n\t"
      "ds_read_b64_tr_b16 a[26:27], v53 offset:2056\n\t"
      "ds_read_b64_tr_b16 a[28:29], v53 offset:4104\n\t"
      "ds_read_b64_tr_b16 a[30:31], v53 offset:6152\n\t"
      "ds_read_b128 v[88:91], v44 offset:32768\n\t"
      "ds_read_b128 v[92:95], v45 offset:32768\n\t"
      "ds_read_b128 v[96:99], v44 offset:34816\n\t"
      "ds_read_b128 v[100:103], v45 offset:34816\n\t"
      "s_waitcnt lgkmcnt(3)\n\t"
      "v_mfma_f32_16x16x32_bf16 v[136:139], a[0:3], v[88:91], 0\n\t"
      "s_mul_i32 s94, s93, 524288\n\t"
      "s_add_u32 s48, s97, s94\n\t"
      "s_addc_u32 s49, s98, 0\n\t"
      "s_add_u32 m0, s89, 98304\n\t"
      "s_nop 0\n\t"
      "buffer_load_dwordx4 v48, s[48:51], s76 offen offset:0 lds\n\t"
      "s_waitcnt lgkmcnt(2)\n\t"
      "v_mfma_f32_16x16x32_bf16 v[136:139], a[4:7], v[92:95], v[136:139]\n\t"
      "v_mfma_f32_16x16x32_bf16 v[140:143], a[8:11], v[88:91], 0\n\t"
      "buffer_load_dwordx4 v48, s[48:51], s77 offen offset:1024 lds\n\t"
      "v_mfma_f32_16x16x32_bf16 v[140:143], a[12:15], v[92:95], v[140:143]\n\t"
      "v_mfma_f32_16x16x32_bf16 v[144:147], a[16:19], v[88:91], 0\n\t"
      "v_mfma_f32_16x16x32_bf16 v[144:147], a[20:23], v[92:95], v[144:147]\n\t"
      "buffer_load_dwordx4 v48, s[48:51], s78 offen offset:2048 lds\n\t"
      "v_mfma_f32_16x16x32_bf16 v[148:151], a[24:27], v[88:91], 0\n\t"
      "v_mfma_f32_16x16x32_bf16 v[148:151], a[28:31], v[92:95], v[148:151]\n\t"
      "ds_read_b128 v[88:91], v44 offset:36864\n\t"
      "ds_read_b128 v[92:95], v45 offset:36864\n\t"
      "s_waitcnt lgkmcnt(3)\n\t"
      "v_mfma_f32_16x16x32_bf16 v[152:155], a[0:3], v[96:99], 0\n\t"
      "buffer_load_dwordx4 v48, s[48:51], s79 offen offset:3072 lds\n\t"
      "s_waitcnt lgkmcnt(2)\n\t"
      "v_mfma_f32_16x16x32_bf16 v[152:155], a[4:7], v[100:103], v[152:155]\n\t"
      "v_mfma_f32_16x16x32_bf16 v[156:159], a[8:11], v[96:99], 0\n\t"
      "s_add_u32 m0, s89, 102400\n\t"
      "s_nop 0\n\t"
      "buffer_load_dwordx4 v48, s[48:51], s80 offen offset:0 lds\n\t"
      "v_mfma_f32_16x16x32_bf16 v[156:159], a[12:15], v[100:103], v[156:159]\n\t"
      "v_mfma_f32_16x16x32_bf16 v[160:163], a[16:19], v[96:99], 0\n\t"
      "buffer_load_dwordx4 v48, s[48:51], s81 offen offset:1024 lds\n\t"
      "v_mfma_f32_16x16x32_bf16 v[160:163], a[20:23], v[100:103], v[160:163]\n\t"
      "v_mfma_f32_16x16x32_bf16 v[164:167], a[24:27], v[96:99], 0\n\t"
      "v_mfma_f32_16x16x32_bf16 v[164:167], a[28:31], v[100:103], v[164:167]\n\t"
      "ds_read_b128 v[96:99], v44 offset:38912\n\t"
      "ds_read_b128 v[100:103], v45 offset:38912\n\t"
      "s_waitcnt lgkmcnt(3)\n\t"
      "v_mfma_f32_16x16x32_bf16 v[168:171], a[0:3], v[88:91], 0\n\t"
      "buffer_load_dwordx4 v48, s[48:51], s82 offen offset:2048 lds\n\t"
      "s_waitcnt lgkmcnt(2)\n\t"
      "v_mfma_f32_16x16x32_bf16 v[168:171], a[4:7], v[92:95], v[168:171]\n\t"
      "buffer_load_dwordx4 v48, s[48:51], s83 offen offset:3072 lds\n\t"
      "s_add_u32 s93, s93, 1\n\t"
      "s_cmp_eq_u32 s93, 7\n\t"
      "s_cselect_b32 s93, 0, s93\n\t"
      "v_mfma_f32_16x16x32_bf16 v[172:175], a[8:11], v[88:91], 0\n\t"
      "v_mfma_f32_16x16x32_bf16 v[172:175], a[12:15], v[92:95], v[172:175]\n\t"
      "v_mfma_f32_16x16x32_bf16 v[176:179], a[16:19], v[88:91], 0\n\t"
      "s_mul_i32 s94, s93, 524288\n\t"
      "s_add_u32 s48, s97, s94\n\t"
      "s_addc_u32 s49, s98, 0\n\t"
      "s_add_u32 m0, s89, 65536\n\t"
      "s_nop 0\n\t"
      "buffer_load_dwordx4 v48, s[48:51], s76 offen offset:0 lds\n\t"
      "v_mfma_f32_16x16x32_bf16 v[176:179], a[20:23], v[92:95], v[176:179]\n\t"
      "v_mfma_f32_16x16x32_bf16 v[180:183], a[24:27], v[88:91], 0\n\t"
      "v_mfma_f32_16x16x32_bf16 v[180:183], a[28:31], v[92:95], v[180:183]\n\t"
      "ds_read_b128 v[88:91], v44 offset:40960\n\t"
      "ds_read_b128 v[92:95], v45 offset:40960\n\t"
      "s_waitcnt lgkmcnt(3)\n\t"
      "v_mfma_f32_16x16x32_bf16 v[184:187], a[0:3], v[96:99], 0\n\t"
      "buffer_load_dwordx4 v48, s[48:51], s77 offen offset:1024 lds\n\t"
      "s_waitcnt lgkmcnt(2)\n\t"
      "v_mfma_f32_16x16x32_bf16 v[184:187], a[4:7], v[100:103], v[184:187]\n\t"
      "buffer_load_dwordx4 v48, s[48:51], s78 offen offset:2048 lds\n\t"
      "v_mfma_f32_16x16x32_bf16 v[188:191], a[8:11], v[96:99], 0\n\t"
      "v_mfma_f32_16x16x32_bf16 v[188:191], a[12:15], v[100:103], v[188:191]\n\t"
      "buffer_load_dwordx4 v48, s[48:51], s79 offen offset:3072 lds\n\t"
      "v_mfma_f32_16x16x32_bf16 v[192:195], a[16:19], v[96:99], 0\n\t"
      "v_mfma_f32_16x16x32_bf16 v[192:195], a[20:23], v[100:103], v[192:195]\n\t"
      "v_mfma_f32_16x16x32_bf16 v[196:199], a[24:27], v[96:99], 0\n\t"
      "s_add_u32 m0, s89, 69632\n\t"
      "s_nop 0\n\t"
      "buffer_load_dwordx4 v48, s[48:51], s80 offen offset:0 lds\n\t"
      "v_mfma_f32_16x16x32_bf16 v[196:199], a[28:31], v[100:103], v[196:199]\n\t"
      "ds_read_b128 v[96:99], v44 offset:43008\n\t"
      "ds_read_b128 v[100:103], v45 offset:43008\n\t"
      "s_waitcnt lgkmcnt(3)\n\t"
      "v_mfma_f32_16x16x32_bf16 v[200:203], a[0:3], v[88:91], 0\n\t"
      "s_waitcnt vmcnt(5)\n\t"
      "ds_read_b64_tr_b16 a[32:33], v52 offset:32768\n\t"
      "s_waitcnt lgkmcnt(3)\n\t"
      "v_mfma_f32_16x16x32_bf16 v[200:203], a[4:7], v[92:95], v[200:203]\n\t"
      "ds_read_b64_tr_b16 a[34:35], v52 offset:34816\n\t"
      "ds_read_b64_tr_b16 a[36:37], v52 offset:36864\n\t"
      "v_mfma_f32_16x16x32_bf16 v[204:207], a[8:11], v[88:91], 0\n\t"
      "ds_read_b64_tr_b16 a[38:39], v52 offset:38912\n\t"
      "ds_read_b64_tr_b16 a[40:41], v52 offset:32776\n\t"
      "v_mfma_f32_16x16x32_bf16 v[204:207], a[12:15], v[92:95], v[204:207]\n\t"
      "ds_read_b64_tr_b16 a[42:43], v52 offset:34824\n\t"
      "ds_read_b64_tr_b16 a[44:45], v52 offset:36872\n\t"
      "v_mfma_f32_16x16x32_bf16 v[208:211], a[16:19], v[88:91], 0\n\t"
      "ds_read_b64_tr_b16 a[46:47], v52 offset:38920\n\t"
      "ds_read_b64_tr_b16 a[48:49], v53 offset:32768\n\t"
      "v_mfma_f32_16x16x32_bf16 v[208:211], a[20:23], v[92:95], v[208:211]\n\t"
      "ds_read_b64_tr_b16 a[50:51], v53 offset:34816\n\t"
      "ds_read_b64_tr_b16 a[52:53], v53 offset:36864\n\t"
      "v_mfma_f32_16x16x32_bf16 v[212:215], a[24:27], v[88:91], 0\n\t"
      "ds_read_b64_tr_b16 a[54:55], v53 offset:38912\n\t"
      "ds_read_b64_tr_b16 a[56:57], v53 offset:32776\n\t"
      "v_mfma_f32_16x16x32_bf16 v[212:215], a[28:31], v[92:95], v[212:215]\n\t"
      "ds_read_b128 v[88:91], v44 offset:45056\n\t"
      "ds_read_b128 v[92:95], v45 offset:45056\n\t"
      "s_waitcnt lgkmcnt(15)\n\t"
      "v_mfma_f32_16x16x32_bf16 v[216:219], a[0:3], v[96:99], 0\n\t"
      "ds_read_b64_tr_b16 a[58:59], v53 offset:34824\n\t"
      "ds_read_b64_tr_b16 a[60:61], v53 offset:36872\n\t"
      "v_mfma_f32_16x16x32_bf16 v[216:219], a[4:7], v[100:103], v[216:219]\n\t"
      "ds_read_b64_tr_b16 a[62:63], v53 offset:38920\n\t"
      "buffer_load_dwordx4 v48, s[48:51], s81 offen offset:1024 lds\n\t"
      "v_mfma_f32_16x16x32_bf16 v[220:223], a[8:11], v[96:99], 0\n\t"
      "buffer_load_dwordx4 v48, s[48:51], s82 offen offset:2048 lds\n\t"
      "v_mfma_f32_16x16x32_bf16 v[220:223], a[12:15], v[100:103], v[220:223]\n\t"
      "buffer_load_dwordx4 v48, s[48:51], s83 offen offset:3072 lds\n\t"
      "s_add_u32 s93, s93, 1\n\t"
      "s_cmp_eq_u32 s93, 7\n\t"
      "s_cselect_b32 s93, 0, s93\n\t"
      "v_mfma_f32_16x16x32_bf16 v[224:227], a[16:19], v[96:99], 0\n\t"
      "v_mfma_f32_16x16x32_bf16 v[224:227], a[20:23], v[100:103], v[224:227]\n\t"
      "v_mfma_f32_16x16x32_bf16 v[228:231], a[24:27], v[96:99], 0\n\t"
      "s_waitcnt lgkmcnt(3)\n\t"
      "v_mfma_f32_16x16x32_bf16 v[228:231], a[28:31], v[100:103], v[228:231]\n\t"
      "s_barrier\n\t"
      "ds_read_b128 v[96:99], v44 offset:49152\n\t"
      "v_mfma_f32_16x16x32_bf16 v[232:235], a[0:3], v[88:91], 0\n\t"
      "ds_read_b128 v[100:103], v45 offset:49152\n\t"
      "s_lshl_b32 s95, s92, 7\n\t"
      "s_add_u32 s96, s84, s95\n\t"
      "s_add_u32 m0, s90, 131072\n\t"
      "s_nop 0\n\t"
      "buffer_load_dwordx4 v46, s[44:47], s96 offen offset:0 lds\n\t"
      "v_mfma_f32_16x16x32_bf16 v[232:235], a[4:7], v[92:95], v[232:235]\n\t"
      "v_mfma_f32_16x16x32_bf16 v[236:239], a[8:11], v[88:91], 0\n\t"
      "s_add_u32 s96, s85, s95\n\t"
      "buffer_load_dwordx4 v47, s[44:47], s96 offen offset:1024 lds\n\t"
      "v_mfma_f32_16x16x32_bf16 v[236:239], a[12:15], v[92:95], v[236:239]\n\t"
      "v_mfma_f32_16x16x32_bf16 v[240:243], a[16:19], v[88:91], 0\n\t"
      "s_add_u32 s96, s86, s95\n\t"
      "buffer_load_dwordx4 v46, s[44:47], s96 offen offset:2048 lds\n\t"
      "v_mfma_f32_16x16x32_bf16 v[240:243], a[20:23], v[92:95], v[240:243]\n\t"
      "s_waitcnt lgkmcnt(2)\n\t"
      "v_mfma_f32_16x16x32_bf16 v[244:247], a[24:27], v[88:91], 0\n\t"
      "s_mul_i32 s94, s93, 524288\n\t"
      "s_add_u32 s48, s97, s94\n\t"
      "s_addc_u32 s49, s98, 0\n\t"
      "s_add_u32 m0, s89, 98304\n\t"
      "s_nop 0\n\t"
      "buffer_load_dwordx4 v48, s[48:51], s76 offen offset:0 lds\n\t"
      "v_mfma_f32_16x16x32_bf16 v[244:247], a[28:31], v[92:95], v[244:247]\n\t"
      "s_waitcnt lgkmcnt(1)\n\t"
      "v_mfma_f32_16x16x32_bf16 v[136:139], a[32:35], v[96:99], v[136:139]\n\t"
      "ds_read_b128 v[88:91], v44 offset:51200\n\t"
      "s_waitcnt lgkmcnt(1)\n\t"
      "v_mfma_f32_16x16x32_bf16 v[136:139], a[36:39], v[100:103], v[136:139]\n\t"
      "ds_read_b128 v[92:95], v45 offset:51200\n\t"
      "s_add_u32 s96, s87, s95\n\t"
      "s_add_u32 m0, s90, 131072\n\t"
      "s_nop 0\n\t"
      "buffer_load_dwordx4 v47, s[44:47], s96 offen offset:3072 lds\n\t"
      "s_add_u32 s92, s92, 1\n\t"
      "s_cmp_eq_u32 s92, 7\n\t"
      "s_cselect_b32 s92, 0, s92\n\t"
      "v_mfma_f32_16x16x32_bf16 v[140:143], a[40:43], v[96:99], v[140:143]\n\t"
      "v_mfma_f32_16x16x32_bf16 v[140:143], a[44:47], v[100:103], v[140:143]\n\t"
      "v_mfma_f32_16x16x32_bf16 v[144:147], a[48:51], v[96:99], v[144:147]\n\t"
      "s_add_u32 m0, s89, 98304\n\t"
      "s_nop 0\n\t"
      "buffer_load_dwordx4 v48, s[48:51], s77 offen offset:1024 lds\n\t"
      "v_mfma_f32_16x16x32_bf16 v[144:147], a[52:55], v[100:103], v[144:147]\n\t"
      "buffer_load_dwordx4 v48, s[48:51], s78 offen offset:2048 lds\n\t"
      "v_mfma_f32_16x16x32_bf16 v[148:151], a[56:59], v[96:99], v[148:151]\n\t"
      "buffer_load_dwordx4 v48, s[48:51], s79 offen offset:3072 lds\n\t"
      "v_mfma_f32_16x16x32_bf16 v[148:151], a[60:63], v[100:103], v[148:151]\n\t"
      "ds_read_b128 v[96:99], v44 offset:53248\n\t"
      "s_waitcnt lgkmcnt(2)\n\t"
      "v_mfma_f32_16x16x32_bf16 v[152:155], a[32:35], v[88:91], v[152:155]\n\t"
      "ds_read_b128 v[100:103], v45 offset:53248\n\t"
      "s_waitcnt vmcnt(8)\n\t"
      "s_waitcnt lgkmcnt(2)\n\t"
      "v_mfma_f32_16x16x32_bf16 v[152:155], a[36:39], v[92:95], v[152:155]\n\t"
      "ds_read_b64_tr_b16 a[64:65], v52 offset:0\n\t"
      "ds_read_b64_tr_b16 a[66:67], v52 offset:2048\n\t"
      "v_mfma_f32_16x16x32_bf16 v[156:159], a[40:43], v[88:91], v[156:159]\n\t"
      "ds_read_b64_tr_b16 a[68:69], v52 offset:4096\n\t"
      "ds_read_b64_tr_b16 a[70:71], v52 offset:6144\n\t"
      "v_mfma_f32_16x16x32_bf16 v[156:159], a[44:47], v[92:95], v[156:159]\n\t"
      "ds_read_b64_tr_b16 a[72:73], v52 offset:8\n\t"
      "ds_read_b64_tr_b16 a[74:75], v52 offset:2056\n\t"
      "v_mfma_f32_16x16x32_bf16 v[160:163], a[48:51], v[88:91], v[160:163]\n\t"
      "ds_read_b64_tr_b16 a[76:77], v52 offset:4104\n\t"
      "ds_read_b64_tr_b16 a[78:79], v52 offset:6152\n\t"
      "v_mfma_f32_16x16x32_bf16 v[160:163], a[52:55], v[92:95], v[160:163]\n\t"
      "ds_read_b64_tr_b16 a[80:81], v53 offset:0\n\t"
      "ds_read_b64_tr_b16 a[82:83], v53 offset:2048\n\t"
      "v_mfma_f32_16x16x32_bf16 v[164:167], a[56:59], v[88:91], v[164:167]\n\t"
      "ds_read_b64_tr_b16 a[84:85], v53 offset:4096\n\t"
      "ds_read_b64_tr_b16 a[86:87], v53 offset:6144\n\t"
      "v_mfma_f32_16x16x32_bf16 v[164:167], a[60:63], v[92:95], v[164:167]\n\t"
      "ds_read_b128 v[88:91], v44 offset:55296\n\t"
      "ds_read_b128 v[92:95], v45 offset:55296\n\t"
      "s_waitcnt lgkmcnt(15)\n\t"
      "v_mfma_f32_16x16x32_bf16 v[168:171], a[32:35], v[96:99], v[168:171]\n\t"
      "ds_read_b64_tr_b16 a[88:89], v53 offset:8\n\t"
      "ds_read_b64_tr_b16 a[90:91], v53 offset:2056\n\t"
      "s_waitcnt lgkmcnt(15)\n\t"
      "v_mfma_f32_16x16x32_bf16 v[168:171], a[36:39], v[100:103], v[168:171]\n\t"
      "ds_read_b64_tr_b16 a[92:93], v53 offset:4104\n\t"
      "ds_read_b64_tr_b16 a[94:95], v53 offset:6152\n\t"
      "v_mfma_f32_16x16x32_bf16 v[172:175], a[40:43], v[96:99], v[172:175]\n\t"
      "s_add_u32 m0, s89, 102400\n\t"
      "s_nop 0\n\t"
      "buffer_load_dwordx4 v48, s[48:51], s80 offen offset:0 lds\n\t"
      "v_mfma_f32_16x16x32_bf16 v[172:175], a[44:47], v[100:103], v[172:175]\n\t"
      "buffer_load_dwordx4 v48, s[48:51], s81 offen offset:1024 lds\n\t"
      "v_mfma_f32_16x16x32_bf16 v[176:179], a[48:51], v[96:99], v[176:179]\n\t"
      "buffer_load_dwordx4 v48, s[48:51], s82 offen offset:2048 lds\n\t"
      "v_mfma_f32_16x16x32_bf16 v[176:179], a[52:55], v[100:103], v[176:179]\n\t"
      "buffer_load_dwordx4 v48, s[48:51], s83 offen offset:3072 lds\n\t"
      "s_add_u32 s93, s93, 1\n\t"
      "s_cmp_eq_u32 s93, 7\n\t"
      "s_cselect_b32 s93, 0, s93\n\t"
      "v_mfma_f32_16x16x32_bf16 v[180:183], a[56:59], v[96:99], v[180:183]\n\t"
      "v_mfma_f32_16x16x32_bf16 v[180:183], a[60:63], v[100:103], v[180:183]\n\t"
      "ds_read_b128 v[96:99], v44 offset:57344\n\t"
      "s_waitcnt lgkmcnt(6)\n\t"
      "v_mfma_f32_16x16x32_bf16 v[184:187], a[32:35], v[88:91], v[184:187]\n\t"
      "ds_read_b128 v[100:103], v45 offset:57344\n\t"
      "s_waitcnt lgkmcnt(2)\n\t"
      "v_mfma_f32_16x16x32_bf16 v[184:187], a[36:39], v[92:95], v[184:187]\n\t"
      "s_mul_i32 s94, s93, 524288\n\t"
      "s_add_u32 s48, s97, s94\n\t"
      "s_addc_u32 s49, s98, 0\n\t"
      "s_add_u32 m0, s89, 65536\n\t"
      "s_nop 0\n\t"
      "buffer_load_dwordx4 v48, s[48:51], s76 offen offset:0 lds\n\t"
      "v_mfma_f32_16x16x32_bf16 v[188:191], a[40:43], v[88:91], v[188:191]\n\t"
      "v_mfma_f32_16x16x32_bf16 v[188:191], a[44:47], v[92:95], v[188:191]\n\t"
      "buffer_load_dwordx4 v48, s[48:51], s77 offen offset:1024 lds\n\t"
      "v_mfma_f32_16x16x32_bf16 v[192:195], a[48:51], v[88:91], v[192:195]\n\t"
      "buffer_load_dwordx4 v48, s[48:51], s78 offen offset:2048 lds\n\t"
      "v_mfma_f32_16x16x32_bf16 v[192:195], a[52:55], v[92:95], v[192:195]\n\t"
      "buffer_load_dwordx4 v48, s[48:51], s79 offen offset:3072 lds\n\t"
      "v_mfma_f32_16x16x32_bf16 v[196:199], a[56:59], v[88:91], v[196:199]\n\t"
      "s_add_u32 m0, s89, 69632\n\t"
      "s_nop 0\n\t"
      "buffer_load_dwordx4 v48, s[48:51], s80 offen offset:0 lds\n\t"
      "v_mfma_f32_16x16x32_bf16 v[196:199], a[60:63], v[92:95], v[196:199]\n\t"
      "ds_read_b128 v[88:91], v44 offset:59392\n\t"
      "s_waitcnt lgkmcnt(2)\n\t"
      "v_mfma_f32_16x16x32_bf16 v[200:203], a[32:35], v[96:99], v[200:203]\n\t"
      "ds_read_b128 v[92:95], v45 offset:59392\n\t"
      "buffer_load_dwordx4 v48, s[48:51], s81 offen offset:1024 lds\n\t"
      "s_waitcnt lgkmcnt(2)\n\t"
      "v_mfma_f32_16x16x32_bf16 v[200:203], a[36:39], v[100:103], v[200:203]\n\t"
      "v_mfma_f32_16x16x32_bf16 v[204:207], a[40:43], v[96:99], v[204:207]\n\t"
      "v_mfma_f32_16x16x32_bf16 v[204:207], a[44:47], v[100:103], v[204:207]\n\t"
      "buffer_load_dwordx4 v48, s[48:51], s82 offen offset:2048 lds\n\t"
      "v_mfma_f32_16x16x32_bf16 v[208:211], a[48:51], v[96:99], v[208:211]\n\t"
      "v_mfma_f32_16x16x32_bf16 v[208:211], a[52:55], v[100:103], v[208:211]\n\t"
      "buffer_load_dwordx4 v48, s[48:51], s83 offen offset:3072 lds\n\t"
      "s_add_u32 s93, s93, 1\n\t"
      "s_cmp_eq_u32 s93, 7\n\t"
      "s_cselect_b32 s93, 0, s93\n\t"
      "v_mfma_f32_16x16x32_bf16 v[212:215], a[56:59], v[96:99], v[212:215]\n\t"
      "v_mfma_f32_16x16x32_bf16 v[212:215], a[60:63], v[100:103], v[212:215]\n\t"
      "ds_read_b128 v[96:99], v44 offset:61440\n\t"
      "s_waitcnt lgkmcnt(2)\n\t"
      "v_mfma_f32_16x16x32_bf16 v[216:219], a[32:35], v[88:91], v[216:219]\n\t"
      "ds_read_b128 v[100:103], v45 offset:61440\n\t"
      "s_waitcnt lgkmcnt(2)\n\t"
      "v_mfma_f32_16x16x32_bf16 v[216:219], a[36:39], v[92:95], v[216:219]\n\t"
      "v_mfma_f32_16x16x32_bf16 v[220:223], a[40:43], v[88:91], v[220:223]\n\t"
      "v_mfma_f32_16x16x32_bf16 v[220:223], a[44:47], v[92:95], v[220:223]\n\t"
      "s_waitcnt vmcnt(8)\n\t"
      "ds_read_b64_tr_b16 a[96:97], v52 offset:32768\n\t"
      "v_mfma_f32_16x16x32_bf16 v[224:227], a[48:51], v[88:91], v[224:227]\n\t"
      "ds_read_b64_tr_b16 a[98:99], v52 offset:34816\n\t"
      "ds_read_b64_tr_b16 a[100:101], v52 offset:36864\n\t"
      "v_mfma_f32_16x16x32_bf16 v[224:227], a[52:55], v[92:95], v[224:227]\n\t"
      "s_waitcnt lgkmcnt(3)\n\t"
      "v_mfma_f32_16x16x32_bf16 v[228:231], a[56:59], v[88:91], v[228:231]\n\t"
      "s_barrier\n\t"
      "ds_read_b64_tr_b16 a[102:103], v52 offset:38912\n\t"
      "v_mfma_f32_16x16x32_bf16 v[228:231], a[60:63], v[92:95], v[228:231]\n\t"
      "ds_read_b128 v[88:91], v44 offset:32768\n\t"
      "ds_read_b128 v[92:95], v45 offset:32768\n\t"
      "v_mfma_f32_16x16x32_bf16 v[232:235], a[32:35], v[96:99], v[232:235]\n\t"
      "ds_read_b64_tr_b16 a[104:105], v52 offset:32776\n\t"
      "ds_read_b64_tr_b16 a[106:107], v52 offset:34824\n\t"
      "v_mfma_f32_16x16x32_bf16 v[232:235], a[36:39], v[100:103], v[232:235]\n\t"
      "ds_read_b64_tr_b16 a[108:109], v52 offset:36872\n\t"
      "ds_read_b64_tr_b16 a[110:111], v52 offset:38920\n\t"
      "v_mfma_f32_16x16x32_bf16 v[236:239], a[40:43], v[96:99], v[236:239]\n\t"
      "ds_read_b64_tr_b16 a[112:113], v53 offset:32768\n\t"
      "ds_read_b64_tr_b16 a[114:115], v53 offset:34816\n\t"
      "v_mfma_f32_16x16x32_bf16 v[236:239], a[44:47], v[100:103], v[236:239]\n\t"
      "ds_read_b64_tr_b16 a[116:117], v53 offset:36864\n\t"
      "ds_read_b64_tr_b16 a[118:119], v53 offset:38912\n\t"
      "v_mfma_f32_16x16x32_bf16 v[240:243], a[48:51], v[96:99], v[240:243]\n\t"
      "ds_read_b64_tr_b16 a[120:121], v53 offset:32776\n\t"
      "ds_read_b64_tr_b16 a[122:123], v53 offset:34824\n\t"
      "v_mfma_f32_16x16x32_bf16 v[240:243], a[52:55], v[100:103], v[240:243]\n\t"
      "ds_read_b64_tr_b16 a[124:125], v53 offset:36872\n\t"
      "ds_read_b64_tr_b16 a[126:127], v53 offset:38920\n\t"
      "v_mfma_f32_16x16x32_bf16 v[244:247], a[56:59], v[96:99], v[244:247]\n\t"
      "s_lshl_b32 s95, s92, 7\n\t"
      "s_add_u32 s96, s84, s95\n\t"
      "s_add_u32 m0, s90, 147456\n\t"
      "s_nop 0\n\t"
      "buffer_load_dwordx4 v46, s[44:47], s96 offen offset:0 lds\n\t"
      "v_mfma_f32_16x16x32_bf16 v[244:247], a[60:63], v[100:103], v[244:247]\n\t"
      "s_waitcnt lgkmcnt(13)\n\t"
      "v_mfma_f32_16x16x32_bf16 v[136:139], a[64:67], v[88:91], v[136:139]\n\t"
      "ds_read_b128 v[96:99], v44 offset:34816\n\t"
      "ds_read_b128 v[100:103], v45 offset:34816\n\t"
      "s_waitcnt lgkmcnt(14)\n\t"
      "v_mfma_f32_16x16x32_bf16 v[136:139], a[68:71], v[92:95], v[136:139]\n\t"
      "s_add_u32 s96, s85, s95\n\t"
      "buffer_load_dwordx4 v47, s[44:47], s96 offen offset:1024 lds\n\t"
      "v_mfma_f32_16x16x32_bf16 v[140:143], a[72:75], v[88:91], v[140:143]\n\t"
      "s_add_u32 s96, s86, s95\n\t"
      "buffer_load_dwordx4 v46, s[44:47], s96 offen offset:2048 lds\n\t"
      "v_mfma_f32_16x16x32_bf16 v[140:143], a[76:79], v[92:95], v[140:143]\n\t"
      "v_mfma_f32_16x16x32_bf16 v[144:147], a[80:83], v[88:91], v[144:147]\n\t"
      "s_add_u32 s96, s87, s95\n\t"
      "buffer_load_dwordx4 v47, s[44:47], s96 offen offset:3072 lds\n\t"
      "s_add_u32 s92, s92, 1\n\t"
      "s_cmp_eq_u32 s92, 7\n\t"
      "s_cselect_b32 s92, 0, s92\n\t"
      "v_mfma_f32_16x16x32_bf16 v[144:147], a[84:87], v[92:95], v[144:147]\n\t"
      "v_mfma_f32_16x16x32_bf16 v[148:151], a[88:91], v[88:91], v[148:151]\n\t"
      "v_mfma_f32_16x16x32_bf16 v[148:151], a[92:95], v[92:95], v[148:151]\n\t"
      "ds_read_b128 v[88:91], v44 offset:36864\n\t"
      "ds_read_b128 v[92:95], v45 offset:36864\n\t"
      "s_waitcnt lgkmcnt(3)\n\t"
      "v_mfma_f32_16x16x32_bf16 v[152:155], a[64:67], v[96:99], v[152:155]\n\t"
      "s_mul_i32 s94, s93, 524288\n\t"
      "s_add_u32 s48, s97, s94\n\t"
      "s_addc_u32 s49, s98, 0\n\t"
      "s_add_u32 m0, s89, 98304\n\t"
      "s_nop 0\n\t"
      "buffer_load_dwordx4 v48, s[48:51], s76 offen offset:0 lds\n\t"
      "s_waitcnt lgkmcnt(2)\n\t"
      "v_mfma_f32_16x16x32_bf16 v[152:155], a[68:71], v[100:103], v[152:155]\n\t"
      "v_mfma_f32_16x16x32_bf16 v[156:159], a[72:75], v[96:99], v[156:159]\n\t"
      "v_mfma_f32_16x16x32_bf16 v[156:159], a[76:79], v[100:103], v[156:159]\n\t"
      "buffer_load_dwordx4 v48, s[48:51], s77 offen offset:1024 lds\n\t"
      "v_mfma_f32_16x16x32_bf16 v[160:163], a[80:83], v[96:99], v[160:163]\n\t"
      "buffer_load_dwordx4 v48, s[48:51], s78 offen offset:2048 lds\n\t"
      "v_mfma_f32_16x16x32_bf16 v[160:163], a[84:87], v[100:103], v[160:163]\n\t"
      "buffer_load_dwordx4 v48, s[48:51], s79 offen offset:3072 lds\n\t"
      "v_mfma_f32_16x16x32_bf16 v[164:167], a[88:91], v[96:99], v[164:167]\n\t"
      "s_add_u32 m0, s89, 102400\n\t"
      "s_nop 0\n\t"
      "buffer_load_dwordx4 v48, s[48:51], s80 offen offset:0 lds\n\t"
      "v_mfma_f32_16x16x32_bf16 v[164:167], a[92:95], v[100:103], v[164:167]\n\t"
      "ds_read_b128 v[96:99], v44 offset:38912\n\t"
      "ds_read_b128 v[100:103], v45 offset:38912\n\t"
      "s_waitcnt lgkmcnt(3)\n\t"
      "v_mfma_f32_16x16x32_bf16 v[168:171], a[64:67], v[88:91], v[168:171]\n\t"
      "buffer_load_dwordx4 v48, s[48:51], s81 offen offset:1024 lds\n\t"
      "s_waitcnt lgkmcnt(2)\n\t"
      "v_mfma_f32_16x16x32_bf16 v[168:171], a[68:71], v[92:95], v[168:171]\n\t"
      "s_waitcnt vmcnt(10)\n\t"
      "ds_read_b64_tr_b16 a[128:129], v52 offset:0\n\t"
      "v_mfma_f32_16x16x32_bf16 v[172:175], a[72:75], v[88:91], v[172:175]\n\t"
      "ds_read_b64_tr_b16 a[130:131], v52 offset:2048\n\t"
      "ds_read_b64_tr_b16 a[132:133], v52 offset:4096\n\t"
      "v_mfma_f32_16x16x32_bf16 v[172:175], a[76:79], v[92:95], v[172:175]\n\t"
      "ds_read_b64_tr_b16 a[134:135], v52 offset:6144\n\t"
      "ds_read_b64_tr_b16 a[136:137], v52 offset:8\n\t"
      "v_mfma_f32_16x16x32_bf16 v[176:179], a[80:83], v[88:91], v[176:179]\n\t"
      "ds_read_b64_tr_b16 a[138:139], v52 offset:2056\n\t"
      "ds_read_b64_tr_b16 a[140:141], v52 offset:4104\n\t"
      "v_mfma_f32_16x16x32_bf16 v[176:179], a[84:87], v[92:95], v[176:179]\n\t"
      "ds_read_b64_tr_b16 a[142:143], v52 offset:6152\n\t"
      "ds_read_b64_tr_b16 a[144:145], v53 offset:0\n\t"
      "v_mfma_f32_16x16x32_bf16 v[180:183], a[88:91], v[88:91], v[180:183]\n\t"
      "ds_read_b64_tr_b16 a[146:147], v53 offset:2048\n\t"
      "ds_read_b64_tr_b16 a[148:149], v53 offset:4096\n\t"
      "v_mfma_f32_16x16x32_bf16 v[180:183], a[92:95], v[92:95], v[180:183]\n\t"
      "ds_read_b128 v[88:91], v44 offset:40960\n\t"
      "ds_read_b128 v[92:95], v45 offset:40960\n\t"
      "s_waitcnt lgkmcnt(14)\n\t"
      "v_mfma_f32_16x16x32_bf16 v[184:187], a[64:67], v[96:99], v[184:187]\n\t"
      "ds_read_b64_tr_b16 a[150:151], v53 offset:6144\n\t"
      "ds_read_b64_tr_b16 a[152:153], v53 offset:8\n\t"
      "s_waitcnt lgkmcnt(15)\n\t"
      "v_mfma_f32_16x16x32_bf16 v[184:187], a[68:71], v[100:103], v[184:187]\n\t"
      "ds_read_b64_tr_b16 a[154:155], v53 offset:2056\n\t"
      "ds_read_b64_tr_b16 a[156:157], v53 offset:4104\n\t"
      "v_mfma_f32_16x16x32_bf16 v[188:191], a[72:75], v[96:99], v[188:191]\n\t"
      "ds_read_b64_tr_b16 a[158:159], v53 offset:6152\n\t"
      "buffer_load_dwordx4 v48, s[48:51], s82 offen offset:2048 lds\n\t"
      "v_mfma_f32_16x16x32_bf16 v[188:191], a[76:79], v[100:103], v[188:191]\n\t"
      "buffer_load_dwordx4 v48, s[48:51], s83 offen offset:3072 lds\n\t"
      "s_add_u32 s93, s93, 1\n\t"
      "s_cmp_eq_u32 s93, 7\n\t"
      "s_cselect_b32 s93, 0, s93\n\t"
      "v_mfma_f32_16x16x32_bf16 v[192:195], a[80:83], v[96:99], v[192:195]\n\t"
      "v_mfma_f32_16x16x32_bf16 v[192:195], a[84:87], v[100:103], v[192:195]\n\t"
      "v_mfma_f32_16x16x32_bf16 v[196:199], a[88:91], v[96:99], v[196:199]\n\t"
      "s_waitcnt lgkmcnt(0)\n\t"
      "s_mul_i32 s94, s93, 524288\n\t"
      "s_add_u32 s48, s97, s94\n\t"
      "s_addc_u32 s49, s98, 0\n\t"
      "s_add_u32 m0, s89, 65536\n\t"
      "s_nop 0\n\t"
      "buffer_load_dwordx4 v48, s[48:51], s76 offen offset:0 lds\n\t"
      "v_mfma_f32_16x16x32_bf16 v[196:199], a[92:95], v[100:103], v[196:199]\n\t"
      "v_mfma_f32_16x16x32_bf16 v[200:203], a[64:67], v[88:91], v[200:203]\n\t"
      "v_mfma_f32_16x16x32_bf16 v[200:203], a[68:71], v[92:95], v[200:203]\n\t"
      "ds_read_b128 v[96:99], v44 offset:43008\n\t"
      "ds_read_b128 v[100:103], v45 offset:43008\n\t"
      "v_mfma_f32_16x16x32_bf16 v[204:207], a[72:75], v[88:91], v[204:207]\n\t"
      "buffer_load_dwordx4 v48, s[48:51], s77 offen offset:1024 lds\n\t"
      "v_mfma_f32_16x16x32_bf16 v[204:207], a[76:79], v[92:95], v[204:207]\n\t"
      "buffer_load_dwordx4 v48, s[48:51], s78 offen offset:2048 lds\n\t"
      "v_mfma_f32_16x16x32_bf16 v[208:211], a[80:83], v[88:91], v[208:211]\n\t"
      "buffer_load_dwordx4 v48, s[48:51], s79 offen offset:3072 lds\n\t"
      "v_mfma_f32_16x16x32_bf16 v[208:211], a[84:87], v[92:95], v[208:211]\n\t"
      "v_mfma_f32_16x16x32_bf16 v[212:215], a[88:91], v[88:91], v[212:215]\n\t"
      "v_mfma_f32_16x16x32_bf16 v[212:215], a[92:95], v[92:95], v[212:215]\n\t"
      "ds_read_b128 v[88:91], v44 offset:45056\n\t"
      "ds_read_b128 v[92:95], v45 offset:45056\n\t"
      "s_waitcnt lgkmcnt(3)\n\t"
      "v_mfma_f32_16x16x32_bf16 v[216:219], a[64:67], v[96:99], v[216:219]\n\t"
      "s_add_u32 m0, s89, 69632\n\t"
      "s_nop 0\n\t"
      "buffer_load_dwordx4 v48, s[48:51], s80 offen offset:0 lds\n\t"
      "s_waitcnt lgkmcnt(2)\n\t"
      "v_mfma_f32_16x16x32_bf16 v[216:219], a[68:71], v[100:103], v[216:219]\n\t"
      "buffer_load_dwordx4 v48, s[48:51], s81 offen offset:1024 lds\n\t"
      "v_mfma_f32_16x16x32_bf16 v[220:223], a[72:75], v[96:99], v[220:223]\n\t"
      "v_mfma_f32_16x16x32_bf16 v[220:223], a[76:79], v[100:103], v[220:223]\n\t"
      "v_mfma_f32_16x16x32_bf16 v[224:227], a[80:83], v[96:99], v[224:227]\n\t"
      "buffer_load_dwordx4 v48, s[48:51], s82 offen offset:2048 lds\n\t"
      "v_mfma_f32_16x16x32_bf16 v[224:227], a[84:87], v[100:103], v[224:227]\n\t"
      "s_waitcnt lgkmcnt(0)\n\t"
      "s_waitcnt vmcnt(15)\n\t"
      "v_mfma_f32_16x16x32_bf16 v[228:231], a[88:91], v[96:99], v[228:231]\n\t"
      "s_barrier\n\t"
      "buffer_load_dwordx4 v48, s[48:51], s83 offen offset:3072 lds\n\t"
      "s_add_u32 s93, s93, 1\n\t"
      "s_cmp_eq_u32 s93, 7\n\t"
      "s_cselect_b32 s93, 0, s93\n\t"
      "v_mfma_f32_16x16x32_bf16 v[228:231], a[92:95], v[100:103], v[228:231]\n\t"
      "v_mfma_f32_16x16x32_bf16 v[232:235], a[64:67], v[88:91], v[232:235]\n\t"
      "v_mfma_f32_16x16x32_bf16 v[232:235], a[68:71], v[92:95], v[232:235]\n\t"
      "ds_read_b128 v[96:99], v44 offset:49152\n\t"
      "ds_read_b128 v[100:103], v45 offset:49152\n\t"
      "v_mfma_f32_16x16x32_bf16 v[236:239], a[72:75], v[88:91], v[236:239]\n\t"
      "s_lshl_b32 s95, s92, 7\n\t"
      "s_add_u32 s96, s84, s95\n\t"
      "s_add_u32 m0, s90, 131072\n\t"
      "s_nop 0\n\t"
      "buffer_load_dwordx4 v46, s[44:47], s96 offen offset:0 lds\n\t"
      "v_mfma_f32_16x16x32_bf16 v[236:239], a[76:79], v[92:95], v[236:239]\n\t"
      "v_mfma_f32_16x16x32_bf16 v[240:243], a[80:83], v[88:91], v[240:243]\n\t"
      "s_add_u32 s96, s85, s95\n\t"
      "buffer_load_dwordx4 v47, s[44:47], s96 offen offset:1024 lds\n\t"
      "v_mfma_f32_16x16x32_bf16 v[240:243], a[84:87], v[92:95], v[240:243]\n\t"
      "s_add_u32 s96, s86, s95\n\t"
      "buffer_load_dwordx4 v46, s[44:47], s96 offen offset:2048 lds\n\t"
      "v_mfma_f32_16x16x32_bf16 v[244:247], a[88:91], v[88:91], v[244:247]\n\t"
      "v_mfma_f32_16x16x32_bf16 v[244:247], a[92:95], v[92:95], v[244:247]\n\t"
      "ds_read_b128 v[88:91], v44 offset:51200\n\t"
      "ds_read_b128 v[92:95], v45 offset:51200\n\t"
      "s_waitcnt lgkmcnt(3)\n\t"
      "v_mfma_f32_16x16x32_bf16 v[136:139], a[96:99], v[96:99], v[136:139]\n\t"
      "s_add_u32 s96, s87, s95\n\t"
      "buffer_load_dwordx4 v47, s[44:47], s96 offen offset:3072 lds\n\t"
      "s_add_u32 s92, s92, 1\n\t"
      "s_cmp_eq_u32 s92, 7\n\t"
      "s_cselect_b32 s92, 0, s92\n\t"
      "s_waitcnt lgkmcnt(2)\n\t"
      "v_mfma_f32_16x16x32_bf16 v[136:139], a[100:103], v[100:103], v[136:139]\n\t"
      "v_mfma_f32_16x16x32_bf16 v[140:143], a[104:107], v[96:99], v[140:143]\n\t"
      "v_mfma_f32_16x16x32_bf16 v[140:143], a[108:111], v[100:103], v[140:143]\n\t"
      "s_add_u32 m0, s89, 32768\n\t"
      "s_nop 0\n\t"
      "buffer_load_dwordx4 v12, s[36:39], s60 offen offset:0 nt lds\n\t"
      "v_mfma_f32_16x16x32_bf16 v[144:147], a[112:115], v[96:99], v[144:147]\n\t"
      "buffer_load_dwordx4 v13, s[36:39], s61 offen offset:1024 nt lds\n\t"
      "v_mfma_f32_16x16x32_bf16 v[144:147], a[116:119], v[100:103], v[144:147]\n\t"
      "buffer_load_dwordx4 v12, s[36:39], s62 offen offset:2048 nt lds\n\t"
      "v_mfma_f32_16x16x32_bf16 v[148:151], a[120:123], v[96:99], v[148:151]\n\t"
      "buffer_load_dwordx4 v13, s[36:39], s63 offen offset:3072 nt lds\n\t"
      "v_mfma_f32_16x16x32_bf16 v[148:151], a[124:127], v[100:103], v[148:151]\n\t"
      "ds_read_b128 v[96:99], v44 offset:53248\n\t"
      "ds_read_b128 v[100:103], v45 offset:53248\n\t"
      "s_waitcnt lgkmcnt(3)\n\t"
      "v_mfma_f32_16x16x32_bf16 v[152:155], a[96:99], v[88:91], v[152:155]\n\t"
      "s_add_u32 m0, s89, 36864\n\t"
      "s_nop 0\n\t"
      "buffer_load_dwordx4 v12, s[36:39], s64 offen offset:0 nt lds\n\t"
      "s_waitcnt lgkmcnt(2)\n\t"
      "v_mfma_f32_16x16x32_bf16 v[152:155], a[100:103], v[92:95], v[152:155]\n\t"
      "buffer_load_dwordx4 v13, s[36:39], s65 offen offset:1024 nt lds\n\t"
      "v_mfma_f32_16x16x32_bf16 v[156:159], a[104:107], v[88:91], v[156:159]\n\t"
      "v_mfma_f32_16x16x32_bf16 v[156:159], a[108:111], v[92:95], v[156:159]\n\t"
      "v_mfma_f32_16x16x32_bf16 v[160:163], a[112:115], v[88:91], v[160:163]\n\t"
      "buffer_load_dwordx4 v12, s[36:39], s66 offen offset:2048 nt lds\n\t"
      "v_mfma_f32_16x16x32_bf16 v[160:163], a[116:119], v[92:95], v[160:163]\n\t"
      "v_mfma_f32_16x16x32_bf16 v[164:167], a[120:123], v[88:91], v[164:167]\n\t"
      "v_mfma_f32_16x16x32_bf16 v[164:167], a[124:127], v[92:95], v[164:167]\n\t"
      "ds_read_b128 v[88:91], v44 offset:55296\n\t"
      "ds_read_b128 v[92:95], v45 offset:55296\n\t"
      "s_waitcnt lgkmcnt(3)\n\t"
      "v_mfma_f32_16x16x32_bf16 v[168:171], a[96:99], v[96:99], v[168:171]\n\t"
      "buffer_load_dwordx4 v13, s[36:39], s67 offen offset:3072 nt lds\n\t"
      "s_add_u32 s36, s36, 32768\n\t"
      "s_addc_u32 s37, s37, 0\n\t"
      "s_waitcnt lgkmcnt(2)\n\t"
      "v_mfma_f32_16x16x32_bf16 v[168:171], a[100:103], v[100:103], v[168:171]\n\t"
      "v_mfma_f32_16x16x32_bf16 v[172:175], a[104:107], v[96:99], v[172:175]\n\t"
      "s_waitcnt vmcnt(20)\n\t"
      "ds_read_b64_tr_b16 a[160:161], v52 offset:32768\n\t"
      "v_mfma_f32_16x16x32_bf16 v[172:175], a[108:111], v[100:103], v[172:175]\n\t"
      "ds_read_b64_tr_b16 a[162:163], v52 offset:34816\n\t"
      "ds_read_b64_tr_b16 a[164:165], v52 offset:36864\n\t"
      "v_mfma_f32_16x16x32_bf16 v[176:179], a[112:115], v[96:99], v[176:179]\n\t"
      "ds_read_b64_tr_b16 a[166:167], v52 offset:38912\n\t"
      "ds_read_b64_tr_b16 a[168:169], v52 offset:32776\n\t"
      "v_mfma_f32_16x16x32_bf16 v[176:179], a[116:119], v[100:103], v[176:179]\n\t"
      "ds_read_b64_tr_b16 a[170:171], v52 offset:34824\n\t"
      "ds_read_b64_tr_b16 a[172:173], v52 offset:36872\n\t"
      "v_mfma_f32_16x16x32_bf16 v[180:183], a[120:123], v[96:99], v[180:183]\n\t"
      "ds_read_b64_tr_b16 a[174:175], v52 offset:38920\n\t"
      "ds_read_b64_tr_b16 a[176:177], v53 offset:32768\n\t"
      "v_mfma_f32_16x16x32_bf16 v[180:183], a[124:127], v[100:103], v[180:183]\n\t"
      "ds_read_b128 v[96:99], v44 offset:57344\n\t"
      "ds_read_b128 v[100:103], v45 offset:57344\n\t"
      "s_waitcnt lgkmcnt(12)\n\t"
      "v_mfma_f32_16x16x32_bf16 v[184:187], a[96:99], v[88:91], v[184:187]\n\t"
      "ds_read_b64_tr_b16 a[178:179], v53 offset:34816\n\t"
      "ds_read_b64_tr_b16 a[180:181], v53 offset:36864\n\t"
      "s_waitcnt lgkmcnt(13)\n\t"
      "v_mfma_f32_16x16x32_bf16 v[184:187], a[100:103], v[92:95], v[184:187]\n\t"
      "ds_read_b64_tr_b16 a[182:183], v53 offset:38912\n\t"
      "ds_read_b64_tr_b16 a[184:185], v53 offset:32776\n\t"
      "v_mfma_f32_16x16x32_bf16 v[188:191], a[104:107], v[88:91], v[188:191]\n\t"
      "ds_read_b64_tr_b16 a[186:187], v53 offset:34824\n\t"
      "ds_read_b64_tr_b16 a[188:189], v53 offset:36872\n\t"
      "v_mfma_f32_16x16x32_bf16 v[188:191], a[108:111], v[92:95], v[188:191]\n\t"
      "ds_read_b64_tr_b16 a[190:191], v53 offset:38920\n\t"
      "s_waitcnt vmcnt(12)\n\t"
      "v_mfma_f32_16x16x32_bf16 v[192:195], a[112:115], v[88:91], v[192:195]\n\t"
      "ds_read_b64_tr_b16 a[192:193], v52 offset:0\n\t"
      "ds_read_b64_tr_b16 a[194:195], v52 offset:2048\n\t"
      "v_mfma_f32_16x16x32_bf16 v[192:195], a[116:119], v[92:95], v[192:195]\n\t"
      "ds_read_b64_tr_b16 a[196:197], v52 offset:4096\n\t"
      "ds_read_b64_tr_b16 a[198:199], v52 offset:6144\n\t"
      "v_mfma_f32_16x16x32_bf16 v[196:199], a[120:123], v[88:91], v[196:199]\n\t"
      "ds_read_b64_tr_b16 a[200:201], v52 offset:8\n\t"
      "ds_read_b64_tr_b16 a[202:203], v52 offset:2056\n\t"
      "v_mfma_f32_16x16x32_bf16 v[196:199], a[124:127], v[92:95], v[196:199]\n\t"
      "ds_read_b128 v[88:91], v44 offset:59392\n\t"
      "ds_read_b128 v[92:95], v45 offset:59392\n\t"
      "s_waitcnt lgkmcnt(15)\n\t"
      "v_mfma_f32_16x16x32_bf16 v[200:203], a[96:99], v[96:99], v[200:203]\n\t"
      "ds_read_b64_tr_b16 a[204:205], v52 offset:4104\n\t"
      "ds_read_b64_tr_b16 a[206:207], v52 offset:6152\n\t"
      "v_mfma_f32_16x16x32_bf16 v[200:203], a[100:103], v[100:103], v[200:203]\n\t"
      "ds_read_b64_tr_b16 a[208:209], v53 offset:0\n\t"
      "ds_read_b64_tr_b16 a[210:211], v53 offset:2048\n\t"
      "v_mfma_f32_16x16x32_bf16 v[204:207], a[104:107], v[96:99], v[204:207]\n\t"
      "ds_read_b64_tr_b16 a[212:213], v53 offset:4096\n\t"
      "ds_read_b64_tr_b16 a[214:215], v53 offset:6144\n\t"
      "v_mfma_f32_16x16x32_bf16 v[204:207], a[108:111], v[100:103], v[204:207]\n\t"
      "ds_read_b64_tr_b16 a[216:217], v53 offset:8\n\t"
      "ds_read_b64_tr_b16 a[218:219], v53 offset:2056\n\t"
      "v_mfma_f32_16x16x32_bf16 v[208:211], a[112:115], v[96:99], v[208:211]\n\t"
      "ds_read_b64_tr_b16 a[220:221], v53 offset:4104\n\t"
      "ds_read_b64_tr_b16 a[222:223], v53 offset:6152\n\t"
      "v_mfma_f32_16x16x32_bf16 v[208:211], a[116:119], v[100:103], v[208:211]\n\t"
      "v_mfma_f32_16x16x32_bf16 v[212:215], a[120:123], v[96:99], v[212:215]\n\t"
      "v_mfma_f32_16x16x32_bf16 v[212:215], a[124:127], v[100:103], v[212:215]\n\t"
      "ds_read_b128 v[96:99], v44 offset:61440\n\t"
      "ds_read_b128 v[100:103], v45 offset:61440\n\t"
      "s_waitcnt lgkmcnt(13)\n\t"
      "v_mfma_f32_16x16x32_bf16 v[216:219], a[96:99], v[88:91], v[216:219]\n\t"
      "s_waitcnt lgkmcnt(12)\n\t"
      "v_mfma_f32_16x16x32_bf16 v[216:219], a[100:103], v[92:95], v[216:219]\n\t"
      "v_mfma_f32_16x16x32_bf16 v[220:223], a[104:107], v[88:91], v[220:223]\n\t"
      "v_mfma_f32_16x16x32_bf16 v[220:223], a[108:111], v[92:95], v[220:223]\n\t"
      "v_mfma_f32_16x16x32_bf16 v[224:227], a[112:115], v[88:91], v[224:227]\n\t"
      "v_mfma_f32_16x16x32_bf16 v[224:227], a[116:119], v[92:95], v[224:227]\n\t"
      "s_waitcnt lgkmcnt(0)\n\t"
      "v_mfma_f32_16x16x32_bf16 v[228:231], a[120:123], v[88:91], v[228:231]\n\t"
      "s_waitcnt vmcnt(8)\n\t"
      "s_barrier\n\t"
      "v_mfma_f32_16x16x32_bf16 v[228:231], a[124:127], v[92:95], v[228:231]\n\t"
      "ds_read_b128 v[88:91], v44 offset:32768\n\t"
      "ds_read_b128 v[92:95], v45 offset:32768\n\t"
      "v_mfma_f32_16x16x32_bf16 v[232:235], a[96:99], v[96:99], v[232:235]\n\t"
      "s_lshl_b32 s95, s92, 7\n\t"
      "s_add_u32 s96, s84, s95\n\t"
      "s_add_u32 m0, s90, 147456\n\t"
      "s_nop 0\n\t"
      "buffer_load_dwordx4 v46, s[44:47], s96 offen offset:0 lds\n\t"
      "v_mfma_f32_16x16x32_bf16 v[232:235], a[100:103], v[100:103], v[232:235]\n\t"
      "v_mfma_f32_16x16x32_bf16 v[236:239], a[104:107], v[96:99], v[236:239]\n\t"
      "s_add_u32 s96, s85, s95\n\t"
      "buffer_load_dwordx4 v47, s[44:47], s96 offen offset:1024 lds\n\t"
      "v_mfma_f32_16x16x32_bf16 v[236:239], a[108:111], v[100:103], v[236:239]\n\t"
      "v_mfma_f32_16x16x32_bf16 v[240:243], a[112:115], v[96:99], v[240:243]\n\t"
      "s_add_u32 s96, s86, s95\n\t"
      "buffer_load_dwordx4 v46, s[44:47], s96 offen offset:2048 lds\n\t"
      "v_mfma_f32_16x16x32_bf16 v[240:243], a[116:119], v[100:103], v[240:243]\n\t"
      "v_mfma_f32_16x16x32_bf16 v[244:247], a[120:123], v[96:99], v[244:247]\n\t"
      "v_mfma_f32_16x16x32_bf16 v[244:247], a[124:127], v[100:103], v[244:247]\n\t"
      "ds_read_b128 v[96:99], v44 offset:34816\n\t"
      "ds_read_b128 v[100:103], v45 offset:34816\n\t"
      "s_waitcnt lgkmcnt(3)\n\t"
      "v_mfma_f32_16x16x32_bf16 v[136:139], a[128:131], v[88:91], v[136:139]\n\t"
      "s_add_u32 s96, s87, s95\n\t"
      "buffer_load_dwordx4 v47, s[44:47], s96 offen offset:3072 lds\n\t"
      "s_add_u32 s92, s92, 1\n\t"
      "s_cmp_eq_u32 s92, 7\n\t"
      "s_cselect_b32 s92, 0, s92\n\t"
      "s_waitcnt lgkmcnt(2)\n\t"
      "v_mfma_f32_16x16x32_bf16 v[136:139], a[132:135], v[92:95], v[136:139]\n\t"
      "v_mfma_f32_16x16x32_bf16 v[140:143], a[136:139], v[88:91], v[140:143]\n\t"
      "v_mfma_f32_16x16x32_bf16 v[140:143], a[140:143], v[92:95], v[140:143]\n\t"
      "s_lshl_b32 s95, s92, 7\n\t"
      "s_add_u32 s96, s84, s95\n\t"
      "s_add_u32 m0, s90, 98304\n\t"
      "s_nop 0\n\t"
      "buffer_load_dwordx4 v46, s[44:47], s96 offen offset:0 lds\n\t"
      "v_mfma_f32_16x16x32_bf16 v[144:147], a[144:147], v[88:91], v[144:147]\n\t"
      "v_mfma_f32_16x16x32_bf16 v[144:147], a[148:151], v[92:95], v[144:147]\n\t"
      "s_add_u32 s96, s85, s95\n\t"
      "buffer_load_dwordx4 v47, s[44:47], s96 offen offset:1024 lds\n\t"
      "v_mfma_f32_16x16x32_bf16 v[148:151], a[152:155], v[88:91], v[148:151]\n\t"
      "v_mfma_f32_16x16x32_bf16 v[148:151], a[156:159], v[92:95], v[148:151]\n\t"
      "ds_read_b128 v[88:91], v44 offset:36864\n\t"
      "ds_read_b128 v[92:95], v45 offset:36864\n\t"
      "s_waitcnt lgkmcnt(3)\n\t"
      "v_mfma_f32_16x16x32_bf16 v[152:155], a[128:131], v[96:99], v[152:155]\n\t"
      "s_add_u32 s96, s86, s95\n\t"
      "buffer_load_dwordx4 v46, s[44:47], s96 offen offset:2048 lds\n\t"
      "s_waitcnt lgkmcnt(2)\n\t"
      "v_mfma_f32_16x16x32_bf16 v[152:155], a[132:135], v[100:103], v[152:155]\n\t"
      "s_add_u32 s96, s87, s95\n\t"
      "buffer_load_dwordx4 v47, s[44:47], s96 offen offset:3072 lds\n\t"
      "s_add_u32 s92, s92, 1\n\t"
      "s_cmp_eq_u32 s92, 7\n\t"
      "s_cselect_b32 s92, 0, s92\n\t"
      "v_mfma_f32_16x16x32_bf16 v[156:159], a[136:139], v[96:99], v[156:159]\n\t"
      "v_mfma_f32_16x16x32_bf16 v[156:159], a[140:143], v[100:103], v[156:159]\n\t"
      "v_mfma_f32_16x16x32_bf16 v[160:163], a[144:147], v[96:99], v[160:163]\n\t"
      "s_add_u32 m0, s89, 0\n\t"
      "s_nop 0\n\t"
      "buffer_load_dwordx4 v12, s[36:39], s60 offen offset:0 nt lds\n\t"
      "v_mfma_f32_16x16x32_bf16 v[160:163], a[148:151], v[100:103], v[160:163]\n\t"
      "buffer_load_dwordx4 v13, s[36:39], s61 offen offset:1024 nt lds\n\t"
      "v_mfma_f32_16x16x32_bf16 v[164:167], a[152:155], v[96:99], v[164:167]\n\t"
      "v_mfma_f32_16x16x32_bf16 v[164:167], a[156:159], v[100:103], v[164:167]\n\t"
      "ds_read_b128 v[96:99], v44 offset:38912\n\t"
      "ds_read_b128 v[100:103], v45 offset:38912\n\t"
      "s_waitcnt lgkmcnt(3)\n\t"
      "v_mfma_f32_16x16x32_bf16 v[168:171], a[128:131], v[88:91], v[168:171]\n\t"
      "buffer_load_dwordx4 v12, s[36:39], s62 offen offset:2048 nt lds\n\t"
      "s_waitcnt lgkmcnt(2)\n\t"
      "v_mfma_f32_16x16x32_bf16 v[168:171], a[132:135], v[92:95], v[168:171]\n\t"
      "v_mfma_f32_16x16x32_bf16 v[172:175], a[136:139], v[88:91], v[172:175]\n\t"
      "buffer_load_dwordx4 v13, s[36:39], s63 offen offset:3072 nt lds\n\t"
      "v_mfma_f32_16x16x32_bf16 v[172:175], a[140:143], v[92:95], v[172:175]\n\t"
      "v_mfma_f32_16x16x32_bf16 v[176:179], a[144:147], v[88:91], v[176:179]\n\t"
      "v_mfma_f32_16x16x32_bf16 v[176:179], a[148:151], v[92:95], v[176:179]\n\t"
      "s_add_u32 m0, s89, 4096\n\t"
      "s_nop 0\n\t"
      "buffer_load_dwordx4 v12, s[36:39], s64 offen offset:0 nt lds\n\t"
      "v_mfma_f32_16x16x32_bf16 v[180:183], a[152:155], v[88:91], v[180:183]\n\t"
      "v_mfma_f32_16x16x32_bf16 v[180:183], a[156:159], v[92:95], v[180:183]\n\t"
      "ds_read_b128 v[88:91], v44 offset:40960\n\t"
      "ds_read_b128 v[92:95], v45 offset:40960\n\t"
      "s_waitcnt lgkmcnt(3)\n\t"
      "v_mfma_f32_16x16x32_bf16 v[184:187], a[128:131], v[96:99], v[184:187]\n\t"
      "s_waitcnt lgkmcnt(2)\n\t"
      "v_mfma_f32_16x16x32_bf16 v[184:187], a[132:135], v[100:103], v[184:187]\n\t"
      "buffer_load_dwordx4 v13, s[36:39], s65 offen offset:1024 nt lds\n\t"
      "v_mfma_f32_16x16x32_bf16 v[188:191], a[136:139], v[96:99], v[188:191]\n\t"
      "v_mfma_f32_16x16x32_bf16 v[188:191], a[140:143], v[100:103], v[188:191]\n\t"
      "v_mfma_f32_16x16x32_bf16 v[192:195], a[144:147], v[96:99], v[192:195]\n\t"
      "buffer_load_dwordx4 v12, s[36:39], s66 offen offset:2048 nt lds\n\t"
      "v_mfma_f32_16x16x32_bf16 v[192:195], a[148:151], v[100:103], v[192:195]\n\t"
      "v_mfma_f32_16x16x32_bf16 v[196:199], a[152:155], v[96:99], v[196:199]\n\t"
      "v_mfma_f32_16x16x32_bf16 v[196:199], a[156:159], v[100:103], v[196:199]\n\t"
      "ds_read_b128 v[96:99], v44 offset:43008\n\t"
      "ds_read_b128 v[100:103], v45 offset:43008\n\t"
      "s_waitcnt lgkmcnt(3)\n\t"
      "v_mfma_f32_16x16x32_bf16 v[200:203], a[128:131], v[88:91], v[200:203]\n\t"
      "buffer_load_dwordx4 v13, s[36:39], s67 offen offset:3072 nt lds\n\t"
      "s_add_u32 s36, s36, 32768\n\t"
      "s_addc_u32 s37, s37, 0\n\t"
      "s_waitcnt lgkmcnt(2)\n\t"
      "v_mfma_f32_16x16x32_bf16 v[200:203], a[132:135], v[92:95], v[200:203]\n\t"
      "v_mfma_f32_16x16x32_bf16 v[204:207], a[136:139], v[88:91], v[204:207]\n\t"
      "v_mfma_f32_16x16x32_bf16 v[204:207], a[140:143], v[92:95], v[204:207]\n\t"
      "v_mfma_f32_16x16x32_bf16 v[208:211], a[144:147], v[88:91], v[208:211]\n\t"
      "v_mfma_f32_16x16x32_bf16 v[208:211], a[148:151], v[92:95], v[208:211]\n\t"
      "v_mfma_f32_16x16x32_bf16 v[212:215], a[152:155], v[88:91], v[212:215]\n\t"
      "v_mfma_f32_16x16x32_bf16 v[212:215], a[156:159], v[92:95], v[212:215]\n\t"
      "ds_read_b128 v[88:91], v44 offset:45056\n\t"
      "ds_read_b128 v[92:95], v45 offset:45056\n\t"
      "s_waitcnt lgkmcnt(3)\n\t"
      "v_mfma_f32_16x16x32_bf16 v[216:219], a[128:131], v[96:99], v[216:219]\n\t"
      "s_waitcnt lgkmcnt(2)\n\t"
      "v_mfma_f32_16x16x32_bf16 v[216:219], a[132:135], v[100:103], v[216:219]\n\t"
      "v_mfma_f32_16x16x32_bf16 v[220:223], a[136:139], v[96:99], v[220:223]\n\t"
      "v_mfma_f32_16x16x32_bf16 v[220:223], a[140:143], v[100:103], v[220:223]\n\t"
      "v_mfma_f32_16x16x32_bf16 v[224:227], a[144:147], v[96:99], v[224:227]\n\t"
      "v_mfma_f32_16x16x32_bf16 v[224:227], a[148:151], v[100:103], v[224:227]\n\t"
      "s_waitcnt lgkmcnt(0)\n\t"
      "s_waitcnt vmcnt(12)\n\t"
      "v_mfma_f32_16x16x32_bf16 v[228:231], a[152:155], v[96:99], v[228:231]\n\t"
      "s_waitcnt vmcnt(8)\n\t"
      "s_barrier\n\t"
      "v_mfma_f32_16x16x32_bf16 v[228:231], a[156:159], v[100:103], v[228:231]\n\t"
      "ds_read_b128 v[96:99], v44 offset:49152\n\t"
      "ds_read_b128 v[100:103], v45 offset:49152\n\t"
      "v_mfma_f32_16x16x32_bf16 v[232:235], a[128:131], v[88:91], v[232:235]\n\t"
      "v_mfma_f32_16x16x32_bf16 v[232:235], a[132:135], v[92:95], v[232:235]\n\t"
      "v_mfma_f32_16x16x32_bf16 v[236:239], a[136:139], v[88:91], v[236:239]\n\t"
      "v_mfma_f32_16x16x32_bf16 v[236:239], a[140:143], v[92:95], v[236:239]\n\t"
      "v_mfma_f32_16x16x32_bf16 v[240:243], a[144:147], v[88:91], v[240:243]\n\t"
      "v_mfma_f32_16x16x32_bf16 v[240:243], a[148:151], v[92:95], v[240:243]\n\t"
      "v_mfma_f32_16x16x32_bf16 v[244:247], a[152:155], v[88:91], v[244:247]\n\t"
      "v_mfma_f32_16x16x32_bf16 v[244:247], a[156:159], v[92:95], v[244:247]\n\t"
      "ds_read_b128 v[56:59], v44 offset:0\n\t"
      "ds_read_b128 v[60:63], v45 offset:0\n\t"
      "s_waitcnt lgkmcnt(3)\n\t"
      "v_mfma_f32_16x16x32_bf16 v[136:139], a[160:163], v[96:99], v[136:139]\n\t"
      "ds_read_b128 a[224:227], v4 offset:32768\n\t"
      "s_waitcnt lgkmcnt(3)\n\t"
      "v_mfma_f32_16x16x32_bf16 v[136:139], a[164:167], v[100:103], v[136:139]\n\t"
      "ds_read_b128 v[88:91], v44 offset:51200\n\t"
      "ds_read_b128 v[92:95], v45 offset:51200\n\t"
      "v_mfma_f32_16x16x32_bf16 v[140:143], a[168:171], v[96:99], v[140:143]\n\t"
      "s_barrier\n\t"
      "v_mfma_f32_16x16x32_bf16 v[140:143], a[172:175], v[100:103], v[140:143]\n\t"
      "ds_read_b128 v[64:67], v44 offset:2048\n\t"
      "ds_read_b128 v[68:71], v45 offset:2048\n\t"
      "v_mfma_f32_16x16x32_bf16 v[144:147], a[176:179], v[96:99], v[144:147]\n\t"
      "ds_read_b128 a[228:231], v5 offset:32768\n\t"
      "ds_read_b128 a[232:235], v4 offset:34816\n\t"
      "v_mfma_f32_16x16x32_bf16 v[144:147], a[180:183], v[100:103], v[144:147]\n\t"
      "ds_read_b128 a[236:239], v5 offset:34816\n\t"
      "ds_read_b128 a[240:243], v4 offset:36864\n\t"
      "v_mfma_f32_16x16x32_bf16 v[148:151], a[184:187], v[96:99], v[148:151]\n\t"
      "ds_read_b128 a[244:247], v5 offset:36864\n\t"
      "ds_read_b128 a[248:251], v4 offset:38912\n\t"
      "v_mfma_f32_16x16x32_bf16 v[148:151], a[188:191], v[100:103], v[148:151]\n\t"
      "ds_read_b128 a[252:255], v5 offset:38912\n\t"
      "s_waitcnt lgkmcnt(13)\n\t"
      "v_mfma_f32_16x16x32_bf16 v[136:139], a[192:195], v[56:59], v[136:139]\n\t"
      "s_waitcnt lgkmcnt(12)\n\t"
      "v_mfma_f32_16x16x32_bf16 v[136:139], a[196:199], v[60:63], v[136:139]\n\t"
      "v_mfma_f32_16x16x32_bf16 v[140:143], a[200:203], v[56:59], v[140:143]\n\t"
      "v_mfma_f32_16x16x32_bf16 v[140:143], a[204:207], v[60:63], v[140:143]\n\t"
      "v_mfma_f32_16x16x32_bf16 v[144:147], a[208:211], v[56:59], v[144:147]\n\t"
      "v_mfma_f32_16x16x32_bf16 v[144:147], a[212:215], v[60:63], v[144:147]\n\t"
      "v_mfma_f32_16x16x32_bf16 v[148:151], a[216:219], v[56:59], v[148:151]\n\t"
      "v_mfma_f32_16x16x32_bf16 v[148:151], a[220:223], v[60:63], v[148:151]\n\t"
      "s_waitcnt lgkmcnt(10)\n\t"
      "v_mfma_f32_16x16x32_bf16 v[152:155], a[160:163], v[88:91], v[152:155]\n\t"
      "s_waitcnt lgkmcnt(0)\n\t"
      "s_add_u32 m0, s89, 32768\n\t"
      "v_mfma_f32_16x16x32_bf16 v[152:155], a[164:167], v[92:95], v[152:155]\n\t"
      "ds_read_b128 v[96:99], v44 offset:53248\n\t"
      "ds_read_b128 v[100:103], v45 offset:53248\n\t"
      "v_mfma_f32_16x16x32_bf16 v[156:159], a[168:171], v[88:91], v[156:159]\n\t"
      "buffer_load_dwordx4 v12, s[36:39], s60 offen offset:0 nt lds\n\t"
      "v_mfma_f32_16x16x32_bf16 v[156:159], a[172:175], v[92:95], v[156:159]\n\t"
      "ds_read_b128 v[56:59], v44 offset:4096\n\t"
      "ds_read_b128 v[60:63], v45 offset:4096\n\t"
      "v_mfma_f32_16x16x32_bf16 v[160:163], a[176:179], v[88:91], v[160:163]\n\t"
      "buffer_load_dwordx4 v13, s[36:39], s61 offen offset:1024 nt lds\n\t"
      "v_mul_f32 v136, s99, v136\n\t"
      "v_mfma_f32_16x16x32_bf16 v[160:163], a[180:183], v[92:95], v[160:163]\n\t"
      "v_mul_f32 v137, s99, v137\n\t"
      "v_mul_f32 v138, s99, v138\n\t"
      "v_mfma_f32_16x16x32_bf16 v[164:167], a[184:187], v[88:91], v[164:167]\n\t"
      "buffer_load_dwordx4 v12, s[36:39], s62 offen offset:2048 nt lds\n\t"
      "v_mul_f32 v139, s99, v139\n\t"
      "v_mfma_f32_16x16x32_bf16 v[164:167], a[188:191], v[92:95], v[164:167]\n\t"
      "v_cvt_pk_bf16_f32 v16, v136, v137\n\t"
      "v_cvt_pk_bf16_f32 v17, v138, v139\n\t"
      "v_mfma_f32_16x16x32_bf16 v[152:155], a[192:195], v[64:67], v[152:155]\n\t"
      "v_mul_f32 v140, s99, v140\n\t"
      "v_mul_f32 v141, s99, v141\n\t"
      "v_mfma_f32_16x16x32_bf16 v[152:155], a[196:199], v[68:71], v[152:155]\n\t"
      "buffer_load_dwordx4 v13, s[36:39], s63 offen offset:3072 nt lds\n\t"
      "v_mul_f32 v142, s99, v142\n\t"
      "v_mfma_f32_16x16x32_bf16 v[156:159], a[200:203], v[64:67], v[156:159]\n\t"
      "v_mul_f32 v143, s99, v143\n\t"
      "v_cvt_pk_bf16_f32 v18, v140, v141\n\t"
      "v_mfma_f32_16x16x32_bf16 v[156:159], a[204:207], v[68:71], v[156:159]\n\t"
      "v_cvt_pk_bf16_f32 v19, v142, v143\n\t"
      "ds_write_b128 v49, v[16:19] offset:0\n\t"
      "v_mfma_f32_16x16x32_bf16 v[160:163], a[208:211], v[64:67], v[160:163]\n\t"
      "v_mfma_f32_16x16x32_bf16 v[160:163], a[212:215], v[68:71], v[160:163]\n\t"
      "s_add_u32 m0, s89, 36864\n\t"
      "s_nop 0\n\t"
      "buffer_load_dwordx4 v12, s[36:39], s64 offen offset:0 nt lds\n\t"
      "v_mfma_f32_16x16x32_bf16 v[164:167], a[216:219], v[64:67], v[164:167]\n\t"
      "v_mul_f32 v144, s99, v144\n\t"
      "v_mul_f32 v145, s99, v145\n\t"
      "v_mfma_f32_16x16x32_bf16 v[164:167], a[220:223], v[68:71], v[164:167]\n\t"
      "v_mul_f32 v146, s99, v146\n\t"
      "v_mul_f32 v147, s99, v147\n\t"
      "s_waitcnt lgkmcnt(4)\n\t"
      "v_mfma_f32_16x16x32_bf16 v[168:171], a[160:163], v[96:99], v[168:171]\n\t"
      "buffer_load_dwordx4 v13, s[36:39], s65 offen offset:1024 nt lds\n\t"
      "v_cvt_pk_bf16_f32 v20, v144, v145\n\t"
      "s_waitcnt lgkmcnt(3)\n\t"
      "v_mfma_f32_16x16x32_bf16 v[168:171], a[164:167], v[100:103], v[168:171]\n\t"
      "ds_read_b128 v[88:91], v44 offset:55296\n\t"
      "ds_read_b128 v[92:95], v45 offset:55296\n\t"
      "v_mfma_f32_16x16x32_bf16 v[172:175], a[168:171], v[96:99], v[172:175]\n\t"
      "v_cvt_pk_bf16_f32 v21, v146, v147\n\t"
      "v_mul_f32 v148, s99, v148\n\t"
      "v_mfma_f32_16x16x32_bf16 v[172:175], a[172:175], v[100:103], v[172:175]\n\t"
      "ds_read_b128 v[64:67], v44 offset:6144\n\t"
      "ds_read_b128 v[68:71], v45 offset:6144\n\t"
      "v_mfma_f32_16x16x32_bf16 v[176:179], a[176:179], v[96:99], v[176:179]\n\t"
      "buffer_load_dwordx4 v12, s[36:39], s66 offen offset:2048 nt lds\n\t"
      "v_mul_f32 v149, s99, v149\n\t"
      "v_mfma_f32_16x16x32_bf16 v[176:179], a[180:183], v[100:103], v[176:179]\n\t"
      "v_mul_f32 v150, s99, v150\n\t"
      "v_mul_f32 v151, s99, v151\n\t"
      "v_mfma_f32_16x16x32_bf16 v[180:183], a[184:187], v[96:99], v[180:183]\n\t"
      "buffer_load_dwordx4 v13, s[36:39], s67 offen offset:3072 nt lds\n\t"
      "s_add_u32 s36, s36, 32768\n\t"
      "v_mfma_f32_16x16x32_bf16 v[180:183], a[188:191], v[100:103], v[180:183]\n\t"
      "s_addc_u32 s37, s37, 0\n\t"
      "v_cvt_pk_bf16_f32 v22, v148, v149\n\t"
      "s_waitcnt lgkmcnt(6)\n\t"
      "v_mfma_f32_16x16x32_bf16 v[168:171], a[192:195], v[56:59], v[168:171]\n\t"
      "v_cvt_pk_bf16_f32 v23, v150, v151\n\t"
      "ds_write_b128 v49, v[20:23] offset:1024\n\t"
      "s_waitcnt lgkmcnt(6)\n\t"
      "v_mfma_f32_16x16x32_bf16 v[168:171], a[196:199], v[60:63], v[168:171]\n\t"
      "v_mfma_f32_16x16x32_bf16 v[172:175], a[200:203], v[56:59], v[172:175]\n\t"
      "v_mul_f32 v152, s99, v152\n\t"
      "v_mul_f32 v153, s99, v153\n\t"
      "v_mfma_f32_16x16x32_bf16 v[172:175], a[204:207], v[60:63], v[172:175]\n\t"
      "v_mul_f32 v154, s99, v154\n\t"
      "v_mul_f32 v155, s99, v155\n\t"
      "v_mfma_f32_16x16x32_bf16 v[176:179], a[208:211], v[56:59], v[176:179]\n\t"
      "v_cvt_pk_bf16_f32 v24, v152, v153\n\t"
      "v_cvt_pk_bf16_f32 v25, v154, v155\n\t"
      "v_mfma_f32_16x16x32_bf16 v[176:179], a[212:215], v[60:63], v[176:179]\n\t"
      "v_mul_f32 v156, s99, v156\n\t"
      "v_mul_f32 v157, s99, v157\n\t"
      "v_mfma_f32_16x16x32_bf16 v[180:183], a[216:219], v[56:59], v[180:183]\n\t"
      "v_mul_f32 v158, s99, v158\n\t"
      "v_mul_f32 v159, s99, v159\n\t"
      "v_mfma_f32_16x16x32_bf16 v[180:183], a[220:223], v[60:63], v[180:183]\n\t"
      "v_cvt_pk_bf16_f32 v26, v156, v157\n\t"
      "v_cvt_pk_bf16_f32 v27, v158, v159\n\t"
      "s_waitcnt lgkmcnt(4)\n\t"
      "v_mfma_f32_16x16x32_bf16 v[184:187], a[160:163], v[88:91], v[184:187]\n\t"
      "ds_write_b128 v49, v[24:27] offset:8192\n\t"
      "s_waitcnt lgkmcnt(4)\n\t"
      "v_mfma_f32_16x16x32_bf16 v[184:187], a[164:167], v[92:95], v[184:187]\n\t"
      "ds_read_b128 v[96:99], v44 offset:57344\n\t"
      "v_mfma_f32_16x16x32_bf16 v[188:191], a[168:171], v[88:91], v[188:191]\n\t"
      "ds_read_b128 v[100:103], v45 offset:57344\n\t"
      "v_mul_f32 v160, s99, v160\n\t"
      "v_mfma_f32_16x16x32_bf16 v[188:191], a[172:175], v[92:95], v[188:191]\n\t"
      "ds_read_b128 v[56:59], v44 offset:8192\n\t"
      "ds_read_b128 v[60:63], v45 offset:8192\n\t"
      "v_mfma_f32_16x16x32_bf16 v[192:195], a[176:179], v[88:91], v[192:195]\n\t"
      "v_mul_f32 v161, s99, v161\n\t"
      "v_mul_f32 v162, s99, v162\n\t"
      "v_mfma_f32_16x16x32_bf16 v[192:195], a[180:183], v[92:95], v[192:195]\n\t"
      "v_mul_f32 v163, s99, v163\n\t"
      "v_cvt_pk_bf16_f32 v28, v160, v161\n\t"
      "v_mfma_f32_16x16x32_bf16 v[196:199], a[184:187], v[88:91], v[196:199]\n\t"
      "v_cvt_pk_bf16_f32 v29, v162, v163\n\t"
      "v_mul_f32 v164, s99, v164\n\t"
      "v_mfma_f32_16x16x32_bf16 v[196:199], a[188:191], v[92:95], v[196:199]\n\t"
      "v_mul_f32 v165, s99, v165\n\t"
      "v_mul_f32 v166, s99, v166\n\t"
      "s_waitcnt lgkmcnt(7)\n\t"
      "v_mfma_f32_16x16x32_bf16 v[184:187], a[192:195], v[64:67], v[184:187]\n\t"
      "v_mul_f32 v167, s99, v167\n\t"
      "v_cvt_pk_bf16_f32 v30, v164, v165\n\t"
      "s_waitcnt lgkmcnt(6)\n\t"
      "v_mfma_f32_16x16x32_bf16 v[184:187], a[196:199], v[68:71], v[184:187]\n\t"
      "v_cvt_pk_bf16_f32 v31, v166, v167\n\t"
      "ds_write_b128 v49, v[28:31] offset:9216\n\t"
      "v_mfma_f32_16x16x32_bf16 v[188:191], a[200:203], v[64:67], v[188:191]\n\t"
      "v_mfma_f32_16x16x32_bf16 v[188:191], a[204:207], v[68:71], v[188:191]\n\t"
      "v_mul_f32 v168, s99, v168\n\t"
      "v_mul_f32 v169, s99, v169\n\t"
      "v_mfma_f32_16x16x32_bf16 v[192:195], a[208:211], v[64:67], v[192:195]\n\t"
      "v_mul_f32 v170, s99, v170\n\t"
      "v_mul_f32 v171, s99, v171\n\t"
      "v_mfma_f32_16x16x32_bf16 v[192:195], a[212:215], v[68:71], v[192:195]\n\t"
      "v_cvt_pk_bf16_f32 v16, v168, v169\n\t"
      "v_cvt_pk_bf16_f32 v17, v170, v171\n\t"
      "v_mfma_f32_16x16x32_bf16 v[196:199], a[216:219], v[64:67], v[196:199]\n\t"
      "s_waitcnt vmcnt(8)\n\t"
      "ds_read_b128 v[104:107], v4 offset:0\n\t"
      "v_mfma_f32_16x16x32_bf16 v[196:199], a[220:223], v[68:71], v[196:199]\n\t"
      "ds_read_b128 v[108:111], v5 offset:0\n\t"
      "ds_read_b128 v[112:115], v4 offset:2048\n\t"
      "s_waitcnt lgkmcnt(7)\n\t"
      "v_mfma_f32_16x16x32_bf16 v[200:203], a[160:163], v[96:99], v[200:203]\n\t"
      "ds_read_b128 v[116:119], v5 offset:2048\n\t"
      "ds_read_b128 v[120:123], v4 offset:4096\n\t"
      "s_waitcnt lgkmcnt(8)\n\t"
      "v_mfma_f32_16x16x32_bf16 v[200:203], a[164:167], v[100:103], v[200:203]\n\t"
      "ds_read_b128 v[88:91], v44 offset:59392\n\t"
      "ds_read_b128 v[92:95], v45 offset:59392\n\t"
      "v_mfma_f32_16x16x32_bf16 v[204:207], a[168:171], v[96:99], v[204:207]\n\t"
      "ds_read_b128 v[124:127], v5 offset:4096\n\t"
      "ds_read_b128 v[128:131], v4 offset:6144\n\t"
      "v_mfma_f32_16x16x32_bf16 v[204:207], a[172:175], v[100:103], v[204:207]\n\t"
      "ds_read_b128 v[64:67], v44 offset:10240\n\t"
      "ds_read_b128 v[68:71], v45 offset:10240\n\t"
      "v_mfma_f32_16x16x32_bf16 v[208:211], a[176:179], v[96:99], v[208:211]\n\t"
      "ds_read_b128 v[132:135], v5 offset:6144\n\t"
      "v_mul_f32 v172, s99, v172\n\t"
      "v_mfma_f32_16x16x32_bf16 v[208:211], a[180:183], v[100:103], v[208:211]\n\t"
      "v_mul_f32 v173, s99, v173\n\t"
      "v_mul_f32 v174, s99, v174\n\t"
      "v_mfma_f32_16x16x32_bf16 v[212:215], a[184:187], v[96:99], v[212:215]\n\t"
      "v_mul_f32 v175, s99, v175\n\t"
      "v_cvt_pk_bf16_f32 v18, v172, v173\n\t"
      "v_mfma_f32_16x16x32_bf16 v[212:215], a[188:191], v[100:103], v[212:215]\n\t"
      "v_cvt_pk_bf16_f32 v19, v174, v175\n\t"
      "ds_write_b128 v49, v[16:19] offset:16384\n\t"
      "s_waitcnt lgkmcnt(15)\n\t"
      "v_mfma_f32_16x16x32_bf16 v[200:203], a[192:195], v[56:59], v[200:203]\n\t"
      "s_waitcnt lgkmcnt(14)\n\t"
      "v_mfma_f32_16x16x32_bf16 v[200:203], a[196:199], v[60:63], v[200:203]\n\t"
      "v_mul_f32 v176, s99, v176\n\t"
      "v_mul_f32 v177, s99, v177\n\t"
      "v_mfma_f32_16x16x32_bf16 v[204:207], a[200:203], v[56:59], v[204:207]\n\t"
      "v_mul_f32 v178, s99, v178\n\t"
      "v_mul_f32 v179, s99, v179\n\t"
      "v_mfma_f32_16x16x32_bf16 v[204:207], a[204:207], v[60:63], v[204:207]\n\t"
      "v_cvt_pk_bf16_f32 v20, v176, v177\n\t"
      "v_cvt_pk_bf16_f32 v21, v178, v179\n\t"
      "v_mfma_f32_16x16x32_bf16 v[208:211], a[208:211], v[56:59], v[208:211]\n\t"
      "s_waitcnt lgkmcnt(1)\n\t"
      "s_add_u32 m0, s89, 0\n\t"
      "v_mfma_f32_16x16x32_bf16 v[208:211], a[212:215], v[60:63], v[208:211]\n\t"
      "buffer_load_dwordx4 v12, s[36:39], s60 offen offset:0 nt lds\n\t"
      "v_mul_f32 v180, s99, v180\n\t"
      "v_mfma_f32_16x16x32_bf16 v[212:215], a[216:219], v[56:59], v[212:215]\n\t"
      "v_mul_f32 v181, s99, v181\n\t"
      "v_mul_f32 v182, s99, v182\n\t"
      "v_mfma_f32_16x16x32_bf16 v[212:215], a[220:223], v[60:63], v[212:215]\n\t"
      "buffer_load_dwordx4 v13, s[36:39], s61 offen offset:1024 nt lds\n\t"
      "v_mul_f32 v183, s99, v183\n\t"
      "v_mfma_f32_16x16x32_bf16 v[216:219], a[160:163], v[88:91], v[216:219]\n\t"
      "v_cvt_pk_bf16_f32 v22, v180, v181\n\t"
      "v_cvt_pk_bf16_f32 v23, v182, v183\n\t"
      "v_mfma_f32_16x16x32_bf16 v[216:219], a[164:167], v[92:95], v[216:219]\n\t"
      "ds_read_b128 v[96:99], v44 offset:61440\n\t"
      "ds_read_b128 v[100:103], v45 offset:61440\n\t"
      "v_mfma_f32_16x16x32_bf16 v[220:223], a[168:171], v[88:91], v[220:223]\n\t"
      "buffer_load_dwordx4 v12, s[36:39], s62 offen offset:2048 nt lds\n\t"
      "ds_write_b128 v49, v[20:23] offset:17408\n\t"
      "v_mfma_f32_16x16x32_bf16 v[220:223], a[172:175], v[92:95], v[220:223]\n\t"
      "v_mfma_f32_16x16x32_bf16 v[224:227], a[176:179], v[88:91], v[224:227]\n\t"
      "ds_read_b128 v[56:59], v44 offset:12288\n\t"
      "ds_read_b128 v[60:63], v45 offset:12288\n\t"
      "v_mfma_f32_16x16x32_bf16 v[224:227], a[180:183], v[92:95], v[224:227]\n\t"
      "buffer_load_dwordx4 v13, s[36:39], s63 offen offset:3072 nt lds\n\t"
      "v_mul_f32 v184, s99, v184\n\t"
      "v_mfma_f32_16x16x32_bf16 v[228:231], a[184:187], v[88:91], v[228:231]\n\t"
      "v_mul_f32 v185, s99, v185\n\t"
      "v_mul_f32 v186, s99, v186\n\t"
      "v_mfma_f32_16x16x32_bf16 v[228:231], a[188:191], v[92:95], v[228:231]\n\t"
      "v_mul_f32 v187, s99, v187\n\t"
      "v_cvt_pk_bf16_f32 v24, v184, v185\n\t"
      "v_mfma_f32_16x16x32_bf16 v[216:219], a[192:195], v[64:67], v[216:219]\n\t"
      "s_add_u32 m0, s89, 4096\n\t"
      "s_nop 0\n\t"
      "buffer_load_dwordx4 v12, s[36:39], s64 offen offset:0 nt lds\n\t"
      "v_mfma_f32_16x16x32_bf16 v[216:219], a[196:199], v[68:71], v[216:219]\n\t"
      "v_cvt_pk_bf16_f32 v25, v186, v187\n\t"
      "v_mul_f32 v188, s99, v188\n\t"
      "v_mfma_f32_16x16x32_bf16 v[220:223], a[200:203], v[64:67], v[220:223]\n\t"
      "v_mul_f32 v189, s99, v189\n\t"
      "v_mul_f32 v190, s99, v190\n\t"
      "v_mfma_f32_16x16x32_bf16 v[220:223], a[204:207], v[68:71], v[220:223]\n\t"
      "v_mul_f32 v191, s99, v191\n\t"
      "v_cvt_pk_bf16_f32 v26, v188, v189\n\t"
      "v_mfma_f32_16x16x32_bf16 v[224:227], a[208:211], v[64:67], v[224:227]\n\t"
      "buffer_load_dwordx4 v13, s[36:39], s65 offen offset:1024 nt lds\n\t"
      "v_cvt_pk_bf16_f32 v27, v190, v191\n\t"
      "v_mfma_f32_16x16x32_bf16 v[224:227], a[212:215], v[68:71], v[224:227]\n\t"
      "ds_write_b128 v49, v[24:27] offset:24576\n\t"
      "v_mfma_f32_16x16x32_bf16 v[228:231], a[216:219], v[64:67], v[228:231]\n\t"
      "v_mul_f32 v192, s99, v192\n\t"
      "v_mfma_f32_16x16x32_bf16 v[228:231], a[220:223], v[68:71], v[228:231]\n\t"
      "buffer_load_dwordx4 v12, s[36:39], s66 offen offset:2048 nt lds\n\t"
      "v_mul_f32 v193, s99, v193\n\t"
      "s_waitcnt lgkmcnt(5)\n\t"
      "v_mfma_f32_16x16x32_bf16 v[232:235], a[160:163], v[96:99], v[232:235]\n\t"
      "v_mul_f32 v194, s99, v194\n\t"
      "v_mul_f32 v195, s99, v195\n\t"
      "s_waitcnt lgkmcnt(4)\n\t"
      "v_mfma_f32_16x16x32_bf16 v[232:235], a[164:167], v[100:103], v[232:235]\n\t"
      "v_cvt_pk_bf16_f32 v28, v192, v193\n\t"
      "v_cvt_pk_bf16_f32 v29, v194, v195\n\t"
      "v_mfma_f32_16x16x32_bf16 v[236:239], a[168:171], v[96:99], v[236:239]\n\t"
      "buffer_load_dwordx4 v13, s[36:39], s67 offen offset:3072 nt lds\n\t"
      "s_add_u32 s36, s36, 32768\n\t"
      "v_mfma_f32_16x16x32_bf16 v[236:239], a[172:175], v[100:103], v[236:239]\n\t"
      "s_addc_u32 s37, s37, 0\n\t"
      "v_mul_f32 v196, s99, v196\n\t"
      "v_mfma_f32_16x16x32_bf16 v[240:243], a[176:179], v[96:99], v[240:243]\n\t"
      "v_mul_f32 v197, s99, v197\n\t"
      "v_mul_f32 v198, s99, v198\n\t"
      "v_mfma_f32_16x16x32_bf16 v[240:243], a[180:183], v[100:103], v[240:243]\n\t"
      "v_mul_f32 v199, s99, v199\n\t"
      "v_cvt_pk_bf16_f32 v30, v196, v197\n\t"
      "v_mfma_f32_16x16x32_bf16 v[244:247], a[184:187], v[96:99], v[244:247]\n\t"
      "v_cvt_pk_bf16_f32 v31, v198, v199\n\t"
      "ds_write_b128 v49, v[28:31] offset:25600\n\t"
      "v_mfma_f32_16x16x32_bf16 v[244:247], a[188:191], v[100:103], v[244:247]\n\t"
      "s_waitcnt lgkmcnt(3)\n\t"
      "v_mfma_f32_16x16x32_bf16 v[232:235], a[192:195], v[56:59], v[232:235]\n\t"
      "v_mul_f32 v200, s99, v200\n\t"
      "v_mul_f32 v201, s99, v201\n\t"
      "s_waitcnt lgkmcnt(2)\n\t"
      "v_mfma_f32_16x16x32_bf16 v[232:235], a[196:199], v[60:63], v[232:235]\n\t"
      "v_mul_f32 v202, s99, v202\n\t"
      "v_mul_f32 v203, s99, v203\n\t"
      "v_mfma_f32_16x16x32_bf16 v[236:239], a[200:203], v[56:59], v[236:239]\n\t"
      "v_cvt_pk_bf16_f32 v16, v200, v201\n\t"
      "v_cvt_pk_bf16_f32 v17, v202, v203\n\t"
      "v_mfma_f32_16x16x32_bf16 v[236:239], a[204:207], v[60:63], v[236:239]\n\t"
      "v_mul_f32 v204, s99, v204\n\t"
      "v_mul_f32 v205, s99, v205\n\t"
      "v_mfma_f32_16x16x32_bf16 v[240:243], a[208:211], v[56:59], v[240:243]\n\t"
      "v_mul_f32 v206, s99, v206\n\t"
      "v_mul_f32 v207, s99, v207\n\t"
      "v_mfma_f32_16x16x32_bf16 v[240:243], a[212:215], v[60:63], v[240:243]\n\t"
      "v_cvt_pk_bf16_f32 v18, v204, v205\n\t"
      "v_cvt_pk_bf16_f32 v19, v206, v207\n\t"
      "v_mfma_f32_16x16x32_bf16 v[244:247], a[216:219], v[56:59], v[244:247]\n\t"
      "ds_write_b128 v49, v[16:19] offset:49152\n\t"
      "v_mfma_f32_16x16x32_bf16 v[244:247], a[220:223], v[60:63], v[244:247]\n\t"
      "v_mul_f32 v208, s99, v208\n\t"
      "v_mul_f32 v209, s99, v209\n\t"
      "v_mul_f32 v210, s99, v210\n\t"
      "v_mul_f32 v211, s99, v211\n\t"
      "v_cvt_pk_bf16_f32 v20, v208, v209\n\t"
      "v_cvt_pk_bf16_f32 v21, v210, v211\n\t"
      "v_mul_f32 v212, s99, v212\n\t"
      "v_mul_f32 v213, s99, v213\n\t"
      "v_mul_f32 v214, s99, v214\n\t"
      "v_mul_f32 v215, s99, v215\n\t"
      "v_cvt_pk_bf16_f32 v22, v212, v213\n\t"
      "v_cvt_pk_bf16_f32 v23, v214, v215\n\t"
      "ds_write_b128 v49, v[20:23] offset:50176\n\t"
      "v_mul_f32 v216, s99, v216\n\t"
      "v_mul_f32 v217, s99, v217\n\t"
      "v_mul_f32 v218, s99, v218\n\t"
      "v_mul_f32 v219, s99, v219\n\t"
      "v_cvt_pk_bf16_f32 v24, v216, v217\n\t"
      "v_cvt_pk_bf16_f32 v25, v218, v219\n\t"
      "v_mul_f32 v220, s99, v220\n\t"
      "v_mul_f32 v221, s99, v221\n\t"
      "v_mul_f32 v222, s99, v222\n\t"
      "v_mul_f32 v223, s99, v223\n\t"
      "v_cvt_pk_bf16_f32 v26, v220, v221\n\t"
      "v_cvt_pk_bf16_f32 v27, v222, v223\n\t"
      "ds_write_b128 v49, v[24:27] offset:57344\n\t"
      "v_mul_f32 v224, s99, v224\n\t"
      "v_mul_f32 v225, s99, v225\n\t"
      "v_mul_f32 v226, s99, v226\n\t"
      "v_mul_f32 v227, s99, v227\n\t"
      "v_cvt_pk_bf16_f32 v28, v224, v225\n\t"
      "v_cvt_pk_bf16_f32 v29, v226, v227\n\t"
      "v_mul_f32 v228, s99, v228\n\t"
      "v_mul_f32 v229, s99, v229\n\t"
      "v_mul_f32 v230, s99, v230\n\t"
      "v_mul_f32 v231, s99, v231\n\t"
      "v_cvt_pk_bf16_f32 v30, v228, v229\n\t"
      "v_cvt_pk_bf16_f32 v31, v230, v231\n\t"
      "ds_write_b128 v49, v[28:31] offset:58368\n\t"
      "v_mul_f32 v232, s99, v232\n\t"
      "v_mul_f32 v233, s99, v233\n\t"
      "v_mul_f32 v234, s99, v234\n\t"
      "v_mul_f32 v235, s99, v235\n\t"
      "v_cvt_pk_bf16_f32 v16, v232, v233\n\t"
      "v_cvt_pk_bf16_f32 v17, v234, v235\n\t"
      "v_mul_f32 v236, s99, v236\n\t"
      "v_mul_f32 v237, s99, v237\n\t"
      "v_mul_f32 v238, s99, v238\n\t"
      "v_mul_f32 v239, s99, v239\n\t"
      "v_cvt_pk_bf16_f32 v18, v236, v237\n\t"
      "v_cvt_pk_bf16_f32 v19, v238, v239\n\t"
      "ds_write_b128 v51, v[16:19] offset:0\n\t"
      "v_mul_f32 v240, s99, v240\n\t"
      "v_mul_f32 v241, s99, v241\n\t"
      "v_mul_f32 v242, s99, v242\n\t"
      "v_mul_f32 v243, s99, v243\n\t"
      "v_cvt_pk_bf16_f32 v20, v240, v241\n\t"
      "v_cvt_pk_bf16_f32 v21, v242, v243\n\t"
      "v_mul_f32 v244, s99, v244\n\t"
      "v_mul_f32 v245, s99, v245\n\t"
      "v_mul_f32 v246, s99, v246\n\t"
      "v_mul_f32 v247, s99, v247\n\t"
      "v_cvt_pk_bf16_f32 v22, v244, v245\n\t"
      "v_cvt_pk_bf16_f32 v23, v246, v247\n\t"
      "ds_write_b128 v51, v[20:23] offset:1024\n\t"
      "s_waitcnt lgkmcnt(0)\n\t"
      "s_barrier\n\t"
      "ds_read_b128 a[0:3], v50 offset:0\n\t"
      "ds_read_b128 a[4:7], v50 offset:1024\n\t"
      "ds_read_b128 a[8:11], v50 offset:2048\n\t"
      "ds_read_b128 a[12:15], v50 offset:3072\n\t"
      "ds_read_b128 a[16:19], v50 offset:4096\n\t"
      "ds_read_b128 a[20:23], v50 offset:5120\n\t"
      "ds_read_b128 a[24:27], v50 offset:6144\n\t"
      "ds_read_b128 a[28:31], v50 offset:7168\n\t"
      "ds_read_b128 a[32:35], v50 offset:8192\n\t"
      "ds_read_b128 a[36:39], v50 offset:9216\n\t"
      "ds_read_b128 a[40:43], v50 offset:10240\n\t"
      "ds_read_b128 a[44:47], v50 offset:11264\n\t"
      "s_waitcnt lgkmcnt(11)\n\t"
      "v_mfma_f32_16x16x32_bf16 v[16:19], a[224:227], a[0:3], 0\n\t"
      "ds_read_b128 a[48:51], v50 offset:12288\n\t"
      "ds_read_b128 a[52:55], v50 offset:13312\n\t"
      "s_waitcnt lgkmcnt(12)\n\t"
      "v_mfma_f32_16x16x32_bf16 v[16:19], a[228:231], a[4:7], v[16:19]\n\t"
      "ds_read_b128 a[56:59], v50 offset:14336\n\t"
      "ds_read_b128 a[60:63], v50 offset:15360\n\t"
      "s_waitcnt lgkmcnt(13)\n\t"
      "v_mfma_f32_16x16x32_bf16 v[16:19], a[232:235], a[8:11], v[16:19]\n\t"
      "s_mov_b32 s99, 0\n\t"
      "s_mov_b32 s44, s22\n\t"
      "s_waitcnt lgkmcnt(12)\n\t"
      "v_mfma_f32_16x16x32_bf16 v[16:19], a[236:239], a[12:15], v[16:19]\n\t"
      "s_and_b32 s45, s23, 0xffff\n\t"
      "s_mov_b32 s47, 0x20000\n\t"
      "s_waitcnt lgkmcnt(11)\n\t"
      "v_mfma_f32_16x16x32_bf16 v[16:19], a[240:243], a[16:19], v[16:19]\n\t"
      "ds_read_b128 a[64:67], v50 offset:16384\n\t"
      "ds_read_b128 a[68:71], v50 offset:17408\n\t"
      "s_waitcnt lgkmcnt(12)\n\t"
      "v_mfma_f32_16x16x32_bf16 v[16:19], a[244:247], a[20:23], v[16:19]\n\t"
      "ds_read_b128 a[72:75], v50 offset:18432\n\t"
      "ds_read_b128 a[76:79], v50 offset:19456\n\t"
      "s_waitcnt lgkmcnt(13)\n\t"
      "v_mfma_f32_16x16x32_bf16 v[16:19], a[248:251], a[24:27], v[16:19]\n\t"
      "ds_read_b128 a[80:83], v50 offset:20480\n\t"
      "ds_read_b128 a[84:87], v50 offset:21504\n\t"
      "s_waitcnt lgkmcnt(14)\n\t"
      "v_mfma_f32_16x16x32_bf16 v[16:19], a[252:255], a[28:31], v[16:19]\n\t"
      "ds_read_b128 a[88:91], v50 offset:22528\n\t"
      "ds_read_b128 a[92:95], v50 offset:23552\n\t"
      "s_waitcnt lgkmcnt(15)\n\t"
      "v_mfma_f32_16x16x32_bf16 v[20:23], a[224:227], a[32:35], 0\n\t"
      "s_cmp_eq_u32 s88, 0\n\t"
      "s_cselect_b32 s28, 2048, 0\n\t"
      "s_waitcnt lgkmcnt(14)\n\t"
      "v_mfma_f32_16x16x32_bf16 v[20:23], a[228:231], a[36:39], v[20:23]\n\t"
      "s_or_b32 s94, s88, s7\n\t"
      "s_cmp_eq_u32 s94, 0\n\t"
      "s_waitcnt lgkmcnt(13)\n\t"
      "v_mfma_f32_16x16x32_bf16 v[20:23], a[232:235], a[40:43], v[20:23]\n\t"
      "s_cselect_b32 s29, 2048, 0\n\t"
      "s_lshl_b32 s94, s7, 3\n\t"
      "s_waitcnt lgkmcnt(12)\n\t"
      "v_mfma_f32_16x16x32_bf16 v[20:23], a[236:239], a[44:47], v[20:23]\n\t"
      "v_max3_f32 v72, v16, v17, v18\n\t"
      "v_max_f32 v72, v72, v19\n\t"
      "s_waitcnt lgkmcnt(11)\n\t"
      "v_mfma_f32_16x16x32_bf16 v[20:23], a[240:243], a[48:51], v[20:23]\n\t"
      "ds_read_b128 a[96:99], v50 offset:24576\n\t"
      "ds_read_b128 a[100:103], v50 offset:25600\n\t"
      "s_waitcnt lgkmcnt(12)\n\t"
      "v_mfma_f32_16x16x32_bf16 v[20:23], a[244:247], a[52:55], v[20:23]\n\t"
      "ds_read_b128 a[104:107], v50 offset:26624\n\t"
      "ds_read_b128 a[108:111], v50 offset:27648\n\t"
      "s_waitcnt lgkmcnt(13)\n\t"
      "v_mfma_f32_16x16x32_bf16 v[20:23], a[248:251], a[56:59], v[20:23]\n\t"
      "ds_read_b128 a[112:115], v50 offset:28672\n\t"
      "ds_read_b128 a[116:119], v50 offset:29696\n\t"
      "s_waitcnt lgkmcnt(14)\n\t"
      "v_mfma_f32_16x16x32_bf16 v[20:23], a[252:255], a[60:63], v[20:23]\n\t"
      "ds_read_b128 a[120:123], v50 offset:30720\n\t"
      "ds_read_b128 a[124:127], v50 offset:31744\n\t"
      "s_waitcnt lgkmcnt(15)\n\t"
      "v_mfma_f32_16x16x32_bf16 v[24:27], a[224:227], a[64:67], 0\n\t"
      "v_mov_b32 v248, v72\n\t"
      "s_nop 1\n\t"
      "v_permlane32_swap_b32 v72, v248\n\t"
      "s_waitcnt lgkmcnt(14)\n\t"
      "v_mfma_f32_16x16x32_bf16 v[24:27], a[228:231], a[68:71], v[24:27]\n\t"
      "v_max_f32 v72, v72, v248\n\t"
      "v_mov_b32 v248, v72\n\t"
      "s_waitcnt lgkmcnt(13)\n\t"
      "v_mfma_f32_16x16x32_bf16 v[24:27], a[232:235], a[72:75], v[24:27]\n\t"
      "v_permlane16_swap_b32 v72, v248\n\t"
      "v_max_f32 v72, v72, v248\n\t"
      "s_waitcnt lgkmcnt(12)\n\t"
      "v_mfma_f32_16x16x32_bf16 v[24:27], a[236:239], a[76:79], v[24:27]\n\t"
      "ds_write_b32 v79, v72 offset:0\n\t"
      "v_max3_f32 v73, v20, v21, v22\n\t"
      "s_waitcnt lgkmcnt(12)\n\t"
      "v_mfma_f32_16x16x32_bf16 v[24:27], a[240:243], a[80:83], v[24:27]\n\t"
      "ds_read_b128 a[128:131], v50 offset:49152\n\t"
      "ds_read_b128 a[132:135], v50 offset:50176\n\t"
      "s_waitcnt lgkmcnt(13)\n\t"
      "v_mfma_f32_16x16x32_bf16 v[24:27], a[244:247], a[84:87], v[24:27]\n\t"
      "ds_read_b128 a[136:139], v50 offset:51200\n\t"
      "ds_read_b128 a[140:143], v50 offset:52224\n\t"
      "s_waitcnt lgkmcnt(14)\n\t"
      "v_mfma_f32_16x16x32_bf16 v[24:27], a[248:251], a[88:91], v[24:27]\n\t"
      "s_waitcnt lgkmcnt(5)\n\t"
      "s_barrier\n\t"
      "v_mfma_f32_16x16x32_bf16 v[24:27], a[252:255], a[92:95], v[24:27]\n\t"
      "s_add_u32 m0, s89, 65536\n\t"
      "s_nop 0\n\t"
      "buffer_load_dwordx4 v14, s[40:43], s68 offen offset:0 nt lds\n\t"
      "v_mfma_f32_16x16x32_bf16 v[28:31], a[224:227], a[96:99], 0\n\t"
      "ds_read_b128 a[144:147], v50 offset:53248\n\t"
      "ds_read_b128 a[148:151], v50 offset:54272\n\t"
      "v_mfma_f32_16x16x32_bf16 v[28:31], a[228:231], a[100:103], v[28:31]\n\t"
      "ds_read_b128 a[152:155], v50 offset:55296\n\t"
      "ds_read_b128 a[156:159], v50 offset:56320\n\t"
      "v_mfma_f32_16x16x32_bf16 v[28:31], a[232:235], a[104:107], v[28:31]\n\t"
      "v_max_f32 v73, v73, v23\n\t"
      "v_mov_b32 v249, v73\n\t"
      "v_mfma_f32_16x16x32_bf16 v[28:31], a[236:239], a[108:111], v[28:31]\n\t"
      "buffer_load_dwordx4 v14, s[40:43], s69 offen offset:1024 nt lds\n\t"
      "v_permlane32_swap_b32 v73, v249\n\t"
      "v_mfma_f32_16x16x32_bf16 v[28:31], a[240:243], a[112:115], v[28:31]\n\t"
      "ds_read_b128 a[160:163], v50 offset:57344\n\t"
      "ds_read_b128 a[164:167], v50 offset:58368\n\t"
      "v_mfma_f32_16x16x32_bf16 v[28:31], a[244:247], a[116:119], v[28:31]\n\t"
      "ds_read_b128 a[168:171], v50 offset:59392\n\t"
      "ds_read_b128 a[172:175], v50 offset:60416\n\t"
      "v_mfma_f32_16x16x32_bf16 v[28:31], a[248:251], a[120:123], v[28:31]\n\t"
      "ds_read_b128 a[176:179], v50 offset:61440\n\t"
      "ds_read_b128 a[180:183], v50 offset:62464\n\t"
      "v_mfma_f32_16x16x32_bf16 v[28:31], a[252:255], a[124:127], v[28:31]\n\t"
      "buffer_load_dwordx4 v14, s[40:43], s70 offen offset:2048 nt lds\n\t"
      "ds_read_b128 a[184:187], v50 offset:63488\n\t"
      "s_waitcnt lgkmcnt(14)\n\t"
      "v_mfma_f32_16x16x32_bf16 v[32:35], a[224:227], a[128:131], 0\n\t"
      "ds_read_b128 a[188:191], v50 offset:64512\n\t"
      "v_max_f32 v73, v73, v249\n\t"
      "s_waitcnt lgkmcnt(14)\n\t"
      "v_mfma_f32_16x16x32_bf16 v[32:35], a[228:231], a[132:135], v[32:35]\n\t"
      "v_mov_b32 v249, v73\n\t"
      "s_nop 1\n\t"
      "v_permlane16_swap_b32 v73, v249\n\t"
      "s_waitcnt lgkmcnt(13)\n\t"
      "v_mfma_f32_16x16x32_bf16 v[32:35], a[232:235], a[136:139], v[32:35]\n\t"
      "v_max_f32 v73, v73, v249\n\t"
      "ds_write_b32 v79, v73 offset:256\n\t"
      "s_waitcnt lgkmcnt(13)\n\t"
      "v_mfma_f32_16x16x32_bf16 v[32:35], a[236:239], a[140:143], v[32:35]\n\t"
      "buffer_load_dwordx4 v14, s[40:43], s71 offen offset:3072 nt lds\n\t"
      "v_max3_f32 v74, v24, v25, v26\n\t"
      "s_waitcnt lgkmcnt(12)\n\t"
      "v_mfma_f32_16x16x32_bf16 v[32:35], a[240:243], a[144:147], v[32:35]\n\t"
      "ds_read_b128 a[192:195], v10 offset:0\n\t"
      "ds_read_b128 a[196:199], v10 offset:1024\n\t"
      "s_waitcnt lgkmcnt(13)\n\t"
      "v_mfma_f32_16x16x32_bf16 v[32:35], a[244:247], a[148:151], v[32:35]\n\t"
      "ds_read_b128 a[200:203], v10 offset:2048\n\t"
      "ds_read_b128 a[204:207], v10 offset:3072\n\t"
      "s_waitcnt lgkmcnt(14)\n\t"
      "v_mfma_f32_16x16x32_bf16 v[32:35], a[248:251], a[152:155], v[32:35]\n\t"
      "ds_read_b128 a[208:211], v10 offset:4096\n\t"
      "ds_read_b128 a[212:215], v10 offset:5120\n\t"
      "s_waitcnt lgkmcnt(15)\n\t"
      "v_mfma_f32_16x16x32_bf16 v[32:35], a[252:255], a[156:159], v[32:35]\n\t"
      "s_add_u32 m0, s89, 69632\n\t"
      "s_nop 0\n\t"
      "buffer_load_dwordx4 v14, s[40:43], s72 offen offset:0 nt lds\n\t"
      "s_waitcnt lgkmcnt(14)\n\t"
      "v_mfma_f32_16x16x32_bf16 v[36:39], a[224:227], a[160:163], 0\n\t"
      "ds_read_b128 a[216:219], v10 offset:6144\n\t"
      "ds_read_b128 a[220:223], v10 offset:7168\n\t"
      "s_waitcnt lgkmcnt(15)\n\t"
      "v_mfma_f32_16x16x32_bf16 v[36:39], a[228:231], a[164:167], v[36:39]\n\t"
      "v_max_f32 v74, v74, v27\n\t"
      "v_mov_b32 v248, v74\n\t"
      "s_waitcnt lgkmcnt(14)\n\t"
      "v_mfma_f32_16x16x32_bf16 v[36:39], a[232:235], a[168:171], v[36:39]\n\t"
      "v_permlane32_swap_b32 v74, v248\n\t"
      "v_max_f32 v74, v74, v248\n\t"
      "s_waitcnt lgkmcnt(13)\n\t"
      "v_mfma_f32_16x16x32_bf16 v[36:39], a[236:239], a[172:175], v[36:39]\n\t"
      "buffer_load_dwordx4 v14, s[40:43], s73 offen offset:1024 nt lds\n\t"
      "v_mov_b32 v248, v74\n\t"
      "s_waitcnt lgkmcnt(12)\n\t"
      "v_mfma_f32_16x16x32_bf16 v[36:39], a[240:243], a[176:179], v[36:39]\n\t"
      "v_permlane16_swap_b32 v74, v248\n\t"
      "v_max_f32 v74, v74, v248\n\t"
      "s_waitcnt lgkmcnt(11)\n\t"
      "v_mfma_f32_16x16x32_bf16 v[36:39], a[244:247], a[180:183], v[36:39]\n\t"
      "ds_write_b32 v79, v74 offset:512\n\t"
      "v_max3_f32 v75, v28, v29, v30\n\t"
      "s_waitcnt lgkmcnt(11)\n\t"
      "v_mfma_f32_16x16x32_bf16 v[36:39], a[248:251], a[184:187], v[36:39]\n\t"
      "v_max_f32 v75, v75, v31\n\t"
      "v_mov_b32 v249, v75\n\t"
      "s_waitcnt lgkmcnt(10)\n\t"
      "v_mfma_f32_16x16x32_bf16 v[36:39], a[252:255], a[188:191], v[36:39]\n\t"
      "buffer_load_dwordx4 v14, s[40:43], s74 offen offset:2048 nt lds\n\t"
      "v_permlane32_swap_b32 v75, v249\n\t"
      "s_waitcnt lgkmcnt(8)\n\t"
      "v_mfma_f32_16x16x32_bf16 v[40:43], a[224:227], a[192:195], 0\n\t"
      "v_max_f32 v75, v75, v249\n\t"
      "v_mov_b32 v249, v75\n\t"
      "s_waitcnt lgkmcnt(7)\n\t"
      "v_mfma_f32_16x16x32_bf16 v[40:43], a[228:231], a[196:199], v[40:43]\n\t"
      "v_permlane16_swap_b32 v75, v249\n\t"
      "v_max_f32 v75, v75, v249\n\t"
      "s_waitcnt lgkmcnt(6)\n\t"
      "v_mfma_f32_16x16x32_bf16 v[40:43], a[232:235], a[200:203], v[40:43]\n\t"
      "ds_write_b32 v79, v75 offset:768\n\t"
      "v_max3_f32 v76, v32, v33, v34\n\t"
      "s_waitcnt lgkmcnt(6)\n\t"
      "v_mfma_f32_16x16x32_bf16 v[40:43], a[236:239], a[204:207], v[40:43]\n\t"
      "buffer_load_dwordx4 v14, s[40:43], s75 offen offset:3072 nt lds\n\t"
      "s_add_u32 s40, s40, 32768\n\t"
      "s_waitcnt lgkmcnt(5)\n\t"
      "v_mfma_f32_16x16x32_bf16 v[40:43], a[240:243], a[208:211], v[40:43]\n\t"
      "s_addc_u32 s41, s41, 0\n\t"
      "v_max_f32 v76, v76, v35\n\t"
      "s_waitcnt lgkmcnt(4)\n\t"
      "v_mfma_f32_16x16x32_bf16 v[40:43], a[244:247], a[212:215], v[40:43]\n\t"
      "v_mov_b32 v248, v76\n\t"
      "s_nop 1\n\t"
      "v_permlane32_swap_b32 v76, v248\n\t"
      "s_waitcnt lgkmcnt(3)\n\t"
      "v_mfma_f32_16x16x32_bf16 v[40:43], a[248:251], a[216:219], v[40:43]\n\t"
      "v_max_f32 v76, v76, v248\n\t"
      "v_mov_b32 v248, v76\n\t"
      "s_waitcnt lgkmcnt(2)\n\t"
      "v_mfma_f32_16x16x32_bf16 v[40:43], a[252:255], a[220:223], v[40:43]\n\t"
      "v_permlane16_swap_b32 v76, v248\n\t"
      "v_max_f32 v76, v76, v248\n\t"
      "ds_write_b32 v79, v76 offset:1024\n\t"
      "v_max3_f32 v77, v36, v37, v38\n\t"
      "v_max_f32 v77, v77, v39\n\t"
      "v_mov_b32 v249, v77\n\t"
      "s_nop 1\n\t"
      "v_permlane32_swap_b32 v77, v249\n\t"
      "v_max_f32 v77, v77, v249\n\t"
      "v_mov_b32 v249, v77\n\t"
      "s_nop 1\n\t"
      "v_permlane16_swap_b32 v77, v249\n\t"
      "v_max_f32 v77, v77, v249\n\t"
      "ds_write_b32 v79, v77 offset:1280\n\t"
      "v_max3_f32 v78, v40, v41, v42\n\t"
      "v_max_f32 v78, v78, v43\n\t"
      "v_mov_b32 v248, v78\n\t"
      "s_nop 1\n\t"
      "v_permlane32_swap_b32 v78, v248\n\t"
      "v_max_f32 v78, v78, v248\n\t"
      "v_mov_b32 v248, v78\n\t"
      "s_nop 1\n\t"
      "v_permlane16_swap_b32 v78, v248\n\t"
      "v_max_f32 v78, v78, v248\n\t"
      "ds_write_b32 v79, v78 offset:1536\n\t"
      "s_add_u32 s94, s94, 1024\n\t"
      "v_mov_b32 v87, s94\n\t"
      "s_mov_b32 s46, s28\n\t"
      "buffer_load_dword v81, v87, s[44:47], 0 offen offset:0 sc1\n\t"
      "v_mbcnt_lo_u32_b32 v0, -1, 0\n\t"
      "v_mbcnt_hi_u32_b32 v0, -1, v0\n\t"
      "v_and_b32 v1, 15, v0\n\t"
      "v_lshrrev_b32 v2, 4, v0\n\t"
      "v_lshlrev_b32 v3, 2, v0\n\t"
      "s_min_u32 s46, s29, 448\n\t"
      "buffer_load_dword v252, v3, s[44:47], 0 offen offset:0 sc1\n\t"
      "buffer_load_dword v253, v3, s[44:47], 0 offen offset:256 sc1\n\t"
      "s_waitcnt lgkmcnt(0)\n\t"
      "s_barrier\n\t"
      "X1P%=:\n\t"
      "ds_read_b128 v[44:47], v80 offset:0\n\t"
      "ds_read_b128 v[48:51], v80 offset:256\n\t"
      "ds_read_b128 v[52:55], v80 offset:512\n\t"
      "ds_read_b128 v[56:59], v80 offset:768\n\t"
      "ds_read_b128 v[60:63], v80 offset:1024\n\t"
      "ds_read_b128 v[64:67], v80 offset:1280\n\t"
      "ds_read_b128 v[68:71], v80 offset:1536\n\t"
      "s_add_u32 m0, s89, 98304\n\t"
      "s_nop 0\n\t"
      "buffer_load_dwordx4 v14, s[40:43], s68 offen offset:0 nt lds\n\t"
      "s_waitcnt lgkmcnt(6)\n\t"
      "v_max3_f32 v44, v44, v45, v46\n\t"
      "v_max_f32 v44, v44, v47\n\t"
      "v_sub_f32 v44, 0, v44\n\t"
      "v_add_f32 v16, v16, v44\n\t"
      "v_add_f32 v17, v17, v44\n\t"
      "v_add_f32 v18, v18, v44\n\t"
      "v_add_f32 v19, v19, v44\n\t"
      "v_mov_b32 v45, v44\n\t"
      "v_mov_b32 v46, v44\n\t"
      "v_mov_b32 v47, v44\n\t"
      "v_exp_f32 v16, v16\n\t"
      "v_exp_f32 v17, v17\n\t"
      "v_exp_f32 v18, v18\n\t"
      "v_exp_f32 v19, v19\n\t"
      "v_add_f32 v72, v16, v17\n\t"
      "v_cvt_pk_bf16_f32 v82, v16, v17\n\t"
      "v_add_f32 v72, v72, v18\n\t"
      "v_cvt_pk_bf16_f32 v83, v18, v19\n\t"
      "v_add_f32 v72, v72, v19\n\t"
      "ds_write_b64 v11, v[82:83] offset:0\n\t"
      "v_mfma_f32_16x16x32_bf16 v[16:19], v[104:107], a[0:3], v[44:47]\n\t"
      "buffer_load_dwordx4 v14, s[40:43], s69 offen offset:1024 nt lds\n\t"
      "s_waitcnt lgkmcnt(6)\n\t"
      "v_max3_f32 v48, v48, v49, v50\n\t"
      "v_mfma_f32_16x16x32_bf16 v[16:19], v[108:111], a[4:7], v[16:19]\n\t"
      "v_max_f32 v48, v48, v51\n\t"
      "v_sub_f32 v48, 0, v48\n\t"
      "v_mfma_f32_16x16x32_bf16 v[16:19], v[112:115], a[8:11], v[16:19]\n\t"
      "v_add_f32 v20, v20, v48\n\t"
      "v_add_f32 v21, v21, v48\n\t"
      "v_mfma_f32_16x16x32_bf16 v[16:19], v[116:119], a[12:15], v[16:19]\n\t"
      "v_add_f32 v22, v22, v48\n\t"
      "v_add_f32 v23, v23, v48\n\t"
      "v_mfma_f32_16x16x32_bf16 v[16:19], v[120:123], a[16:19], v[16:19]\n\t"
      "v_mov_b32 v49, v48\n\t"
      "v_mov_b32 v50, v48\n\t"
      "v_mfma_f32_16x16x32_bf16 v[16:19], v[124:127], a[20:23], v[16:19]\n\t"
      "v_mov_b32 v51, v48\n\t"
      "v_exp_f32 v20, v20\n\t"
      "v_mfma_f32_16x16x32_bf16 v[16:19], v[128:131], a[24:27], v[16:19]\n\t"
      "v_exp_f32 v21, v21\n\t"
      "v_mfma_f32_16x16x32_bf16 v[16:19], v[132:135], a[28:31], v[16:19]\n\t"
      "v_exp_f32 v22, v22\n\t"
      "v_exp_f32 v23, v23\n\t"
      "v_add_f32 v73, v20, v21\n\t"
      "v_cvt_pk_bf16_f32 v84, v20, v21\n\t"
      "v_add_f32 v73, v73, v22\n\t"
      "v_cvt_pk_bf16_f32 v85, v22, v23\n\t"
      "v_add_f32 v73, v73, v23\n\t"
      "ds_write_b64 v11, v[84:85] offset:2048\n\t"
      "v_mfma_f32_16x16x32_bf16 v[20:23], v[104:107], a[32:35], v[48:51]\n\t"
      "buffer_load_dwordx4 v14, s[40:43], s70 offen offset:2048 nt lds\n\t"
      "s_waitcnt lgkmcnt(6)\n\t"
      "v_max3_f32 v52, v52, v53, v54\n\t"
      "v_mfma_f32_16x16x32_bf16 v[20:23], v[108:111], a[36:39], v[20:23]\n\t"
      "v_max_f32 v52, v52, v55\n\t"
      "v_sub_f32 v52, 0, v52\n\t"
      "v_mfma_f32_16x16x32_bf16 v[20:23], v[112:115], a[40:43], v[20:23]\n\t"
      "v_add_f32 v24, v24, v52\n\t"
      "v_add_f32 v25, v25, v52\n\t"
      "v_mfma_f32_16x16x32_bf16 v[20:23], v[116:119], a[44:47], v[20:23]\n\t"
      "v_add_f32 v26, v26, v52\n\t"
      "v_add_f32 v27, v27, v52\n\t"
      "v_mfma_f32_16x16x32_bf16 v[20:23], v[120:123], a[48:51], v[20:23]\n\t"
      "v_mov_b32 v53, v52\n\t"
      "v_mov_b32 v54, v52\n\t"
      "v_mfma_f32_16x16x32_bf16 v[20:23], v[124:127], a[52:55], v[20:23]\n\t"
      "v_mov_b32 v55, v52\n\t"
      "v_exp_f32 v24, v24\n\t"
      "v_mfma_f32_16x16x32_bf16 v[20:23], v[128:131], a[56:59], v[20:23]\n\t"
      "v_exp_f32 v25, v25\n\t"
      "v_mfma_f32_16x16x32_bf16 v[20:23], v[132:135], a[60:63], v[20:23]\n\t"
      "v_exp_f32 v26, v26\n\t"
      "v_exp_f32 v27, v27\n\t"
      "v_add_f32 v74, v24, v25\n\t"
      "v_cvt_pk_bf16_f32 v82, v24, v25\n\t"
      "v_add_f32 v74, v74, v26\n\t"
      "v_cvt_pk_bf16_f32 v83, v26, v27\n\t"
      "v_add_f32 v74, v74, v27\n\t"
      "ds_write_b64 v11, v[82:83] offset:4096\n\t"
      "v_mfma_f32_16x16x32_bf16 v[24:27], v[104:107], a[64:67], v[52:55]\n\t"
      "buffer_load_dwordx4 v14, s[40:43], s71 offen offset:3072 nt lds\n\t"
      "s_waitcnt lgkmcnt(6)\n\t"
      "v_max3_f32 v56, v56, v57, v58\n\t"
      "v_mfma_f32_16x16x32_bf16 v[24:27], v[108:111], a[68:71], v[24:27]\n\t"
      "v_max_f32 v56, v56, v59\n\t"
      "v_sub_f32 v56, 0, v56\n\t"
      "v_mfma_f32_16x16x32_bf16 v[24:27], v[112:115], a[72:75], v[24:27]\n\t"
      "v_add_f32 v28, v28, v56\n\t"
      "v_add_f32 v29, v29, v56\n\t"
      "v_mfma_f32_16x16x32_bf16 v[24:27], v[116:119], a[76:79], v[24:27]\n\t"
      "v_add_f32 v30, v30, v56\n\t"
      "v_add_f32 v31, v31, v56\n\t"
      "v_mfma_f32_16x16x32_bf16 v[24:27], v[120:123], a[80:83], v[24:27]\n\t"
      "v_mov_b32 v57, v56\n\t"
      "v_mov_b32 v58, v56\n\t"
      "v_mfma_f32_16x16x32_bf16 v[24:27], v[124:127], a[84:87], v[24:27]\n\t"
      "v_mov_b32 v59, v56\n\t"
      "v_exp_f32 v28, v28\n\t"
      "v_mfma_f32_16x16x32_bf16 v[24:27], v[128:131], a[88:91], v[24:27]\n\t"
      "v_exp_f32 v29, v29\n\t"
      "v_mfma_f32_16x16x32_bf16 v[24:27], v[132:135], a[92:95], v[24:27]\n\t"
      "v_exp_f32 v30, v30\n\t"
      "v_exp_f32 v31, v31\n\t"
      "v_add_f32 v75, v28, v29\n\t"
      "v_cvt_pk_bf16_f32 v84, v28, v29\n\t"
      "v_add_f32 v75, v75, v30\n\t"
      "v_cvt_pk_bf16_f32 v85, v30, v31\n\t"
      "v_add_f32 v75, v75, v31\n\t"
      "ds_write_b64 v11, v[84:85] offset:6144\n\t"
      "v_mfma_f32_16x16x32_bf16 v[28:31], v[104:107], a[96:99], v[56:59]\n\t"
      "s_add_u32 m0, s89, 102400\n\t"
      "s_nop 0\n\t"
      "buffer_load_dwordx4 v14, s[40:43], s72 offen offset:0 nt lds\n\t"
      "v_mfma_f32_16x16x32_bf16 v[28:31], v[108:111], a[100:103], v[28:31]\n\t"
      "s_waitcnt lgkmcnt(6)\n\t"
      "v_max3_f32 v60, v60, v61, v62\n\t"
      "v_max_f32 v60, v60, v63\n\t"
      "v_mfma_f32_16x16x32_bf16 v[28:31], v[112:115], a[104:107], v[28:31]\n\t"
      "v_sub_f32 v60, 0, v60\n\t"
      "v_add_f32 v32, v32, v60\n\t"
      "v_mfma_f32_16x16x32_bf16 v[28:31], v[116:119], a[108:111], v[28:31]\n\t"
      "v_add_f32 v33, v33, v60\n\t"
      "v_add_f32 v34, v34, v60\n\t"
      "v_mfma_f32_16x16x32_bf16 v[28:31], v[120:123], a[112:115], v[28:31]\n\t"
      "v_add_f32 v35, v35, v60\n\t"
      "v_mov_b32 v61, v60\n\t"
      "v_mfma_f32_16x16x32_bf16 v[28:31], v[124:127], a[116:119], v[28:31]\n\t"
      "v_mov_b32 v62, v60\n\t"
      "v_mov_b32 v63, v60\n\t"
      "v_mfma_f32_16x16x32_bf16 v[28:31], v[128:131], a[120:123], v[28:31]\n\t"
      "v_exp_f32 v32, v32\n\t"
      "v_mfma_f32_16x16x32_bf16 v[28:31], v[132:135], a[124:127], v[28:31]\n\t"
      "v_exp_f32 v33, v33\n\t"
      "v_exp_f32 v34, v34\n\t"
      "v_exp_f32 v35, v35\n\t"
      "v_add_f32 v76, v32, v33\n\t"
      "v_cvt_pk_bf16_f32 v82, v32, v33\n\t"
      "v_add_f32 v76, v76, v34\n\t"
      "v_cvt_pk_bf16_f32 v83, v34, v35\n\t"
      "v_add_f32 v76, v76, v35\n\t"
      "ds_write_b64 v11, v[82:83] offset:8192\n\t"
      "v_mfma_f32_16x16x32_bf16 v[32:35], v[104:107], a[128:131], v[60:63]\n\t"
      "buffer_load_dwordx4 v14, s[40:43], s73 offen offset:1024 nt lds\n\t"
      "s_waitcnt lgkmcnt(6)\n\t"
      "v_max3_f32 v64, v64, v65, v66\n\t"
      "v_mfma_f32_16x16x32_bf16 v[32:35], v[108:111], a[132:135], v[32:35]\n\t"
      "v_max_f32 v64, v64, v67\n\t"
      "v_sub_f32 v64, 0, v64\n\t"
      "v_mfma_f32_16x16x32_bf16 v[32:35], v[112:115], a[136:139], v[32:35]\n\t"
      "v_add_f32 v36, v36, v64\n\t"
      "v_add_f32 v37, v37, v64\n\t"
      "v_mfma_f32_16x16x32_bf16 v[32:35], v[116:119], a[140:143], v[32:35]\n\t"
      "v_add_f32 v38, v38, v64\n\t"
      "v_add_f32 v39, v39, v64\n\t"
      "v_mfma_f32_16x16x32_bf16 v[32:35], v[120:123], a[144:147], v[32:35]\n\t"
      "v_mov_b32 v65, v64\n\t"
      "v_mov_b32 v66, v64\n\t"
      "v_mfma_f32_16x16x32_bf16 v[32:35], v[124:127], a[148:151], v[32:35]\n\t"
      "v_mov_b32 v67, v64\n\t"
      "v_exp_f32 v36, v36\n\t"
      "v_mfma_f32_16x16x32_bf16 v[32:35], v[128:131], a[152:155], v[32:35]\n\t"
      "v_exp_f32 v37, v37\n\t"
      "v_mfma_f32_16x16x32_bf16 v[32:35], v[132:135], a[156:159], v[32:35]\n\t"
      "v_exp_f32 v38, v38\n\t"
      "v_exp_f32 v39, v39\n\t"
      "v_add_f32 v77, v36, v37\n\t"
      "v_cvt_pk_bf16_f32 v84, v36, v37\n\t"
      "v_add_f32 v77, v77, v38\n\t"
      "v_cvt_pk_bf16_f32 v85, v38, v39\n\t"
      "v_add_f32 v77, v77, v39\n\t"
      "ds_write_b64 v11, v[84:85] offset:10240\n\t"
      "v_mfma_f32_16x16x32_bf16 v[36:39], v[104:107], a[160:163], v[64:67]\n\t"
      "buffer_load_dwordx4 v14, s[40:43], s74 offen offset:2048 nt lds\n\t"
      "s_waitcnt lgkmcnt(6)\n\t"
      "v_max3_f32 v68, v68, v69, v70\n\t"
      "v_mfma_f32_16x16x32_bf16 v[36:39], v[108:111], a[164:167], v[36:39]\n\t"
      "v_max_f32 v68, v68, v71\n\t"
      "v_sub_f32 v68, 0, v68\n\t"
      "v_mfma_f32_16x16x32_bf16 v[36:39], v[112:115], a[168:171], v[36:39]\n\t"
      "v_add_f32 v40, v40, v68\n\t"
      "v_add_f32 v41, v41, v68\n\t"
      "v_mfma_f32_16x16x32_bf16 v[36:39], v[116:119], a[172:175], v[36:39]\n\t"
      "v_add_f32 v42, v42, v68\n\t"
      "v_add_f32 v43, v43, v68\n\t"
      "v_mfma_f32_16x16x32_bf16 v[36:39], v[120:123], a[176:179], v[36:39]\n\t"
      "v_mov_b32 v69, v68\n\t"
      "v_mov_b32 v70, v68\n\t"
      "v_mfma_f32_16x16x32_bf16 v[36:39], v[124:127], a[180:183], v[36:39]\n\t"
      "v_mov_b32 v71, v68\n\t"
      "v_exp_f32 v40, v40\n\t"
      "v_mfma_f32_16x16x32_bf16 v[36:39], v[128:131], a[184:187], v[36:39]\n\t"
      "v_exp_f32 v41, v41\n\t"
      "v_mfma_f32_16x16x32_bf16 v[36:39], v[132:135], a[188:191], v[36:39]\n\t"
      "v_exp_f32 v42, v42\n\t"
      "v_exp_f32 v43, v43\n\t"
      "v_add_f32 v78, v40, v41\n\t"
      "v_cvt_pk_bf16_f32 v82, v40, v41\n\t"
      "v_add_f32 v78, v78, v42\n\t"
      "v_cvt_pk_bf16_f32 v83, v42, v43\n\t"
      "v_add_f32 v78, v78, v43\n\t"
      "ds_write_b64 v11, v[82:83] offset:12288\n\t"
      "v_mfma_f32_16x16x32_bf16 v[40:43], v[104:107], a[192:195], v[68:71]\n\t"
      "buffer_load_dwordx4 v14, s[40:43], s75 offen offset:3072 nt lds\n\t"
      "s_add_u32 s40, s40, 32768\n\t"
      "v_mfma_f32_16x16x32_bf16 v[40:43], v[108:111], a[196:199], v[40:43]\n\t"
      "s_addc_u32 s41, s41, 0\n\t"
      "v_mfma_f32_16x16x32_bf16 v[40:43], v[112:115], a[200:203], v[40:43]\n\t"
      "v_mfma_f32_16x16x32_bf16 v[40:43], v[116:119], a[204:207], v[40:43]\n\t"
      "v_mfma_f32_16x16x32_bf16 v[40:43], v[120:123], a[208:211], v[40:43]\n\t"
      "v_mfma_f32_16x16x32_bf16 v[40:43], v[124:127], a[212:215], v[40:43]\n\t"
      "v_mfma_f32_16x16x32_bf16 v[40:43], v[128:131], a[216:219], v[40:43]\n\t"
      "v_mfma_f32_16x16x32_bf16 v[40:43], v[132:135], a[220:223], v[40:43]\n\t"
      "s_waitcnt vmcnt(11)\n\t"
      "ds_read_b64_tr_b16 v[104:105], v6 offset:0\n\t"
      "ds_read_b64_tr_b16 v[106:107], v6 offset:2048\n\t"
      "ds_read_b64_tr_b16 v[108:109], v6 offset:4096\n\t"
      "ds_read_b64_tr_b16 v[110:111], v6 offset:6144\n\t"
      "ds_read_b64_tr_b16 v[112:113], v7 offset:0\n\t"
      "ds_read_b64_tr_b16 v[114:115], v7 offset:2048\n\t"
      "ds_read_b64_tr_b16 v[116:117], v7 offset:4096\n\t"
      "ds_read_b64_tr_b16 v[118:119], v7 offset:6144\n\t"
      "ds_read_b64_tr_b16 v[120:121], v8 offset:0\n\t"
      "ds_read_b64_tr_b16 v[122:123], v8 offset:2048\n\t"
      "ds_read_b64_tr_b16 v[124:125], v8 offset:4096\n\t"
      "ds_read_b64_tr_b16 v[126:127], v8 offset:6144\n\t"
      "ds_read_b64_tr_b16 v[128:129], v9 offset:0\n\t"
      "ds_read_b64_tr_b16 v[130:131], v9 offset:2048\n\t"
      "ds_read_b64_tr_b16 v[132:133], v9 offset:4096\n\t"
      "ds_read_b64_tr_b16 v[134:135], v9 offset:6144\n\t"
      "s_waitcnt lgkmcnt(0)\n\t"
      "s_barrier\n\t"
      "s_waitcnt vmcnt(8)\n\t"
      "s_xor_b32 s95, s99, 1\n\t"
      "v_add_u32 v81, s95, v81\n\t"
      "s_lshl_b32 s94, s7, 2\n\t"
      "s_add_u32 s94, s94, 512\n\t"
      "v_mov_b32 v87, s94\n\t"
      "s_cmp_eq_u32 s99, 0\n\t"
      "s_cselect_b32 s46, s28, 0\n\t"
      "buffer_store_dword v81, v87, s[44:47], 0 offen offset:0 sc1\n\t"
      "s_mov_b32 s93, s6\n\t"
      "s_cmp_lg_u32 s99, 0\n\t"
      "s_cbranch_scc1 NLD%=\n\t"
      "s_cmp_eq_u32 s29, 0\n\t"
      "s_cbranch_scc1 NLD%=\n\t"
      "v_mov_b32 v1, 1\n\t"
      "v_mov_b32 v250, 147200\n\t"
      "v_mov_b32 v248, 0\n\t"
      "v_mov_b32 v249, 0\n\t"
      "ds_write_b64 v250, v[248:249] offset:0\n\t"
      "ds_write_b64 v250, v[248:249] offset:8\n\t"
      "v_lshrrev_b32 v3, 5, v252\n\t"
      "v_and_b32 v3, 3, v3\n\t"
      "v_lshlrev_b32 v3, 2, v3\n\t"
      "v_add_u32 v251, v250, v3\n\t"
      "v_and_b32 v3, 31, v252\n\t"
      "v_lshlrev_b32 v3, v3, v1\n\t"
      "ds_or_b32 v251, v3\n\t"
      "v_lshrrev_b32 v3, 5, v253\n\t"
      "v_and_b32 v3, 3, v3\n\t"
      "v_lshlrev_b32 v3, 2, v3\n\t"
      "v_add_u32 v251, v250, v3\n\t"
      "v_and_b32 v3, 31, v253\n\t"
      "v_lshlrev_b32 v3, v3, v1\n\t"
      "ds_or_b32 v251, v3\n\t"
      "ds_read_b128 v[248:251], v250 offset:0\n\t"
      "s_waitcnt lgkmcnt(0)\n\t"
      "v_readfirstlane_b32 s94, v248\n\t"
      "v_readfirstlane_b32 s95, v249\n\t"
      "v_readfirstlane_b32 s96, v250\n\t"
      "v_readfirstlane_b32 s2, v251\n\t"
      "s_mov_b32 s25, 0\n\t"
      "s_not_b32 s2, s2\n\t"
      "s_ff1_i32_b32 s3, s2\n\t"
      "s_add_u32 s3, s3, 96\n\t"
      "s_cmp_lg_u32 s2, 0\n\t"
      "s_cselect_b32 s25, s3, s25\n\t"
      "s_not_b32 s96, s96\n\t"
      "s_ff1_i32_b32 s3, s96\n\t"
      "s_add_u32 s3, s3, 64\n\t"
      "s_cmp_lg_u32 s96, 0\n\t"
      "s_cselect_b32 s25, s3, s25\n\t"
      "s_not_b32 s95, s95\n\t"
      "s_ff1_i32_b32 s3, s95\n\t"
      "s_add_u32 s3, s3, 32\n\t"
      "s_cmp_lg_u32 s95, 0\n\t"
      "s_cselect_b32 s25, s3, s25\n\t"
      "s_not_b32 s94, s94\n\t"
      "s_ff1_i32_b32 s3, s94\n\t"
      "s_add_u32 s3, s3, 0\n\t"
      "s_cmp_lg_u32 s94, 0\n\t"
      "s_cselect_b32 s25, s3, s25\n\t"
      "s_or_b32 s25, s25, 0x5a5a0000\n\t"
      "v_mov_b32 v86, s25\n\t"
      "s_waitcnt lgkmcnt(0)\n\t"
      "NLD%=:\n\t"
      "ds_read_b128 v[88:91], v10 offset:0\n\t"
      "ds_read_b128 v[92:95], v10 offset:1024\n\t"
      "ds_read_b128 v[96:99], v10 offset:2048\n\t"
      "ds_read_b128 v[100:103], v10 offset:3072\n\t"
      "s_waitcnt lgkmcnt(3)\n\t"
      "v_mfma_f32_16x16x32_bf16 v[136:139], v[104:107], v[88:91], 0\n\t"
      "v_exp_f32 v16, v16\n\t"
      "s_waitcnt lgkmcnt(2)\n\t"
      "v_mfma_f32_16x16x32_bf16 v[136:139], v[108:111], v[92:95], v[136:139]\n\t"
      "s_add_u32 m0, s89, 65536\n\t"
      "s_nop 0\n\t"
      "buffer_load_dwordx4 v14, s[40:43], s68 offen offset:0 nt lds\n\t"
      "v_mfma_f32_16x16x32_bf16 v[140:143], v[112:115], v[88:91], 0\n\t"
      "v_exp_f32 v17, v17\n\t"
      "v_mfma_f32_16x16x32_bf16 v[140:143], v[116:119], v[92:95], v[140:143]\n\t"
      "v_exp_f32 v18, v18\n\t"
      "v_mfma_f32_16x16x32_bf16 v[144:147], v[120:123], v[88:91], 0\n\t"
      "v_exp_f32 v19, v19\n\t"
      "v_mfma_f32_16x16x32_bf16 v[144:147], v[124:127], v[92:95], v[144:147]\n\t"
      "v_add_f32 v72, v72, v16\n\t"
      "v_cvt_pk_bf16_f32 v82, v16, v17\n\t"
      "v_mfma_f32_16x16x32_bf16 v[148:151], v[128:131], v[88:91], 0\n\t"
      "v_add_f32 v72, v72, v17\n\t"
      "v_add_f32 v72, v72, v18\n\t"
      "v_mfma_f32_16x16x32_bf16 v[148:151], v[132:135], v[92:95], v[148:151]\n\t"
      "ds_read_b128 v[88:91], v10 offset:4096\n\t"
      "ds_read_b128 v[92:95], v10 offset:5120\n\t"
      "s_waitcnt lgkmcnt(3)\n\t"
      "v_mfma_f32_16x16x32_bf16 v[152:155], v[104:107], v[96:99], 0\n\t"
      "buffer_load_dwordx4 v14, s[40:43], s69 offen offset:1024 nt lds\n\t"
      "v_cvt_pk_bf16_f32 v83, v18, v19\n\t"
      "s_waitcnt lgkmcnt(2)\n\t"
      "v_mfma_f32_16x16x32_bf16 v[152:155], v[108:111], v[100:103], v[152:155]\n\t"
      "v_add_f32 v72, v72, v19\n\t"
      "ds_write_b64 v11, v[82:83] offset:16384\n\t"
      "v_mfma_f32_16x16x32_bf16 v[156:159], v[112:115], v[96:99], 0\n\t"
      "v_exp_f32 v20, v20\n\t"
      "v_mfma_f32_16x16x32_bf16 v[156:159], v[116:119], v[100:103], v[156:159]\n\t"
      "v_exp_f32 v21, v21\n\t"
      "v_mfma_f32_16x16x32_bf16 v[160:163], v[120:123], v[96:99], 0\n\t"
      "v_exp_f32 v22, v22\n\t"
      "v_mfma_f32_16x16x32_bf16 v[160:163], v[124:127], v[100:103], v[160:163]\n\t"
      "v_exp_f32 v23, v23\n\t"
      "v_mfma_f32_16x16x32_bf16 v[164:167], v[128:131], v[96:99], 0\n\t"
      "v_add_f32 v73, v73, v20\n\t"
      "v_mfma_f32_16x16x32_bf16 v[164:167], v[132:135], v[100:103], v[164:167]\n\t"
      "ds_read_b128 v[96:99], v10 offset:6144\n\t"
      "ds_read_b128 v[100:103], v10 offset:7168\n\t"
      "s_waitcnt lgkmcnt(4)\n\t"
      "v_mfma_f32_16x16x32_bf16 v[168:171], v[104:107], v[88:91], 0\n\t"
      "buffer_load_dwordx4 v14, s[40:43], s70 offen offset:2048 nt lds\n\t"
      "v_cvt_pk_bf16_f32 v84, v20, v21\n\t"
      "s_waitcnt lgkmcnt(3)\n\t"
      "v_mfma_f32_16x16x32_bf16 v[168:171], v[108:111], v[92:95], v[168:171]\n\t"
      "v_add_f32 v73, v73, v21\n\t"
      "v_add_f32 v73, v73, v22\n\t"
      "v_mfma_f32_16x16x32_bf16 v[172:175], v[112:115], v[88:91], 0\n\t"
      "v_cvt_pk_bf16_f32 v85, v22, v23\n\t"
      "v_add_f32 v73, v73, v23\n\t"
      "v_mfma_f32_16x16x32_bf16 v[172:175], v[116:119], v[92:95], v[172:175]\n\t"
      "ds_write_b64 v11, v[84:85] offset:18432\n\t"
      "v_mfma_f32_16x16x32_bf16 v[176:179], v[120:123], v[88:91], 0\n\t"
      "v_exp_f32 v24, v24\n\t"
      "v_mfma_f32_16x16x32_bf16 v[176:179], v[124:127], v[92:95], v[176:179]\n\t"
      "v_exp_f32 v25, v25\n\t"
      "v_mfma_f32_16x16x32_bf16 v[180:183], v[128:131], v[88:91], 0\n\t"
      "buffer_load_dwordx4 v14, s[40:43], s71 offen offset:3072 nt lds\n\t"
      "v_exp_f32 v26, v26\n\t"
      "v_mfma_f32_16x16x32_bf16 v[180:183], v[132:135], v[92:95], v[180:183]\n\t"
      "ds_read_b128 v[88:91], v10 offset:8192\n\t"
      "s_waitcnt lgkmcnt(3)\n\t"
      "v_mfma_f32_16x16x32_bf16 v[184:187], v[104:107], v[96:99], 0\n\t"
      "ds_read_b128 v[92:95], v10 offset:9216\n\t"
      "v_exp_f32 v27, v27\n\t"
      "s_waitcnt lgkmcnt(3)\n\t"
      "v_mfma_f32_16x16x32_bf16 v[184:187], v[108:111], v[100:103], v[184:187]\n\t"
      "v_add_f32 v74, v74, v24\n\t"
      "v_mfma_f32_16x16x32_bf16 v[188:191], v[112:115], v[96:99], 0\n\t"
      "v_cvt_pk_bf16_f32 v82, v24, v25\n\t"
      "v_add_f32 v74, v74, v25\n\t"
      "v_mfma_f32_16x16x32_bf16 v[188:191], v[116:119], v[100:103], v[188:191]\n\t"
      "v_add_f32 v74, v74, v26\n\t"
      "v_cvt_pk_bf16_f32 v83, v26, v27\n\t"
      "v_mfma_f32_16x16x32_bf16 v[192:195], v[120:123], v[96:99], 0\n\t"
      "v_add_f32 v74, v74, v27\n\t"
      "ds_write_b64 v11, v[82:83] offset:20480\n\t"
      "v_mfma_f32_16x16x32_bf16 v[192:195], v[124:127], v[100:103], v[192:195]\n\t"
      "s_add_u32 m0, s89, 69632\n\t"
      "v_mfma_f32_16x16x32_bf16 v[196:199], v[128:131], v[96:99], 0\n\t"
      "buffer_load_dwordx4 v14, s[40:43], s72 offen offset:0 nt lds\n\t"
      "v_exp_f32 v28, v28\n\t"
      "v_mfma_f32_16x16x32_bf16 v[196:199], v[132:135], v[100:103], v[196:199]\n\t"
      "ds_read_b128 v[96:99], v10 offset:10240\n\t"
      "s_waitcnt lgkmcnt(3)\n\t"
      "v_mfma_f32_16x16x32_bf16 v[200:203], v[104:107], v[88:91], 0\n\t"
      "ds_read_b128 v[100:103], v10 offset:11264\n\t"
      "v_exp_f32 v29, v29\n\t"
      "s_waitcnt lgkmcnt(3)\n\t"
      "v_mfma_f32_16x16x32_bf16 v[200:203], v[108:111], v[92:95], v[200:203]\n\t"
      "v_exp_f32 v30, v30\n\t"
      "v_mfma_f32_16x16x32_bf16 v[204:207], v[112:115], v[88:91], 0\n\t"
      "v_exp_f32 v31, v31\n\t"
      "v_mfma_f32_16x16x32_bf16 v[204:207], v[116:119], v[92:95], v[204:207]\n\t"
      "v_add_f32 v75, v75, v28\n\t"
      "v_mfma_f32_16x16x32_bf16 v[208:211], v[120:123], v[88:91], 0\n\t"
      "buffer_load_dwordx4 v14, s[40:43], s73 offen offset:1024 nt lds\n\t"
      "v_cvt_pk_bf16_f32 v84, v28, v29\n\t"
      "v_mfma_f32_16x16x32_bf16 v[208:211], v[124:127], v[92:95], v[208:211]\n\t"
      "v_add_f32 v75, v75, v29\n\t"
      "v_add_f32 v75, v75, v30\n\t"
      "v_mfma_f32_16x16x32_bf16 v[212:215], v[128:131], v[88:91], 0\n\t"
      "v_cvt_pk_bf16_f32 v85, v30, v31\n\t"
      "v_add_f32 v75, v75, v31\n\t"
      "v_mfma_f32_16x16x32_bf16 v[212:215], v[132:135], v[92:95], v[212:215]\n\t"
      "ds_read_b128 v[88:91], v10 offset:12288\n\t"
      "ds_read_b128 v[92:95], v10 offset:13312\n\t"
      "s_waitcnt lgkmcnt(3)\n\t"
      "v_mfma_f32_16x16x32_bf16 v[216:219], v[104:107], v[96:99], 0\n\t"
      "ds_read_b128 a[224:227], v4 offset:32768\n\t"
      "s_waitcnt lgkmcnt(3)\n\t"
      "v_mfma_f32_16x16x32_bf16 v[216:219], v[108:111], v[100:103], v[216:219]\n\t"
      "ds_read_b128 a[228:231], v5 offset:32768\n\t"
      "ds_read_b128 a[232:235], v4 offset:34816\n\t"
      "v_mfma_f32_16x16x32_bf16 v[220:223], v[112:115], v[96:99], 0\n\t"
      "ds_read_b128 a[236:239], v5 offset:34816\n\t"
      "ds_read_b128 a[240:243], v4 offset:36864\n\t"
      "v_mfma_f32_16x16x32_bf16 v[220:223], v[116:119], v[100:103], v[220:223]\n\t"
      "ds_read_b128 a[244:247], v5 offset:36864\n\t"
      "ds_read_b128 a[248:251], v4 offset:38912\n\t"
      "v_mfma_f32_16x16x32_bf16 v[224:227], v[120:123], v[96:99], 0\n\t"
      "ds_read_b128 a[252:255], v5 offset:38912\n\t"
      "buffer_load_dwordx4 v14, s[40:43], s74 offen offset:2048 nt lds\n\t"
      "v_mfma_f32_16x16x32_bf16 v[224:227], v[124:127], v[100:103], v[224:227]\n\t"
      "ds_write_b64 v11, v[84:85] offset:22528\n\t"
      "v_mfma_f32_16x16x32_bf16 v[228:231], v[128:131], v[96:99], 0\n\t"
      "v_exp_f32 v32, v32\n\t"
      "v_mfma_f32_16x16x32_bf16 v[228:231], v[132:135], v[100:103], v[228:231]\n\t"
      "v_exp_f32 v33, v33\n\t"
      "s_waitcnt lgkmcnt(10)\n\t"
      "v_mfma_f32_16x16x32_bf16 v[232:235], v[104:107], v[88:91], 0\n\t"
      "v_exp_f32 v34, v34\n\t"
      "s_waitcnt lgkmcnt(9)\n\t"
      "v_mfma_f32_16x16x32_bf16 v[232:235], v[108:111], v[92:95], v[232:235]\n\t"
      "v_exp_f32 v35, v35\n\t"
      "v_mfma_f32_16x16x32_bf16 v[236:239], v[112:115], v[88:91], 0\n\t"
      "buffer_load_dwordx4 v14, s[40:43], s75 offen offset:3072 nt lds\n\t"
      "s_add_u32 s40, s40, 32768\n\t"
      "v_mfma_f32_16x16x32_bf16 v[236:239], v[116:119], v[92:95], v[236:239]\n\t"
      "s_addc_u32 s41, s41, 0\n\t"
      "v_add_f32 v76, v76, v32\n\t"
      "v_mfma_f32_16x16x32_bf16 v[240:243], v[120:123], v[88:91], 0\n\t"
      "v_cvt_pk_bf16_f32 v82, v32, v33\n\t"
      "v_add_f32 v76, v76, v33\n\t"
      "v_mfma_f32_16x16x32_bf16 v[240:243], v[124:127], v[92:95], v[240:243]\n\t"
      "v_add_f32 v76, v76, v34\n\t"
      "v_cvt_pk_bf16_f32 v83, v34, v35\n\t"
      "v_mfma_f32_16x16x32_bf16 v[244:247], v[128:131], v[88:91], 0\n\t"
      "v_add_f32 v76, v76, v35\n\t"
      "ds_write_b64 v11, v[82:83] offset:24576\n\t"
      "v_mfma_f32_16x16x32_bf16 v[244:247], v[132:135], v[92:95], v[244:247]\n\t"
      "v_exp_f32 v36, v36\n\t"
      "v_exp_f32 v37, v37\n\t"
      "v_exp_f32 v38, v38\n\t"
      "v_exp_f32 v39, v39\n\t"
      "v_add_f32 v77, v77, v36\n\t"
      "v_cvt_pk_bf16_f32 v84, v36, v37\n\t"
      "v_add_f32 v77, v77, v37\n\t"
      "v_add_f32 v77, v77, v38\n\t"
      "v_cvt_pk_bf16_f32 v85, v38, v39\n\t"
      "v_add_f32 v77, v77, v39\n\t"
      "ds_write_b64 v11, v[84:85] offset:26624\n\t"
      "v_exp_f32 v40, v40\n\t"
      "v_exp_f32 v41, v41\n\t"
      "v_exp_f32 v42, v42\n\t"
      "v_exp_f32 v43, v43\n\t"
      "v_add_f32 v78, v78, v40\n\t"
      "v_cvt_pk_bf16_f32 v82, v40, v41\n\t"
      "v_add_f32 v78, v78, v41\n\t"
      "v_add_f32 v78, v78, v42\n\t"
      "v_cvt_pk_bf16_f32 v83, v42, v43\n\t"
      "v_add_f32 v78, v78, v43\n\t"
      "ds_write_b64 v11, v[82:83] offset:28672\n\t"
      "s_waitcnt vmcnt(9)\n\t"
      "s_waitcnt lgkmcnt(0)\n\t"
      "s_barrier\n\t"
      "s_mov_b32 s91, 6\n\t"
      "TLOOP%=:\n\t"
      "v_mfma_f32_16x16x32_bf16 v[16:19], a[224:227], a[0:3], v[44:47]\n\t"
      "v_mfma_f32_16x16x32_bf16 v[16:19], a[228:231], a[4:7], v[16:19]\n\t"
      "s_add_u32 m0, s89, 32768\n\t"
      "s_nop 0\n\t"
      "buffer_load_dwordx4 v12, s[36:39], s60 offen offset:0 nt lds\n\t"
      "v_mfma_f32_16x16x32_bf16 v[16:19], a[232:235], a[8:11], v[16:19]\n\t"
      "v_mfma_f32_16x16x32_bf16 v[16:19], a[236:239], a[12:15], v[16:19]\n\t"
      "v_mfma_f32_16x16x32_bf16 v[16:19], a[240:243], a[16:19], v[16:19]\n\t"
      "v_mbcnt_lo_u32_b32 v250, -1, 0\n\t"
      "v_mbcnt_hi_u32_b32 v250, -1, v250\n\t"
      "v_and_b32 v250, 15, v250\n\t"
      "v_lshlrev_b32 v250, 2, v250\n\t"
      "s_mov_b32 s46, s29\n\t"
      "buffer_load_dword v248, v250, s[44:47], 0 offen offset:512 sc1\n\t"
      "v_mfma_f32_16x16x32_bf16 v[16:19], a[244:247], a[20:23], v[16:19]\n\t"
      "v_mfma_f32_16x16x32_bf16 v[16:19], a[248:251], a[24:27], v[16:19]\n\t"
      "v_mfma_f32_16x16x32_bf16 v[16:19], a[252:255], a[28:31], v[16:19]\n\t"
      "v_mfma_f32_16x16x32_bf16 v[20:23], a[224:227], a[32:35], v[48:51]\n\t"
      "buffer_load_dwordx4 v13, s[36:39], s61 offen offset:1024 nt lds\n\t"
      "v_mfma_f32_16x16x32_bf16 v[20:23], a[228:231], a[36:39], v[20:23]\n\t"
      "v_mfma_f32_16x16x32_bf16 v[20:23], a[232:235], a[40:43], v[20:23]\n\t"
      "v_mfma_f32_16x16x32_bf16 v[20:23], a[236:239], a[44:47], v[20:23]\n\t"
      "s_nop 2\n\t"
      "v_exp_f32 v16, v16\n\t"
      "v_mfma_f32_16x16x32_bf16 v[20:23], a[240:243], a[48:51], v[20:23]\n\t"
      "v_exp_f32 v17, v17\n\t"
      "v_mfma_f32_16x16x32_bf16 v[20:23], a[244:247], a[52:55], v[20:23]\n\t"
      "v_exp_f32 v18, v18\n\t"
      "v_mfma_f32_16x16x32_bf16 v[20:23], a[248:251], a[56:59], v[20:23]\n\t"
      "v_exp_f32 v19, v19\n\t"
      "v_mfma_f32_16x16x32_bf16 v[20:23], a[252:255], a[60:63], v[20:23]\n\t"
      "buffer_load_dwordx4 v12, s[36:39], s62 offen offset:2048 nt lds\n\t"
      "v_add_f32 v72, v72, v16\n\t"
      "v_mfma_f32_16x16x32_bf16 v[24:27], a[224:227], a[64:67], v[52:55]\n\t"
      "v_cvt_pk_bf16_f32 v82, v16, v17\n\t"
      "v_add_f32 v72, v72, v17\n\t"
      "v_mfma_f32_16x16x32_bf16 v[24:27], a[228:231], a[68:71], v[24:27]\n\t"
      "v_add_f32 v72, v72, v18\n\t"
      "v_cvt_pk_bf16_f32 v83, v18, v19\n\t"
      "v_mfma_f32_16x16x32_bf16 v[24:27], a[232:235], a[72:75], v[24:27]\n\t"
      "v_add_f32 v72, v72, v19\n\t"
      "ds_write_b64 v11, v[82:83] offset:0\n\t"
      "v_mfma_f32_16x16x32_bf16 v[24:27], a[236:239], a[76:79], v[24:27]\n\t"
      "v_exp_f32 v20, v20\n\t"
      "v_mfma_f32_16x16x32_bf16 v[24:27], a[240:243], a[80:83], v[24:27]\n\t"
      "v_exp_f32 v21, v21\n\t"
      "v_mfma_f32_16x16x32_bf16 v[24:27], a[244:247], a[84:87], v[24:27]\n\t"
      "v_exp_f32 v22, v22\n\t"
      "v_mfma_f32_16x16x32_bf16 v[24:27], a[248:251], a[88:91], v[24:27]\n\t"
      "buffer_load_dwordx4 v13, s[36:39], s63 offen offset:3072 nt lds\n\t"
      "v_mfma_f32_16x16x32_bf16 v[24:27], a[252:255], a[92:95], v[24:27]\n\t"
      "v_exp_f32 v23, v23\n\t"
      "v_mfma_f32_16x16x32_bf16 v[28:31], a[224:227], a[96:99], v[56:59]\n\t"
      "v_add_f32 v73, v73, v20\n\t"
      "v_cvt_pk_bf16_f32 v84, v20, v21\n\t"
      "v_mfma_f32_16x16x32_bf16 v[28:31], a[228:231], a[100:103], v[28:31]\n\t"
      "v_add_f32 v73, v73, v21\n\t"
      "v_add_f32 v73, v73, v22\n\t"
      "v_mfma_f32_16x16x32_bf16 v[28:31], a[232:235], a[104:107], v[28:31]\n\t"
      "v_cvt_pk_bf16_f32 v85, v22, v23\n\t"
      "v_add_f32 v73, v73, v23\n\t"
      "v_mfma_f32_16x16x32_bf16 v[28:31], a[236:239], a[108:111], v[28:31]\n\t"
      "ds_write_b64 v11, v[84:85] offset:2048\n\t"
      "v_mfma_f32_16x16x32_bf16 v[28:31], a[240:243], a[112:115], v[28:31]\n\t"
      "v_exp_f32 v24, v24\n\t"
      "v_mfma_f32_16x16x32_bf16 v[28:31], a[244:247], a[116:119], v[28:31]\n\t"
      "s_add_u32 m0, s89, 36864\n\t"
      "s_nop 0\n\t"
      "buffer_load_dwordx4 v12, s[36:39], s64 offen offset:0 nt lds\n\t"
      "v_mfma_f32_16x16x32_bf16 v[28:31], a[248:251], a[120:123], v[28:31]\n\t"
      "ds_read_b128 v[88:91], v10 offset:16384\n\t"
      "ds_read_b128 v[92:95], v10 offset:17408\n\t"
      "v_mfma_f32_16x16x32_bf16 v[28:31], a[252:255], a[124:127], v[28:31]\n\t"
      "ds_read_b128 v[96:99], v10 offset:18432\n\t"
      "ds_read_b128 v[100:103], v10 offset:19456\n\t"
      "v_mfma_f32_16x16x32_bf16 v[32:35], a[224:227], a[128:131], v[60:63]\n\t"
      "v_exp_f32 v25, v25\n\t"
      "v_mfma_f32_16x16x32_bf16 v[32:35], a[228:231], a[132:135], v[32:35]\n\t"
      "v_exp_f32 v26, v26\n\t"
      "v_mfma_f32_16x16x32_bf16 v[32:35], a[232:235], a[136:139], v[32:35]\n\t"
      "v_exp_f32 v27, v27\n\t"
      "v_mfma_f32_16x16x32_bf16 v[32:35], a[236:239], a[140:143], v[32:35]\n\t"
      "v_add_f32 v74, v74, v24\n\t"
      "v_cvt_pk_bf16_f32 v82, v24, v25\n\t"
      "v_mfma_f32_16x16x32_bf16 v[32:35], a[240:243], a[144:147], v[32:35]\n\t"
      "buffer_load_dwordx4 v13, s[36:39], s65 offen offset:1024 nt lds\n\t"
      "v_add_f32 v74, v74, v25\n\t"
      "v_mfma_f32_16x16x32_bf16 v[32:35], a[244:247], a[148:151], v[32:35]\n\t"
      "v_add_f32 v74, v74, v26\n\t"
      "v_cvt_pk_bf16_f32 v83, v26, v27\n\t"
      "v_mfma_f32_16x16x32_bf16 v[32:35], a[248:251], a[152:155], v[32:35]\n\t"
      "v_add_f32 v74, v74, v27\n\t"
      "ds_write_b64 v11, v[82:83] offset:4096\n\t"
      "v_mfma_f32_16x16x32_bf16 v[32:35], a[252:255], a[156:159], v[32:35]\n\t"
      "v_exp_f32 v28, v28\n\t"
      "v_mfma_f32_16x16x32_bf16 v[36:39], a[224:227], a[160:163], v[64:67]\n\t"
      "v_exp_f32 v29, v29\n\t"
      "v_mfma_f32_16x16x32_bf16 v[36:39], a[228:231], a[164:167], v[36:39]\n\t"
      "v_exp_f32 v30, v30\n\t"
      "v_mfma_f32_16x16x32_bf16 v[36:39], a[232:235], a[168:171], v[36:39]\n\t"
      "s_waitcnt vmcnt(23)\n\t"
      "v_mfma_f32_16x16x32_bf16 v[36:39], a[236:239], a[172:175], v[36:39]\n\t"
      "ds_read_b64_tr_b16 v[104:105], v6 offset:32768\n\t"
      "ds_read_b64_tr_b16 v[106:107], v6 offset:34816\n\t"
      "v_mfma_f32_16x16x32_bf16 v[36:39], a[240:243], a[176:179], v[36:39]\n\t"
      "ds_read_b64_tr_b16 v[108:109], v6 offset:36864\n\t"
      "ds_read_b64_tr_b16 v[110:111], v6 offset:38912\n\t"
      "v_mfma_f32_16x16x32_bf16 v[36:39], a[244:247], a[180:183], v[36:39]\n\t"
      "ds_read_b64_tr_b16 v[112:113], v7 offset:32768\n\t"
      "ds_read_b64_tr_b16 v[114:115], v7 offset:34816\n\t"
      "v_mfma_f32_16x16x32_bf16 v[36:39], a[248:251], a[184:187], v[36:39]\n\t"
      "ds_read_b64_tr_b16 v[116:117], v7 offset:36864\n\t"
      "ds_read_b64_tr_b16 v[118:119], v7 offset:38912\n\t"
      "v_mfma_f32_16x16x32_bf16 v[36:39], a[252:255], a[188:191], v[36:39]\n\t"
      "ds_read_b64_tr_b16 v[120:121], v8 offset:32768\n\t"
      "ds_read_b64_tr_b16 v[122:123], v8 offset:34816\n\t"
      "v_mfma_f32_16x16x32_bf16 v[40:43], a[224:227], a[192:195], v[68:71]\n\t"
      "ds_read_b64_tr_b16 v[124:125], v8 offset:36864\n\t"
      "ds_read_b64_tr_b16 v[126:127], v8 offset:38912\n\t"
      "v_mfma_f32_16x16x32_bf16 v[40:43], a[228:231], a[196:199], v[40:43]\n\t"
      "ds_read_b64_tr_b16 v[128:129], v9 offset:32768\n\t"
      "ds_read_b64_tr_b16 v[130:131], v9 offset:34816\n\t"
      "v_mfma_f32_16x16x32_bf16 v[40:43], a[232:235], a[200:203], v[40:43]\n\t"
      "ds_read_b64_tr_b16 v[132:133], v9 offset:36864\n\t"
      "ds_read_b64_tr_b16 v[134:135], v9 offset:38912\n\t"
      "v_mfma_f32_16x16x32_bf16 v[40:43], a[236:239], a[204:207], v[40:43]\n\t"
      "buffer_load_dwordx4 v12, s[36:39], s66 offen offset:2048 nt lds\n\t"
      "buffer_load_dwordx4 v13, s[36:39], s67 offen offset:3072 nt lds\n\t"
      "v_mfma_f32_16x16x32_bf16 v[40:43], a[240:243], a[208:211], v[40:43]\n\t"
      "s_add_u32 s36, s36, 32768\n\t"
      "s_addc_u32 s37, s37, 0\n\t"
      "v_mfma_f32_16x16x32_bf16 v[40:43], a[244:247], a[212:215], v[40:43]\n\t"
      "v_exp_f32 v31, v31\n\t"
      "v_mfma_f32_16x16x32_bf16 v[40:43], a[248:251], a[216:219], v[40:43]\n\t"
      "v_add_f32 v75, v75, v28\n\t"
      "v_cvt_pk_bf16_f32 v84, v28, v29\n\t"
      "v_mfma_f32_16x16x32_bf16 v[40:43], a[252:255], a[220:223], v[40:43]\n\t"
      "v_add_f32 v75, v75, v29\n\t"
      "v_add_f32 v75, v75, v30\n\t"
      "v_cvt_pk_bf16_f32 v85, v30, v31\n\t"
      "v_add_f32 v75, v75, v31\n\t"
      "ds_write_b64 v11, v[84:85] offset:6144\n\t"
      "s_waitcnt lgkmcnt(15)\n\t"
      "v_mfma_f32_16x16x32_bf16 v[136:139], v[104:107], v[88:91], v[136:139]\n\t"
      "v_exp_f32 v32, v32\n\t"
      "s_waitcnt lgkmcnt(13)\n\t"
      "v_mfma_f32_16x16x32_bf16 v[136:139], v[108:111], v[92:95], v[136:139]\n\t"
      "s_waitcnt lgkmcnt(1)\n\t"
      "s_add_u32 m0, s89, 98304\n\t"
      "v_mfma_f32_16x16x32_bf16 v[140:143], v[112:115], v[88:91], v[140:143]\n\t"
      "buffer_load_dwordx4 v14, s[40:43], s68 offen offset:0 nt lds\n\t"
      "v_exp_f32 v33, v33\n\t"
      "v_mfma_f32_16x16x32_bf16 v[140:143], v[116:119], v[92:95], v[140:143]\n\t"
      "v_exp_f32 v34, v34\n\t"
      "v_mfma_f32_16x16x32_bf16 v[144:147], v[120:123], v[88:91], v[144:147]\n\t"
      "v_exp_f32 v35, v35\n\t"
      "v_mfma_f32_16x16x32_bf16 v[144:147], v[124:127], v[92:95], v[144:147]\n\t"
      "v_add_f32 v76, v76, v32\n\t"
      "v_mfma_f32_16x16x32_bf16 v[148:151], v[128:131], v[88:91], v[148:151]\n\t"
      "v_cvt_pk_bf16_f32 v82, v32, v33\n\t"
      "v_add_f32 v76, v76, v33\n\t"
      "v_mfma_f32_16x16x32_bf16 v[148:151], v[132:135], v[92:95], v[148:151]\n\t"
      "ds_read_b128 v[88:91], v10 offset:20480\n\t"
      "ds_read_b128 v[92:95], v10 offset:21504\n\t"
      "v_mfma_f32_16x16x32_bf16 v[152:155], v[104:107], v[96:99], v[152:155]\n\t"
      "buffer_load_dwordx4 v14, s[40:43], s69 offen offset:1024 nt lds\n\t"
      "v_add_f32 v76, v76, v34\n\t"
      "v_mfma_f32_16x16x32_bf16 v[152:155], v[108:111], v[100:103], v[152:155]\n\t"
      "v_cvt_pk_bf16_f32 v83, v34, v35\n\t"
      "v_add_f32 v76, v76, v35\n\t"
      "v_mfma_f32_16x16x32_bf16 v[156:159], v[112:115], v[96:99], v[156:159]\n\t"
      "ds_write_b64 v11, v[82:83] offset:8192\n\t"
      "v_mfma_f32_16x16x32_bf16 v[156:159], v[116:119], v[100:103], v[156:159]\n\t"
      "v_exp_f32 v36, v36\n\t"
      "v_mfma_f32_16x16x32_bf16 v[160:163], v[120:123], v[96:99], v[160:163]\n\t"
      "v_exp_f32 v37, v37\n\t"
      "v_mfma_f32_16x16x32_bf16 v[160:163], v[124:127], v[100:103], v[160:163]\n\t"
      "v_exp_f32 v38, v38\n\t"
      "v_mfma_f32_16x16x32_bf16 v[164:167], v[128:131], v[96:99], v[164:167]\n\t"
      "v_exp_f32 v39, v39\n\t"
      "v_mfma_f32_16x16x32_bf16 v[164:167], v[132:135], v[100:103], v[164:167]\n\t"
      "ds_read_b128 v[96:99], v10 offset:22528\n\t"
      "ds_read_b128 v[100:103], v10 offset:23552\n\t"
      "s_waitcnt lgkmcnt(4)\n\t"
      "v_mfma_f32_16x16x32_bf16 v[168:171], v[104:107], v[88:91], v[168:171]\n\t"
      "buffer_load_dwordx4 v14, s[40:43], s70 offen offset:2048 nt lds\n\t"
      "v_add_f32 v77, v77, v36\n\t"
      "s_waitcnt lgkmcnt(3)\n\t"
      "v_mfma_f32_16x16x32_bf16 v[168:171], v[108:111], v[92:95], v[168:171]\n\t"
      "v_cvt_pk_bf16_f32 v84, v36, v37\n\t"
      "v_add_f32 v77, v77, v37\n\t"
      "v_mfma_f32_16x16x32_bf16 v[172:175], v[112:115], v[88:91], v[172:175]\n\t"
      "v_add_f32 v77, v77, v38\n\t"
      "v_cvt_pk_bf16_f32 v85, v38, v39\n\t"
      "v_mfma_f32_16x16x32_bf16 v[172:175], v[116:119], v[92:95], v[172:175]\n\t"
      "v_add_f32 v77, v77, v39\n\t"
      "ds_write_b64 v11, v[84:85] offset:10240\n\t"
      "v_mfma_f32_16x16x32_bf16 v[176:179], v[120:123], v[88:91], v[176:179]\n\t"
      "v_exp_f32 v40, v40\n\t"
      "v_mfma_f32_16x16x32_bf16 v[176:179], v[124:127], v[92:95], v[176:179]\n\t"
      "v_exp_f32 v41, v41\n\t"
      "v_mfma_f32_16x16x32_bf16 v[180:183], v[128:131], v[88:91], v[180:183]\n\t"
      "buffer_load_dwordx4 v14, s[40:43], s71 offen offset:3072 nt lds\n\t"
      "v_mfma_f32_16x16x32_bf16 v[180:183], v[132:135], v[92:95], v[180:183]\n\t"
      "ds_read_b128 v[88:91], v10 offset:24576\n\t"
      "ds_read_b128 v[92:95], v10 offset:25600\n\t"
      "s_waitcnt lgkmcnt(4)\n\t"
      "v_mfma_f32_16x16x32_bf16 v[184:187], v[104:107], v[96:99], v[184:187]\n\t"
      "v_exp_f32 v42, v42\n\t"
      "s_waitcnt lgkmcnt(3)\n\t"
      "v_mfma_f32_16x16x32_bf16 v[184:187], v[108:111], v[100:103], v[184:187]\n\t"
      "v_exp_f32 v43, v43\n\t"
      "v_mfma_f32_16x16x32_bf16 v[188:191], v[112:115], v[96:99], v[188:191]\n\t"
      "v_add_f32 v78, v78, v40\n\t"
      "v_cvt_pk_bf16_f32 v82, v40, v41\n\t"
      "v_mfma_f32_16x16x32_bf16 v[188:191], v[116:119], v[100:103], v[188:191]\n\t"
      "v_add_f32 v78, v78, v41\n\t"
      "v_add_f32 v78, v78, v42\n\t"
      "v_mfma_f32_16x16x32_bf16 v[192:195], v[120:123], v[96:99], v[192:195]\n\t"
      "v_cvt_pk_bf16_f32 v83, v42, v43\n\t"
      "v_add_f32 v78, v78, v43\n\t"
      "v_mfma_f32_16x16x32_bf16 v[192:195], v[124:127], v[100:103], v[192:195]\n\t"
      "s_add_u32 m0, s89, 102400\n\t"
      "s_nop 0\n\t"
      "buffer_load_dwordx4 v14, s[40:43], s72 offen offset:0 nt lds\n\t"
      "v_mfma_f32_16x16x32_bf16 v[196:199], v[128:131], v[96:99], v[196:199]\n\t"
      "ds_write_b64 v11, v[82:83] offset:12288\n\t"
      "v_mfma_f32_16x16x32_bf16 v[196:199], v[132:135], v[100:103], v[196:199]\n\t"
      "ds_read_b128 v[96:99], v10 offset:26624\n\t"
      "ds_read_b128 v[100:103], v10 offset:27648\n\t"
      "s_waitcnt lgkmcnt(4)\n\t"
      "v_mfma_f32_16x16x32_bf16 v[200:203], v[104:107], v[88:91], v[200:203]\n\t"
      "s_waitcnt lgkmcnt(3)\n\t"
      "v_mfma_f32_16x16x32_bf16 v[200:203], v[108:111], v[92:95], v[200:203]\n\t"
      "v_mfma_f32_16x16x32_bf16 v[204:207], v[112:115], v[88:91], v[204:207]\n\t"
      "v_mfma_f32_16x16x32_bf16 v[204:207], v[116:119], v[92:95], v[204:207]\n\t"
      "v_mfma_f32_16x16x32_bf16 v[208:211], v[120:123], v[88:91], v[208:211]\n\t"
      "buffer_load_dwordx4 v14, s[40:43], s73 offen offset:1024 nt lds\n\t"
      "v_mfma_f32_16x16x32_bf16 v[208:211], v[124:127], v[92:95], v[208:211]\n\t"
      "v_mfma_f32_16x16x32_bf16 v[212:215], v[128:131], v[88:91], v[212:215]\n\t"
      "v_mfma_f32_16x16x32_bf16 v[212:215], v[132:135], v[92:95], v[212:215]\n\t"
      "ds_read_b128 v[88:91], v10 offset:28672\n\t"
      "ds_read_b128 v[92:95], v10 offset:29696\n\t"
      "s_waitcnt lgkmcnt(3)\n\t"
      "v_mfma_f32_16x16x32_bf16 v[216:219], v[104:107], v[96:99], v[216:219]\n\t"
      "s_waitcnt lgkmcnt(2)\n\t"
      "v_mfma_f32_16x16x32_bf16 v[216:219], v[108:111], v[100:103], v[216:219]\n\t"
      "v_mfma_f32_16x16x32_bf16 v[220:223], v[112:115], v[96:99], v[220:223]\n\t"
      "v_mfma_f32_16x16x32_bf16 v[220:223], v[116:119], v[100:103], v[220:223]\n\t"
      "buffer_load_dwordx4 v14, s[40:43], s74 offen offset:2048 nt lds\n\t"
      "v_mfma_f32_16x16x32_bf16 v[224:227], v[120:123], v[96:99], v[224:227]\n\t"
      "s_waitcnt vmcnt(23)\n\t"
      "ds_read_b128 a[224:227], v4 offset:0\n\t"
      "v_mfma_f32_16x16x32_bf16 v[224:227], v[124:127], v[100:103], v[224:227]\n\t"
      "ds_read_b128 a[228:231], v5 offset:0\n\t"
      "ds_read_b128 a[232:235], v4 offset:2048\n\t"
      "v_mfma_f32_16x16x32_bf16 v[228:231], v[128:131], v[96:99], v[228:231]\n\t"
      "ds_read_b128 a[236:239], v5 offset:2048\n\t"
      "ds_read_b128 a[240:243], v4 offset:4096\n\t"
      "v_mfma_f32_16x16x32_bf16 v[228:231], v[132:135], v[100:103], v[228:231]\n\t"
      "ds_read_b128 a[244:247], v5 offset:4096\n\t"
      "ds_read_b128 a[248:251], v4 offset:6144\n\t"
      "s_waitcnt lgkmcnt(8)\n\t"
      "v_mfma_f32_16x16x32_bf16 v[232:235], v[104:107], v[88:91], v[232:235]\n\t"
      "ds_read_b128 a[252:255], v5 offset:6144\n\t"
      "s_waitcnt lgkmcnt(8)\n\t"
      "v_mfma_f32_16x16x32_bf16 v[232:235], v[108:111], v[92:95], v[232:235]\n\t"
      "v_mfma_f32_16x16x32_bf16 v[236:239], v[112:115], v[88:91], v[236:239]\n\t"
      "buffer_load_dwordx4 v14, s[40:43], s75 offen offset:3072 nt lds\n\t"
      "s_add_u32 s40, s40, 32768\n\t"
      "v_mfma_f32_16x16x32_bf16 v[236:239], v[116:119], v[92:95], v[236:239]\n\t"
      "s_addc_u32 s41, s41, 0\n\t"
      "v_mfma_f32_16x16x32_bf16 v[240:243], v[120:123], v[88:91], v[240:243]\n\t"
      "v_mfma_f32_16x16x32_bf16 v[240:243], v[124:127], v[92:95], v[240:243]\n\t"
      "v_mfma_f32_16x16x32_bf16 v[244:247], v[128:131], v[88:91], v[244:247]\n\t"
      "v_mfma_f32_16x16x32_bf16 v[244:247], v[132:135], v[92:95], v[244:247]\n\t"
      "s_waitcnt lgkmcnt(0)\n\t"
      "s_barrier\n\t"
      "v_mfma_f32_16x16x32_bf16 v[16:19], a[224:227], a[0:3], v[44:47]\n\t"
      "v_mfma_f32_16x16x32_bf16 v[16:19], a[228:231], a[4:7], v[16:19]\n\t"
      "s_add_u32 m0, s89, 0\n\t"
      "s_nop 0\n\t"
      "buffer_load_dwordx4 v12, s[36:39], s60 offen offset:0 nt lds\n\t"
      "v_mfma_f32_16x16x32_bf16 v[16:19], a[232:235], a[8:11], v[16:19]\n\t"
      "v_mfma_f32_16x16x32_bf16 v[16:19], a[236:239], a[12:15], v[16:19]\n\t"
      "v_mfma_f32_16x16x32_bf16 v[16:19], a[240:243], a[16:19], v[16:19]\n\t"
      "v_mfma_f32_16x16x32_bf16 v[16:19], a[244:247], a[20:23], v[16:19]\n\t"
      "v_mfma_f32_16x16x32_bf16 v[16:19], a[248:251], a[24:27], v[16:19]\n\t"
      "v_mfma_f32_16x16x32_bf16 v[16:19], a[252:255], a[28:31], v[16:19]\n\t"
      "v_mfma_f32_16x16x32_bf16 v[20:23], a[224:227], a[32:35], v[48:51]\n\t"
      "buffer_load_dwordx4 v13, s[36:39], s61 offen offset:1024 nt lds\n\t"
      "v_mfma_f32_16x16x32_bf16 v[20:23], a[228:231], a[36:39], v[20:23]\n\t"
      "v_mfma_f32_16x16x32_bf16 v[20:23], a[232:235], a[40:43], v[20:23]\n\t"
      "v_mfma_f32_16x16x32_bf16 v[20:23], a[236:239], a[44:47], v[20:23]\n\t"
      "s_nop 2\n\t"
      "v_exp_f32 v16, v16\n\t"
      "v_mfma_f32_16x16x32_bf16 v[20:23], a[240:243], a[48:51], v[20:23]\n\t"
      "v_exp_f32 v17, v17\n\t"
      "v_mfma_f32_16x16x32_bf16 v[20:23], a[244:247], a[52:55], v[20:23]\n\t"
      "v_exp_f32 v18, v18\n\t"
      "v_mfma_f32_16x16x32_bf16 v[20:23], a[248:251], a[56:59], v[20:23]\n\t"
      "v_exp_f32 v19, v19\n\t"
      "v_mfma_f32_16x16x32_bf16 v[20:23], a[252:255], a[60:63], v[20:23]\n\t"
      "buffer_load_dwordx4 v12, s[36:39], s62 offen offset:2048 nt lds\n\t"
      "v_add_f32 v72, v72, v16\n\t"
      "v_mfma_f32_16x16x32_bf16 v[24:27], a[224:227], a[64:67], v[52:55]\n\t"
      "v_cvt_pk_bf16_f32 v82, v16, v17\n\t"
      "v_add_f32 v72, v72, v17\n\t"
      "v_mfma_f32_16x16x32_bf16 v[24:27], a[228:231], a[68:71], v[24:27]\n\t"
      "v_add_f32 v72, v72, v18\n\t"
      "v_cvt_pk_bf16_f32 v83, v18, v19\n\t"
      "v_mfma_f32_16x16x32_bf16 v[24:27], a[232:235], a[72:75], v[24:27]\n\t"
      "v_add_f32 v72, v72, v19\n\t"
      "ds_write_b64 v11, v[82:83] offset:16384\n\t"
      "v_mfma_f32_16x16x32_bf16 v[24:27], a[236:239], a[76:79], v[24:27]\n\t"
      "v_exp_f32 v20, v20\n\t"
      "v_mfma_f32_16x16x32_bf16 v[24:27], a[240:243], a[80:83], v[24:27]\n\t"
      "v_exp_f32 v21, v21\n\t"
      "v_mfma_f32_16x16x32_bf16 v[24:27], a[244:247], a[84:87], v[24:27]\n\t"
      "v_exp_f32 v22, v22\n\t"
      "v_mfma_f32_16x16x32_bf16 v[24:27], a[248:251], a[88:91], v[24:27]\n\t"
      "buffer_load_dwordx4 v13, s[36:39], s63 offen offset:3072 nt lds\n\t"
      "v_mfma_f32_16x16x32_bf16 v[24:27], a[252:255], a[92:95], v[24:27]\n\t"
      "v_exp_f32 v23, v23\n\t"
      "v_mfma_f32_16x16x32_bf16 v[28:31], a[224:227], a[96:99], v[56:59]\n\t"
      "v_add_f32 v73, v73, v20\n\t"
      "v_cvt_pk_bf16_f32 v84, v20, v21\n\t"
      "v_mfma_f32_16x16x32_bf16 v[28:31], a[228:231], a[100:103], v[28:31]\n\t"
      "v_add_f32 v73, v73, v21\n\t"
      "v_add_f32 v73, v73, v22\n\t"
      "v_mfma_f32_16x16x32_bf16 v[28:31], a[232:235], a[104:107], v[28:31]\n\t"
      "v_cvt_pk_bf16_f32 v85, v22, v23\n\t"
      "v_add_f32 v73, v73, v23\n\t"
      "v_mfma_f32_16x16x32_bf16 v[28:31], a[236:239], a[108:111], v[28:31]\n\t"
      "ds_write_b64 v11, v[84:85] offset:18432\n\t"
      "v_mfma_f32_16x16x32_bf16 v[28:31], a[240:243], a[112:115], v[28:31]\n\t"
      "v_exp_f32 v24, v24\n\t"
      "v_mfma_f32_16x16x32_bf16 v[28:31], a[244:247], a[116:119], v[28:31]\n\t"
      "s_add_u32 m0, s89, 4096\n\t"
      "s_nop 0\n\t"
      "buffer_load_dwordx4 v12, s[36:39], s64 offen offset:0 nt lds\n\t"
      "v_mfma_f32_16x16x32_bf16 v[28:31], a[248:251], a[120:123], v[28:31]\n\t"
      "ds_read_b128 v[88:91], v10 offset:0\n\t"
      "ds_read_b128 v[92:95], v10 offset:1024\n\t"
      "v_mfma_f32_16x16x32_bf16 v[28:31], a[252:255], a[124:127], v[28:31]\n\t"
      "ds_read_b128 v[96:99], v10 offset:2048\n\t"
      "ds_read_b128 v[100:103], v10 offset:3072\n\t"
      "v_mfma_f32_16x16x32_bf16 v[32:35], a[224:227], a[128:131], v[60:63]\n\t"
      "v_exp_f32 v25, v25\n\t"
      "v_mfma_f32_16x16x32_bf16 v[32:35], a[228:231], a[132:135], v[32:35]\n\t"
      "v_exp_f32 v26, v26\n\t"
      "v_mfma_f32_16x16x32_bf16 v[32:35], a[232:235], a[136:139], v[32:35]\n\t"
      "v_exp_f32 v27, v27\n\t"
      "v_mfma_f32_16x16x32_bf16 v[32:35], a[236:239], a[140:143], v[32:35]\n\t"
      "v_add_f32 v74, v74, v24\n\t"
      "v_cvt_pk_bf16_f32 v82, v24, v25\n\t"
      "v_mfma_f32_16x16x32_bf16 v[32:35], a[240:243], a[144:147], v[32:35]\n\t"
      "buffer_load_dwordx4 v13, s[36:39], s65 offen offset:1024 nt lds\n\t"
      "v_add_f32 v74, v74, v25\n\t"
      "v_mfma_f32_16x16x32_bf16 v[32:35], a[244:247], a[148:151], v[32:35]\n\t"
      "v_add_f32 v74, v74, v26\n\t"
      "v_cvt_pk_bf16_f32 v83, v26, v27\n\t"
      "v_mfma_f32_16x16x32_bf16 v[32:35], a[248:251], a[152:155], v[32:35]\n\t"
      "v_add_f32 v74, v74, v27\n\t"
      "ds_write_b64 v11, v[82:83] offset:20480\n\t"
      "v_mfma_f32_16x16x32_bf16 v[32:35], a[252:255], a[156:159], v[32:35]\n\t"
      "v_exp_f32 v28, v28\n\t"
      "v_mfma_f32_16x16x32_bf16 v[36:39], a[224:227], a[160:163], v[64:67]\n\t"
      "v_exp_f32 v29, v29\n\t"
      "v_mfma_f32_16x16x32_bf16 v[36:39], a[228:231], a[164:167], v[36:39]\n\t"
      "v_exp_f32 v30, v30\n\t"
      "v_mfma_f32_16x16x32_bf16 v[36:39], a[232:235], a[168:171], v[36:39]\n\t"
      "s_waitcnt vmcnt(23)\n\t"
      "v_mfma_f32_16x16x32_bf16 v[36:39], a[236:239], a[172:175], v[36:39]\n\t"
      "ds_read_b64_tr_b16 v[104:105], v6 offset:0\n\t"
      "ds_read_b64_tr_b16 v[106:107], v6 offset:2048\n\t"
      "v_mfma_f32_16x16x32_bf16 v[36:39], a[240:243], a[176:179], v[36:39]\n\t"
      "ds_read_b64_tr_b16 v[108:109], v6 offset:4096\n\t"
      "ds_read_b64_tr_b16 v[110:111], v6 offset:6144\n\t"
      "v_mfma_f32_16x16x32_bf16 v[36:39], a[244:247], a[180:183], v[36:39]\n\t"
      "ds_read_b64_tr_b16 v[112:113], v7 offset:0\n\t"
      "ds_read_b64_tr_b16 v[114:115], v7 offset:2048\n\t"
      "v_mfma_f32_16x16x32_bf16 v[36:39], a[248:251], a[184:187], v[36:39]\n\t"
      "ds_read_b64_tr_b16 v[116:117], v7 offset:4096\n\t"
      "ds_read_b64_tr_b16 v[118:119], v7 offset:6144\n\t"
      "v_mfma_f32_16x16x32_bf16 v[36:39], a[252:255], a[188:191], v[36:39]\n\t"
      "ds_read_b64_tr_b16 v[120:121], v8 offset:0\n\t"
      "ds_read_b64_tr_b16 v[122:123], v8 offset:2048\n\t"
      "v_mfma_f32_16x16x32_bf16 v[40:43], a[224:227], a[192:195], v[68:71]\n\t"
      "ds_read_b64_tr_b16 v[124:125], v8 offset:4096\n\t"
      "ds_read_b64_tr_b16 v[126:127], v8 offset:6144\n\t"
      "v_mfma_f32_16x16x32_bf16 v[40:43], a[228:231], a[196:199], v[40:43]\n\t"
      "ds_read_b64_tr_b16 v[128:129], v9 offset:0\n\t"
      "ds_read_b64_tr_b16 v[130:131], v9 offset:2048\n\t"
      "v_mfma_f32_16x16x32_bf16 v[40:43], a[232:235], a[200:203], v[40:43]\n\t"
      "ds_read_b64_tr_b16 v[132:133], v9 offset:4096\n\t"
      "ds_read_b64_tr_b16 v[134:135], v9 offset:6144\n\t"
      "v_mfma_f32_16x16x32_bf16 v[40:43], a[236:239], a[204:207], v[40:43]\n\t"
      "buffer_load_dwordx4 v12, s[36:39], s66 offen offset:2048 nt lds\n\t"
      "buffer_load_dwordx4 v13, s[36:39], s67 offen offset:3072 nt lds\n\t"
      "v_mfma_f32_16x16x32_bf16 v[40:43], a[240:243], a[208:211], v[40:43]\n\t"
      "s_add_u32 s36, s36, 32768\n\t"
      "s_addc_u32 s37, s37, 0\n\t"
      "v_mfma_f32_16x16x32_bf16 v[40:43], a[244:247], a[212:215], v[40:43]\n\t"
      "v_exp_f32 v31, v31\n\t"
      "v_mfma_f32_16x16x32_bf16 v[40:43], a[248:251], a[216:219], v[40:43]\n\t"
      "v_add_f32 v75, v75, v28\n\t"
      "v_cvt_pk_bf16_f32 v84, v28, v29\n\t"
      "v_mfma_f32_16x16x32_bf16 v[40:43], a[252:255], a[220:223], v[40:43]\n\t"
      "v_add_f32 v75, v75, v29\n\t"
      "v_add_f32 v75, v75, v30\n\t"
      "v_cvt_pk_bf16_f32 v85, v30, v31\n\t"
      "v_add_f32 v75, v75, v31\n\t"
      "ds_write_b64 v11, v[84:85] offset:22528\n\t"
      "s_waitcnt lgkmcnt(15)\n\t"
      "v_mfma_f32_16x16x32_bf16 v[136:139], v[104:107], v[88:91], v[136:139]\n\t"
      "v_exp_f32 v32, v32\n\t"
      "s_waitcnt lgkmcnt(13)\n\t"
      "v_mfma_f32_16x16x32_bf16 v[136:139], v[108:111], v[92:95], v[136:139]\n\t"
      "s_waitcnt lgkmcnt(1)\n\t"
      "s_add_u32 m0, s89, 65536\n\t"
      "v_mfma_f32_16x16x32_bf16 v[140:143], v[112:115], v[88:91], v[140:143]\n\t"
      "buffer_load_dwordx4 v14, s[40:43], s68 offen offset:0 nt lds\n\t"
      "v_exp_f32 v33, v33\n\t"
      "v_mfma_f32_16x16x32_bf16 v[140:143], v[116:119], v[92:95], v[140:143]\n\t"
      "v_exp_f32 v34, v34\n\t"
      "v_mfma_f32_16x16x32_bf16 v[144:147], v[120:123], v[88:91], v[144:147]\n\t"
      "v_exp_f32 v35, v35\n\t"
      "v_mfma_f32_16x16x32_bf16 v[144:147], v[124:127], v[92:95], v[144:147]\n\t"
      "v_add_f32 v76, v76, v32\n\t"
      "v_mfma_f32_16x16x32_bf16 v[148:151], v[128:131], v[88:91], v[148:151]\n\t"
      "v_cvt_pk_bf16_f32 v82, v32, v33\n\t"
      "v_add_f32 v76, v76, v33\n\t"
      "v_mfma_f32_16x16x32_bf16 v[148:151], v[132:135], v[92:95], v[148:151]\n\t"
      "ds_read_b128 v[88:91], v10 offset:4096\n\t"
      "ds_read_b128 v[92:95], v10 offset:5120\n\t"
      "v_mfma_f32_16x16x32_bf16 v[152:155], v[104:107], v[96:99], v[152:155]\n\t"
      "buffer_load_dwordx4 v14, s[40:43], s69 offen offset:1024 nt lds\n\t"
      "v_add_f32 v76, v76, v34\n\t"
      "v_mfma_f32_16x16x32_bf16 v[152:155], v[108:111], v[100:103], v[152:155]\n\t"
      "v_cvt_pk_bf16_f32 v83, v34, v35\n\t"
      "v_add_f32 v76, v76, v35\n\t"
      "v_mfma_f32_16x16x32_bf16 v[156:159], v[112:115], v[96:99], v[156:159]\n\t"
      "ds_write_b64 v11, v[82:83] offset:24576\n\t"
      "v_mfma_f32_16x16x32_bf16 v[156:159], v[116:119], v[100:103], v[156:159]\n\t"
      "v_exp_f32 v36, v36\n\t"
      "v_mfma_f32_16x16x32_bf16 v[160:163], v[120:123], v[96:99], v[160:163]\n\t"
      "v_exp_f32 v37, v37\n\t"
      "v_mfma_f32_16x16x32_bf16 v[160:163], v[124:127], v[100:103], v[160:163]\n\t"
      "v_exp_f32 v38, v38\n\t"
      "v_mfma_f32_16x16x32_bf16 v[164:167], v[128:131], v[96:99], v[164:167]\n\t"
      "v_exp_f32 v39, v39\n\t"
      "v_mfma_f32_16x16x32_bf16 v[164:167], v[132:135], v[100:103], v[164:167]\n\t"
      "ds_read_b128 v[96:99], v10 offset:6144\n\t"
      "ds_read_b128 v[100:103], v10 offset:7168\n\t"
      "s_waitcnt lgkmcnt(4)\n\t"
      "v_mfma_f32_16x16x32_bf16 v[168:171], v[104:107], v[88:91], v[168:171]\n\t"
      "buffer_load_dwordx4 v14, s[40:43], s70 offen offset:2048 nt lds\n\t"
      "v_add_f32 v77, v77, v36\n\t"
      "s_waitcnt lgkmcnt(3)\n\t"
      "v_mfma_f32_16x16x32_bf16 v[168:171], v[108:111], v[92:95], v[168:171]\n\t"
      "v_cvt_pk_bf16_f32 v84, v36, v37\n\t"
      "v_add_f32 v77, v77, v37\n\t"
      "v_mfma_f32_16x16x32_bf16 v[172:175], v[112:115], v[88:91], v[172:175]\n\t"
      "v_add_f32 v77, v77, v38\n\t"
      "v_cvt_pk_bf16_f32 v85, v38, v39\n\t"
      "v_mfma_f32_16x16x32_bf16 v[172:175], v[116:119], v[92:95], v[172:175]\n\t"
      "v_add_f32 v77, v77, v39\n\t"
      "ds_write_b64 v11, v[84:85] offset:26624\n\t"
      "v_mfma_f32_16x16x32_bf16 v[176:179], v[120:123], v[88:91], v[176:179]\n\t"
      "v_exp_f32 v40, v40\n\t"
      "v_mfma_f32_16x16x32_bf16 v[176:179], v[124:127], v[92:95], v[176:179]\n\t"
      "v_exp_f32 v41, v41\n\t"
      "v_mfma_f32_16x16x32_bf16 v[180:183], v[128:131], v[88:91], v[180:183]\n\t"
      "buffer_load_dwordx4 v14, s[40:43], s71 offen offset:3072 nt lds\n\t"
      "v_mfma_f32_16x16x32_bf16 v[180:183], v[132:135], v[92:95], v[180:183]\n\t"
      "ds_read_b128 v[88:91], v10 offset:8192\n\t"
      "ds_read_b128 v[92:95], v10 offset:9216\n\t"
      "s_waitcnt lgkmcnt(4)\n\t"
      "v_mfma_f32_16x16x32_bf16 v[184:187], v[104:107], v[96:99], v[184:187]\n\t"
      "v_exp_f32 v42, v42\n\t"
      "s_waitcnt lgkmcnt(3)\n\t"
      "v_mfma_f32_16x16x32_bf16 v[184:187], v[108:111], v[100:103], v[184:187]\n\t"
      "v_exp_f32 v43, v43\n\t"
      "v_mfma_f32_16x16x32_bf16 v[188:191], v[112:115], v[96:99], v[188:191]\n\t"
      "v_add_f32 v78, v78, v40\n\t"
      "v_cvt_pk_bf16_f32 v82, v40, v41\n\t"
      "v_mfma_f32_16x16x32_bf16 v[188:191], v[116:119], v[100:103], v[188:191]\n\t"
      "v_add_f32 v78, v78, v41\n\t"
      "v_add_f32 v78, v78, v42\n\t"
      "v_mfma_f32_16x16x32_bf16 v[192:195], v[120:123], v[96:99], v[192:195]\n\t"
      "v_cvt_pk_bf16_f32 v83, v42, v43\n\t"
      "v_add_f32 v78, v78, v43\n\t"
      "v_mfma_f32_16x16x32_bf16 v[192:195], v[124:127], v[100:103], v[192:195]\n\t"
      "s_add_u32 m0, s89, 69632\n\t"
      "s_nop 0\n\t"
      "buffer_load_dwordx4 v14, s[40:43], s72 offen offset:0 nt lds\n\t"
      "v_mfma_f32_16x16x32_bf16 v[196:199], v[128:131], v[96:99], v[196:199]\n\t"
      "ds_write_b64 v11, v[82:83] offset:28672\n\t"
      "v_mfma_f32_16x16x32_bf16 v[196:199], v[132:135], v[100:103], v[196:199]\n\t"
      "ds_read_b128 v[96:99], v10 offset:10240\n\t"
      "ds_read_b128 v[100:103], v10 offset:11264\n\t"
      "s_waitcnt lgkmcnt(4)\n\t"
      "v_mfma_f32_16x16x32_bf16 v[200:203], v[104:107], v[88:91], v[200:203]\n\t"
      "s_waitcnt lgkmcnt(3)\n\t"
      "v_mfma_f32_16x16x32_bf16 v[200:203], v[108:111], v[92:95], v[200:203]\n\t"
      "v_mfma_f32_16x16x32_bf16 v[204:207], v[112:115], v[88:91], v[204:207]\n\t"
      "v_mfma_f32_16x16x32_bf16 v[204:207], v[116:119], v[92:95], v[204:207]\n\t"
      "v_mfma_f32_16x16x32_bf16 v[208:211], v[120:123], v[88:91], v[208:211]\n\t"
      "buffer_load_dwordx4 v14, s[40:43], s73 offen offset:1024 nt lds\n\t"
      "v_mfma_f32_16x16x32_bf16 v[208:211], v[124:127], v[92:95], v[208:211]\n\t"
      "v_mfma_f32_16x16x32_bf16 v[212:215], v[128:131], v[88:91], v[212:215]\n\t"
      "v_mfma_f32_16x16x32_bf16 v[212:215], v[132:135], v[92:95], v[212:215]\n\t"
      "ds_read_b128 v[88:91], v10 offset:12288\n\t"
      "ds_read_b128 v[92:95], v10 offset:13312\n\t"
      "s_waitcnt lgkmcnt(3)\n\t"
      "v_mfma_f32_16x16x32_bf16 v[216:219], v[104:107], v[96:99], v[216:219]\n\t"
      "s_waitcnt lgkmcnt(2)\n\t"
      "v_mfma_f32_16x16x32_bf16 v[216:219], v[108:111], v[100:103], v[216:219]\n\t"
      "v_mfma_f32_16x16x32_bf16 v[220:223], v[112:115], v[96:99], v[220:223]\n\t"
      "v_mfma_f32_16x16x32_bf16 v[220:223], v[116:119], v[100:103], v[220:223]\n\t"
      "buffer_load_dwordx4 v14, s[40:43], s74 offen offset:2048 nt lds\n\t"
      "v_mfma_f32_16x16x32_bf16 v[224:227], v[120:123], v[96:99], v[224:227]\n\t"
      "s_waitcnt vmcnt(23)\n\t"
      "ds_read_b128 a[224:227], v4 offset:32768\n\t"
      "v_mfma_f32_16x16x32_bf16 v[224:227], v[124:127], v[100:103], v[224:227]\n\t"
      "ds_read_b128 a[228:231], v5 offset:32768\n\t"
      "ds_read_b128 a[232:235], v4 offset:34816\n\t"
      "v_mfma_f32_16x16x32_bf16 v[228:231], v[128:131], v[96:99], v[228:231]\n\t"
      "ds_read_b128 a[236:239], v5 offset:34816\n\t"
      "ds_read_b128 a[240:243], v4 offset:36864\n\t"
      "v_mfma_f32_16x16x32_bf16 v[228:231], v[132:135], v[100:103], v[228:231]\n\t"
      "ds_read_b128 a[244:247], v5 offset:36864\n\t"
      "ds_read_b128 a[248:251], v4 offset:38912\n\t"
      "s_waitcnt lgkmcnt(8)\n\t"
      "v_mfma_f32_16x16x32_bf16 v[232:235], v[104:107], v[88:91], v[232:235]\n\t"
      "ds_read_b128 a[252:255], v5 offset:38912\n\t"
      "s_waitcnt lgkmcnt(8)\n\t"
      "v_mfma_f32_16x16x32_bf16 v[232:235], v[108:111], v[92:95], v[232:235]\n\t"
      "v_mfma_f32_16x16x32_bf16 v[236:239], v[112:115], v[88:91], v[236:239]\n\t"
      "buffer_load_dwordx4 v14, s[40:43], s75 offen offset:3072 nt lds\n\t"
      "s_add_u32 s40, s40, 32768\n\t"
      "v_mfma_f32_16x16x32_bf16 v[236:239], v[116:119], v[92:95], v[236:239]\n\t"
      "s_addc_u32 s41, s41, 0\n\t"
      "v_mfma_f32_16x16x32_bf16 v[240:243], v[120:123], v[88:91], v[240:243]\n\t"
      "v_lshlrev_b32 v251, 1, v250\n\t"
      "v_mov_b32 v249, v86\n\t"
      "s_mov_b32 s46, s29\n\t"
      "buffer_store_dwordx2 v[248:249], v251, s[44:47], 0 offen offset:1024 sc1\n\t"
      "v_mfma_f32_16x16x32_bf16 v[240:243], v[124:127], v[92:95], v[240:243]\n\t"
      "v_mfma_f32_16x16x32_bf16 v[244:247], v[128:131], v[88:91], v[244:247]\n\t"
      "v_mfma_f32_16x16x32_bf16 v[244:247], v[132:135], v[92:95], v[244:247]\n\t"
      "s_waitcnt lgkmcnt(0)\n\t"
      "s_barrier\n\t"
      "s_sub_u32 s91, s91, 1\n\t"
      "s_cmp_lg_u32 s91, 0\n\t"
      "s_cbranch_scc1 TLOOP%=\n\t"
      "v_mbcnt_lo_u32_b32 v0, -1, 0\n\t"
      "v_mbcnt_hi_u32_b32 v0, -1, v0\n\t"
      "v_and_b32 v1, 15, v0\n\t"
      "v_lshrrev_b32 v2, 4, v0\n\t"
      "v_lshrrev_b32 v248, 3, v0\n\t"
      "v_and_b32 v249, 7, v0\n\t"
      "v_lshlrev_b32 v3, 1, v2\n\t"
      "v_xor_b32 v3, v3, v249\n\t"
      "v_lshlrev_b32 v3, 4, v3\n\t"
      "v_mul_u32_u24 v248, 896, v248\n\t"
      "v_add_u32 v254, v248, v3\n\t"
      "s_mul_i32 s94, s88, 57344\n\t"
      "s_add_u32 s76, s94, 0\n\t"
      "s_add_u32 s77, s94, 6144\n\t"
      "s_add_u32 s78, s94, 12288\n\t"
      "s_add_u32 s79, s94, 18432\n\t"
      "s_add_u32 s80, s94, 28672\n\t"
      "s_add_u32 s81, s94, 34816\n\t"
      "s_add_u32 s82, s94, 40960\n\t"
      "s_add_u32 s83, s94, 47104\n\t"
      "s_mov_b32 s97, s16\n\t"
      "s_and_b32 s98, s17, 0xffff\n\t"
      "s_lshl_b32 s94, s7, 3\n\t"
      "s_add_u32 s94, s94, 1024\n\t"
      "v_mov_b32 v87, s94\n\t"
      "s_mov_b32 s46, s28\n\t"
      "buffer_load_dwordx2 v[250:251], v87, s[44:47], 0 offen offset:0 sc1\n\t"
      "v_mfma_f32_16x16x32_bf16 v[16:19], a[224:227], a[0:3], v[44:47]\n\t"
      "v_mfma_f32_16x16x32_bf16 v[16:19], a[228:231], a[4:7], v[16:19]\n\t"
      "v_mfma_f32_16x16x32_bf16 v[16:19], a[232:235], a[8:11], v[16:19]\n\t"
      "v_mfma_f32_16x16x32_bf16 v[16:19], a[236:239], a[12:15], v[16:19]\n\t"
      "v_mfma_f32_16x16x32_bf16 v[16:19], a[240:243], a[16:19], v[16:19]\n\t"
      "v_mfma_f32_16x16x32_bf16 v[16:19], a[244:247], a[20:23], v[16:19]\n\t"
      "v_mfma_f32_16x16x32_bf16 v[16:19], a[248:251], a[24:27], v[16:19]\n\t"
      "v_mfma_f32_16x16x32_bf16 v[16:19], a[252:255], a[28:31], v[16:19]\n\t"
      "v_mfma_f32_16x16x32_bf16 v[20:23], a[224:227], a[32:35], v[48:51]\n\t"
      "v_mfma_f32_16x16x32_bf16 v[20:23], a[228:231], a[36:39], v[20:23]\n\t"
      "v_mfma_f32_16x16x32_bf16 v[20:23], a[232:235], a[40:43], v[20:23]\n\t"
      "v_mfma_f32_16x16x32_bf16 v[20:23], a[236:239], a[44:47], v[20:23]\n\t"
      "s_nop 3\n\t"
      "v_exp_f32 v16, v16\n\t"
      "v_mfma_f32_16x16x32_bf16 v[20:23], a[240:243], a[48:51], v[20:23]\n\t"
      "v_exp_f32 v17, v17\n\t"
      "v_mfma_f32_16x16x32_bf16 v[20:23], a[244:247], a[52:55], v[20:23]\n\t"
      "v_exp_f32 v18, v18\n\t"
      "v_mfma_f32_16x16x32_bf16 v[20:23], a[248:251], a[56:59], v[20:23]\n\t"
      "v_exp_f32 v19, v19\n\t"
      "v_mfma_f32_16x16x32_bf16 v[20:23], a[252:255], a[60:63], v[20:23]\n\t"
      "v_add_f32 v72, v72, v16\n\t"
      "v_cvt_pk_bf16_f32 v82, v16, v17\n\t"
      "v_mfma_f32_16x16x32_bf16 v[24:27], a[224:227], a[64:67], v[52:55]\n\t"
      "v_add_f32 v72, v72, v17\n\t"
      "v_add_f32 v72, v72, v18\n\t"
      "v_mfma_f32_16x16x32_bf16 v[24:27], a[228:231], a[68:71], v[24:27]\n\t"
      "v_cvt_pk_bf16_f32 v83, v18, v19\n\t"
      "v_add_f32 v72, v72, v19\n\t"
      "v_mfma_f32_16x16x32_bf16 v[24:27], a[232:235], a[72:75], v[24:27]\n\t"
      "ds_write_b64 v11, v[82:83] offset:0\n\t"
      "v_mfma_f32_16x16x32_bf16 v[24:27], a[236:239], a[76:79], v[24:27]\n\t"
      "v_exp_f32 v20, v20\n\t"
      "v_mfma_f32_16x16x32_bf16 v[24:27], a[240:243], a[80:83], v[24:27]\n\t"
      "v_exp_f32 v21, v21\n\t"
      "v_mfma_f32_16x16x32_bf16 v[24:27], a[244:247], a[84:87], v[24:27]\n\t"
      "v_exp_f32 v22, v22\n\t"
      "v_mfma_f32_16x16x32_bf16 v[24:27], a[248:251], a[88:91], v[24:27]\n\t"
      "v_exp_f32 v23, v23\n\t"
      "v_mfma_f32_16x16x32_bf16 v[24:27], a[252:255], a[92:95], v[24:27]\n\t"
      "v_add_f32 v73, v73, v20\n\t"
      "v_cvt_pk_bf16_f32 v84, v20, v21\n\t"
      "v_mfma_f32_16x16x32_bf16 v[28:31], a[224:227], a[96:99], v[56:59]\n\t"
      "v_add_f32 v73, v73, v21\n\t"
      "v_add_f32 v73, v73, v22\n\t"
      "v_mfma_f32_16x16x32_bf16 v[28:31], a[228:231], a[100:103], v[28:31]\n\t"
      "v_cvt_pk_bf16_f32 v85, v22, v23\n\t"
      "v_add_f32 v73, v73, v23\n\t"
      "v_mfma_f32_16x16x32_bf16 v[28:31], a[232:235], a[104:107], v[28:31]\n\t"
      "ds_write_b64 v11, v[84:85] offset:2048\n\t"
      "v_mfma_f32_16x16x32_bf16 v[28:31], a[236:239], a[108:111], v[28:31]\n\t"
      "v_exp_f32 v24, v24\n\t"
      "v_mfma_f32_16x16x32_bf16 v[28:31], a[240:243], a[112:115], v[28:31]\n\t"
      "v_exp_f32 v25, v25\n\t"
      "v_mfma_f32_16x16x32_bf16 v[28:31], a[244:247], a[116:119], v[28:31]\n\t"
      "v_exp_f32 v26, v26\n\t"
      "v_mfma_f32_16x16x32_bf16 v[28:31], a[248:251], a[120:123], v[28:31]\n\t"
      "ds_read_b128 v[88:91], v10 offset:16384\n\t"
      "ds_read_b128 v[92:95], v10 offset:17408\n\t"
      "v_mfma_f32_16x16x32_bf16 v[28:31], a[252:255], a[124:127], v[28:31]\n\t"
      "ds_read_b128 v[96:99], v10 offset:18432\n\t"
      "ds_read_b128 v[100:103], v10 offset:19456\n\t"
      "v_mfma_f32_16x16x32_bf16 v[32:35], a[224:227], a[128:131], v[60:63]\n\t"
      "v_exp_f32 v27, v27\n\t"
      "v_mfma_f32_16x16x32_bf16 v[32:35], a[228:231], a[132:135], v[32:35]\n\t"
      "v_add_f32 v74, v74, v24\n\t"
      "v_cvt_pk_bf16_f32 v82, v24, v25\n\t"
      "v_mfma_f32_16x16x32_bf16 v[32:35], a[232:235], a[136:139], v[32:35]\n\t"
      "v_add_f32 v74, v74, v25\n\t"
      "v_add_f32 v74, v74, v26\n\t"
      "v_mfma_f32_16x16x32_bf16 v[32:35], a[236:239], a[140:143], v[32:35]\n\t"
      "v_cvt_pk_bf16_f32 v83, v26, v27\n\t"
      "v_add_f32 v74, v74, v27\n\t"
      "v_mfma_f32_16x16x32_bf16 v[32:35], a[240:243], a[144:147], v[32:35]\n\t"
      "ds_write_b64 v11, v[82:83] offset:4096\n\t"
      "v_mfma_f32_16x16x32_bf16 v[32:35], a[244:247], a[148:151], v[32:35]\n\t"
      "v_exp_f32 v28, v28\n\t"
      "v_mfma_f32_16x16x32_bf16 v[32:35], a[248:251], a[152:155], v[32:35]\n\t"
      "v_exp_f32 v29, v29\n\t"
      "v_mfma_f32_16x16x32_bf16 v[32:35], a[252:255], a[156:159], v[32:35]\n\t"
      "v_exp_f32 v30, v30\n\t"
      "v_mfma_f32_16x16x32_bf16 v[36:39], a[224:227], a[160:163], v[64:67]\n\t"
      "v_exp_f32 v31, v31\n\t"
      "v_mfma_f32_16x16x32_bf16 v[36:39], a[228:231], a[164:167], v[36:39]\n\t"
      "v_add_f32 v75, v75, v28\n\t"
      "v_cvt_pk_bf16_f32 v84, v28, v29\n\t"
      "v_mfma_f32_16x16x32_bf16 v[36:39], a[232:235], a[168:171], v[36:39]\n\t"
      "s_waitcnt vmcnt(18)\n\t"
      "ds_read_b64_tr_b16 v[104:105], v6 offset:32768\n\t"
      "v_mfma_f32_16x16x32_bf16 v[36:39], a[236:239], a[172:175], v[36:39]\n\t"
      "ds_read_b64_tr_b16 v[106:107], v6 offset:34816\n\t"
      "ds_read_b64_tr_b16 v[108:109], v6 offset:36864\n\t"
      "v_mfma_f32_16x16x32_bf16 v[36:39], a[240:243], a[176:179], v[36:39]\n\t"
      "ds_read_b64_tr_b16 v[110:111], v6 offset:38912\n\t"
      "ds_read_b64_tr_b16 v[112:113], v7 offset:32768\n\t"
      "v_mfma_f32_16x16x32_bf16 v[36:39], a[244:247], a[180:183], v[36:39]\n\t"
      "ds_read_b64_tr_b16 v[114:115], v7 offset:34816\n\t"
      "ds_read_b64_tr_b16 v[116:117], v7 offset:36864\n\t"
      "v_mfma_f32_16x16x32_bf16 v[36:39], a[248:251], a[184:187], v[36:39]\n\t"
      "ds_read_b64_tr_b16 v[118:119], v7 offset:38912\n\t"
      "ds_read_b64_tr_b16 v[120:121], v8 offset:32768\n\t"
      "v_mfma_f32_16x16x32_bf16 v[36:39], a[252:255], a[188:191], v[36:39]\n\t"
      "ds_read_b64_tr_b16 v[122:123], v8 offset:34816\n\t"
      "ds_read_b64_tr_b16 v[124:125], v8 offset:36864\n\t"
      "v_mfma_f32_16x16x32_bf16 v[40:43], a[224:227], a[192:195], v[68:71]\n\t"
      "ds_read_b64_tr_b16 v[126:127], v8 offset:38912\n\t"
      "ds_read_b64_tr_b16 v[128:129], v9 offset:32768\n\t"
      "v_mfma_f32_16x16x32_bf16 v[40:43], a[228:231], a[196:199], v[40:43]\n\t"
      "ds_read_b64_tr_b16 v[130:131], v9 offset:34816\n\t"
      "ds_read_b64_tr_b16 v[132:133], v9 offset:36864\n\t"
      "v_mfma_f32_16x16x32_bf16 v[40:43], a[232:235], a[200:203], v[40:43]\n\t"
      "ds_read_b64_tr_b16 v[134:135], v9 offset:38912\n\t"
      "v_add_f32 v75, v75, v29\n\t"
      "v_mfma_f32_16x16x32_bf16 v[40:43], a[236:239], a[204:207], v[40:43]\n\t"
      "v_add_f32 v75, v75, v30\n\t"
      "v_cvt_pk_bf16_f32 v85, v30, v31\n\t"
      "v_mfma_f32_16x16x32_bf16 v[40:43], a[240:243], a[208:211], v[40:43]\n\t"
      "v_add_f32 v75, v75, v31\n\t"
      "ds_write_b64 v11, v[84:85] offset:6144\n\t"
      "v_mfma_f32_16x16x32_bf16 v[40:43], a[244:247], a[212:215], v[40:43]\n\t"
      "v_mfma_f32_16x16x32_bf16 v[40:43], a[248:251], a[216:219], v[40:43]\n\t"
      "v_mfma_f32_16x16x32_bf16 v[40:43], a[252:255], a[220:223], v[40:43]\n\t"
      "s_waitcnt lgkmcnt(15)\n\t"
      "v_mfma_f32_16x16x32_bf16 v[136:139], v[104:107], v[88:91], v[136:139]\n\t"
      "v_exp_f32 v32, v32\n\t"
      "s_waitcnt lgkmcnt(13)\n\t"
      "v_mfma_f32_16x16x32_bf16 v[136:139], v[108:111], v[92:95], v[136:139]\n\t"
      "s_waitcnt lgkmcnt(1)\n\t"
      "s_add_u32 m0, s89, 98304\n\t"
      "v_mfma_f32_16x16x32_bf16 v[140:143], v[112:115], v[88:91], v[140:143]\n\t"
      "buffer_load_dwordx4 v14, s[40:43], s68 offen offset:0 nt lds\n\t"
      "v_exp_f32 v33, v33\n\t"
      "v_mfma_f32_16x16x32_bf16 v[140:143], v[116:119], v[92:95], v[140:143]\n\t"
      "v_exp_f32 v34, v34\n\t"
      "v_mfma_f32_16x16x32_bf16 v[144:147], v[120:123], v[88:91], v[144:147]\n\t"
      "v_exp_f32 v35, v35\n\t"
      "v_mfma_f32_16x16x32_bf16 v[144:147], v[124:127], v[92:95], v[144:147]\n\t"
      "v_add_f32 v76, v76, v32\n\t"
      "v_mfma_f32_16x16x32_bf16 v[148:151], v[128:131], v[88:91], v[148:151]\n\t"
      "v_cvt_pk_bf16_f32 v82, v32, v33\n\t"
      "v_add_f32 v76, v76, v33\n\t"
      "v_mfma_f32_16x16x32_bf16 v[148:151], v[132:135], v[92:95], v[148:151]\n\t"
      "ds_read_b128 v[88:91], v10 offset:20480\n\t"
      "ds_read_b128 v[92:95], v10 offset:21504\n\t"
      "v_mfma_f32_16x16x32_bf16 v[152:155], v[104:107], v[96:99], v[152:155]\n\t"
      "buffer_load_dwordx4 v14, s[40:43], s69 offen offset:1024 nt lds\n\t"
      "v_add_f32 v76, v76, v34\n\t"
      "v_mfma_f32_16x16x32_bf16 v[152:155], v[108:111], v[100:103], v[152:155]\n\t"
      "v_cvt_pk_bf16_f32 v83, v34, v35\n\t"
      "v_add_f32 v76, v76, v35\n\t"
      "v_mfma_f32_16x16x32_bf16 v[156:159], v[112:115], v[96:99], v[156:159]\n\t"
      "ds_write_b64 v11, v[82:83] offset:8192\n\t"
      "v_mfma_f32_16x16x32_bf16 v[156:159], v[116:119], v[100:103], v[156:159]\n\t"
      "v_exp_f32 v36, v36\n\t"
      "v_mfma_f32_16x16x32_bf16 v[160:163], v[120:123], v[96:99], v[160:163]\n\t"
      "v_exp_f32 v37, v37\n\t"
      "v_mfma_f32_16x16x32_bf16 v[160:163], v[124:127], v[100:103], v[160:163]\n\t"
      "v_exp_f32 v38, v38\n\t"
      "v_mfma_f32_16x16x32_bf16 v[164:167], v[128:131], v[96:99], v[164:167]\n\t"
      "v_exp_f32 v39, v39\n\t"
      "v_mfma_f32_16x16x32_bf16 v[164:167], v[132:135], v[100:103], v[164:167]\n\t"
      "ds_read_b128 v[96:99], v10 offset:22528\n\t"
      "ds_read_b128 v[100:103], v10 offset:23552\n\t"
      "s_waitcnt lgkmcnt(4)\n\t"
      "v_mfma_f32_16x16x32_bf16 v[168:171], v[104:107], v[88:91], v[168:171]\n\t"
      "buffer_load_dwordx4 v14, s[40:43], s70 offen offset:2048 nt lds\n\t"
      "v_add_f32 v77, v77, v36\n\t"
      "s_waitcnt lgkmcnt(3)\n\t"
      "v_mfma_f32_16x16x32_bf16 v[168:171], v[108:111], v[92:95], v[168:171]\n\t"
      "v_cvt_pk_bf16_f32 v84, v36, v37\n\t"
      "v_add_f32 v77, v77, v37\n\t"
      "v_mfma_f32_16x16x32_bf16 v[172:175], v[112:115], v[88:91], v[172:175]\n\t"
      "v_add_f32 v77, v77, v38\n\t"
      "v_cvt_pk_bf16_f32 v85, v38, v39\n\t"
      "v_mfma_f32_16x16x32_bf16 v[172:175], v[116:119], v[92:95], v[172:175]\n\t"
      "v_add_f32 v77, v77, v39\n\t"
      "ds_write_b64 v11, v[84:85] offset:10240\n\t"
      "v_mfma_f32_16x16x32_bf16 v[176:179], v[120:123], v[88:91], v[176:179]\n\t"
      "v_exp_f32 v40, v40\n\t"
      "v_mfma_f32_16x16x32_bf16 v[176:179], v[124:127], v[92:95], v[176:179]\n\t"
      "v_exp_f32 v41, v41\n\t"
      "v_mfma_f32_16x16x32_bf16 v[180:183], v[128:131], v[88:91], v[180:183]\n\t"
      "buffer_load_dwordx4 v14, s[40:43], s71 offen offset:3072 nt lds\n\t"
      "v_mfma_f32_16x16x32_bf16 v[180:183], v[132:135], v[92:95], v[180:183]\n\t"
      "ds_read_b128 v[88:91], v10 offset:24576\n\t"
      "ds_read_b128 v[92:95], v10 offset:25600\n\t"
      "s_waitcnt lgkmcnt(4)\n\t"
      "v_mfma_f32_16x16x32_bf16 v[184:187], v[104:107], v[96:99], v[184:187]\n\t"
      "v_exp_f32 v42, v42\n\t"
      "s_waitcnt lgkmcnt(3)\n\t"
      "v_mfma_f32_16x16x32_bf16 v[184:187], v[108:111], v[100:103], v[184:187]\n\t"
      "v_exp_f32 v43, v43\n\t"
      "v_mfma_f32_16x16x32_bf16 v[188:191], v[112:115], v[96:99], v[188:191]\n\t"
      "v_add_f32 v78, v78, v40\n\t"
      "v_cvt_pk_bf16_f32 v82, v40, v41\n\t"
      "v_mfma_f32_16x16x32_bf16 v[188:191], v[116:119], v[100:103], v[188:191]\n\t"
      "v_add_f32 v78, v78, v41\n\t"
      "v_add_f32 v78, v78, v42\n\t"
      "v_mfma_f32_16x16x32_bf16 v[192:195], v[120:123], v[96:99], v[192:195]\n\t"
      "v_cvt_pk_bf16_f32 v83, v42, v43\n\t"
      "v_add_f32 v78, v78, v43\n\t"
      "v_mfma_f32_16x16x32_bf16 v[192:195], v[124:127], v[100:103], v[192:195]\n\t"
      "s_add_u32 m0, s89, 102400\n\t"
      "s_nop 0\n\t"
      "buffer_load_dwordx4 v14, s[40:43], s72 offen offset:0 nt lds\n\t"
      "v_mfma_f32_16x16x32_bf16 v[196:199], v[128:131], v[96:99], v[196:199]\n\t"
      "ds_write_b64 v11, v[82:83] offset:12288\n\t"
      "v_mfma_f32_16x16x32_bf16 v[196:199], v[132:135], v[100:103], v[196:199]\n\t"
      "ds_read_b128 v[96:99], v10 offset:26624\n\t"
      "ds_read_b128 v[100:103], v10 offset:27648\n\t"
      "s_waitcnt lgkmcnt(4)\n\t"
      "v_mfma_f32_16x16x32_bf16 v[200:203], v[104:107], v[88:91], v[200:203]\n\t"
      "s_waitcnt lgkmcnt(3)\n\t"
      "v_mfma_f32_16x16x32_bf16 v[200:203], v[108:111], v[92:95], v[200:203]\n\t"
      "v_mfma_f32_16x16x32_bf16 v[204:207], v[112:115], v[88:91], v[204:207]\n\t"
      "v_mfma_f32_16x16x32_bf16 v[204:207], v[116:119], v[92:95], v[204:207]\n\t"
      "v_mfma_f32_16x16x32_bf16 v[208:211], v[120:123], v[88:91], v[208:211]\n\t"
      "buffer_load_dwordx4 v14, s[40:43], s73 offen offset:1024 nt lds\n\t"
      "v_mfma_f32_16x16x32_bf16 v[208:211], v[124:127], v[92:95], v[208:211]\n\t"
      "v_mfma_f32_16x16x32_bf16 v[212:215], v[128:131], v[88:91], v[212:215]\n\t"
      "v_mfma_f32_16x16x32_bf16 v[212:215], v[132:135], v[92:95], v[212:215]\n\t"
      "ds_read_b128 v[88:91], v10 offset:28672\n\t"
      "ds_read_b128 v[92:95], v10 offset:29696\n\t"
      "s_waitcnt lgkmcnt(3)\n\t"
      "v_mfma_f32_16x16x32_bf16 v[216:219], v[104:107], v[96:99], v[216:219]\n\t"
      "s_waitcnt lgkmcnt(2)\n\t"
      "v_mfma_f32_16x16x32_bf16 v[216:219], v[108:111], v[100:103], v[216:219]\n\t"
      "v_mfma_f32_16x16x32_bf16 v[220:223], v[112:115], v[96:99], v[220:223]\n\t"
      "v_mfma_f32_16x16x32_bf16 v[220:223], v[116:119], v[100:103], v[220:223]\n\t"
      "buffer_load_dwordx4 v14, s[40:43], s74 offen offset:2048 nt lds\n\t"
      "v_mfma_f32_16x16x32_bf16 v[224:227], v[120:123], v[96:99], v[224:227]\n\t"
      "s_waitcnt vmcnt(17)\n\t"
      "ds_read_b128 a[224:227], v4 offset:0\n\t"
      "v_mfma_f32_16x16x32_bf16 v[224:227], v[124:127], v[100:103], v[224:227]\n\t"
      "ds_read_b128 a[228:231], v5 offset:0\n\t"
      "ds_read_b128 a[232:235], v4 offset:2048\n\t"
      "v_mfma_f32_16x16x32_bf16 v[228:231], v[128:131], v[96:99], v[228:231]\n\t"
      "ds_read_b128 a[236:239], v5 offset:2048\n\t"
      "ds_read_b128 a[240:243], v4 offset:4096\n\t"
      "v_mfma_f32_16x16x32_bf16 v[228:231], v[132:135], v[100:103], v[228:231]\n\t"
      "ds_read_b128 a[244:247], v5 offset:4096\n\t"
      "ds_read_b128 a[248:251], v4 offset:6144\n\t"
      "s_waitcnt lgkmcnt(8)\n\t"
      "v_mfma_f32_16x16x32_bf16 v[232:235], v[104:107], v[88:91], v[232:235]\n\t"
      "ds_read_b128 a[252:255], v5 offset:6144\n\t"
      "s_waitcnt lgkmcnt(8)\n\t"
      "v_mfma_f32_16x16x32_bf16 v[232:235], v[108:111], v[92:95], v[232:235]\n\t"
      "v_mfma_f32_16x16x32_bf16 v[236:239], v[112:115], v[88:91], v[236:239]\n\t"
      "buffer_load_dwordx4 v14, s[40:43], s75 offen offset:3072 nt lds\n\t"
      "s_add_u32 s40, s40, 32768\n\t"
      "v_mfma_f32_16x16x32_bf16 v[236:239], v[116:119], v[92:95], v[236:239]\n\t"
      "s_addc_u32 s41, s41, 0\n\t"
      "v_mfma_f32_16x16x32_bf16 v[240:243], v[120:123], v[88:91], v[240:243]\n\t"
      "v_mfma_f32_16x16x32_bf16 v[240:243], v[124:127], v[92:95], v[240:243]\n\t"
      "v_mfma_f32_16x16x32_bf16 v[244:247], v[128:131], v[88:91], v[244:247]\n\t"
      "v_mfma_f32_16x16x32_bf16 v[244:247], v[132:135], v[92:95], v[244:247]\n\t"
      "s_waitcnt lgkmcnt(0)\n\t"
      "s_barrier\n\t"
      "v_mfma_f32_16x16x32_bf16 v[16:19], a[224:227], a[0:3], v[44:47]\n\t"
      "v_mfma_f32_16x16x32_bf16 v[16:19], a[228:231], a[4:7], v[16:19]\n\t"
      "s_lshl_b32 s92, s100, 7\n\t"
      "s_add_u32 s52, s97, s92\n\t"
      "v_mfma_f32_16x16x32_bf16 v[16:19], a[232:235], a[8:11], v[16:19]\n\t"
      "s_addc_u32 s53, s98, 0\n\t"
      "s_add_u32 m0, s89, 32768\n\t"
      "v_mfma_f32_16x16x32_bf16 v[16:19], a[236:239], a[12:15], v[16:19]\n\t"
      "buffer_load_dwordx4 v254, s[52:55], s76 offen offset:0 lds\n\t"
      "v_mfma_f32_16x16x32_bf16 v[16:19], a[240:243], a[16:19], v[16:19]\n\t"
      "v_mfma_f32_16x16x32_bf16 v[16:19], a[244:247], a[20:23], v[16:19]\n\t"
      "buffer_load_dwordx4 v254, s[52:55], s77 offen offset:1024 lds\n\t"
      "v_mfma_f32_16x16x32_bf16 v[16:19], a[248:251], a[24:27], v[16:19]\n\t"
      "v_mfma_f32_16x16x32_bf16 v[16:19], a[252:255], a[28:31], v[16:19]\n\t"
      "v_mfma_f32_16x16x32_bf16 v[20:23], a[224:227], a[32:35], v[48:51]\n\t"
      "v_mfma_f32_16x16x32_bf16 v[20:23], a[228:231], a[36:39], v[20:23]\n\t"
      "buffer_load_dwordx4 v254, s[52:55], s78 offen offset:2048 lds\n\t"
      "v_mfma_f32_16x16x32_bf16 v[20:23], a[232:235], a[40:43], v[20:23]\n\t"
      "v_mfma_f32_16x16x32_bf16 v[20:23], a[236:239], a[44:47], v[20:23]\n\t"
      "s_nop 2\n\t"
      "v_exp_f32 v16, v16\n\t"
      "v_mfma_f32_16x16x32_bf16 v[20:23], a[240:243], a[48:51], v[20:23]\n\t"
      "v_exp_f32 v17, v17\n\t"
      "v_mfma_f32_16x16x32_bf16 v[20:23], a[244:247], a[52:55], v[20:23]\n\t"
      "buffer_load_dwordx4 v254, s[52:55], s79 offen offset:3072 lds\n\t"
      "v_exp_f32 v18, v18\n\t"
      "v_mfma_f32_16x16x32_bf16 v[20:23], a[248:251], a[56:59], v[20:23]\n\t"
      "v_exp_f32 v19, v19\n\t"
      "v_mfma_f32_16x16x32_bf16 v[20:23], a[252:255], a[60:63], v[20:23]\n\t"
      "v_add_f32 v72, v72, v16\n\t"
      "v_mfma_f32_16x16x32_bf16 v[24:27], a[224:227], a[64:67], v[52:55]\n\t"
      "v_cvt_pk_bf16_f32 v82, v16, v17\n\t"
      "v_add_f32 v72, v72, v17\n\t"
      "v_mfma_f32_16x16x32_bf16 v[24:27], a[228:231], a[68:71], v[24:27]\n\t"
      "s_add_u32 m0, s89, 36864\n\t"
      "s_nop 0\n\t"
      "buffer_load_dwordx4 v254, s[52:55], s80 offen offset:0 lds\n\t"
      "v_mfma_f32_16x16x32_bf16 v[24:27], a[232:235], a[72:75], v[24:27]\n\t"
      "v_add_f32 v72, v72, v18\n\t"
      "v_cvt_pk_bf16_f32 v83, v18, v19\n\t"
      "v_mfma_f32_16x16x32_bf16 v[24:27], a[236:239], a[76:79], v[24:27]\n\t"
      "v_add_f32 v72, v72, v19\n\t"
      "ds_write_b64 v11, v[82:83] offset:16384\n\t"
      "v_mfma_f32_16x16x32_bf16 v[24:27], a[240:243], a[80:83], v[24:27]\n\t"
      "v_exp_f32 v20, v20\n\t"
      "v_mfma_f32_16x16x32_bf16 v[24:27], a[244:247], a[84:87], v[24:27]\n\t"
      "buffer_load_dwordx4 v254, s[52:55], s81 offen offset:1024 lds\n\t"
      "v_mfma_f32_16x16x32_bf16 v[24:27], a[248:251], a[88:91], v[24:27]\n\t"
      "v_exp_f32 v21, v21\n\t"
      "v_mfma_f32_16x16x32_bf16 v[24:27], a[252:255], a[92:95], v[24:27]\n\t"
      "v_exp_f32 v22, v22\n\t"
      "v_mfma_f32_16x16x32_bf16 v[28:31], a[224:227], a[96:99], v[56:59]\n\t"
      "v_exp_f32 v23, v23\n\t"
      "v_mfma_f32_16x16x32_bf16 v[28:31], a[228:231], a[100:103], v[28:31]\n\t"
      "buffer_load_dwordx4 v254, s[52:55], s82 offen offset:2048 lds\n\t"
      "v_add_f32 v73, v73, v20\n\t"
      "v_mfma_f32_16x16x32_bf16 v[28:31], a[232:235], a[104:107], v[28:31]\n\t"
      "v_cvt_pk_bf16_f32 v84, v20, v21\n\t"
      "v_add_f32 v73, v73, v21\n\t"
      "v_mfma_f32_16x16x32_bf16 v[28:31], a[236:239], a[108:111], v[28:31]\n\t"
      "v_add_f32 v73, v73, v22\n\t"
      "v_cvt_pk_bf16_f32 v85, v22, v23\n\t"
      "v_mfma_f32_16x16x32_bf16 v[28:31], a[240:243], a[112:115], v[28:31]\n\t"
      "v_add_f32 v73, v73, v23\n\t"
      "ds_write_b64 v11, v[84:85] offset:18432\n\t"
      "v_mfma_f32_16x16x32_bf16 v[28:31], a[244:247], a[116:119], v[28:31]\n\t"
      "buffer_load_dwordx4 v254, s[52:55], s83 offen offset:3072 lds\n\t"
      "v_mfma_f32_16x16x32_bf16 v[28:31], a[248:251], a[120:123], v[28:31]\n\t"
      "ds_read_b128 v[88:91], v10 offset:0\n\t"
      "ds_read_b128 v[92:95], v10 offset:1024\n\t"
      "v_mfma_f32_16x16x32_bf16 v[28:31], a[252:255], a[124:127], v[28:31]\n\t"
      "ds_read_b128 v[96:99], v10 offset:2048\n\t"
      "ds_read_b128 v[100:103], v10 offset:3072\n\t"
      "v_mfma_f32_16x16x32_bf16 v[32:35], a[224:227], a[128:131], v[60:63]\n\t"
      "s_add_u32 s100, s100, 1\n\t"
      "s_cmp_eq_u32 s100, 7\n\t"
      "v_mfma_f32_16x16x32_bf16 v[32:35], a[228:231], a[132:135], v[32:35]\n\t"
      "s_cselect_b32 s100, 0, s100\n\t"
      "v_exp_f32 v24, v24\n\t"
      "v_mfma_f32_16x16x32_bf16 v[32:35], a[232:235], a[136:139], v[32:35]\n\t"
      "v_exp_f32 v25, v25\n\t"
      "v_mfma_f32_16x16x32_bf16 v[32:35], a[236:239], a[140:143], v[32:35]\n\t"
      "v_exp_f32 v26, v26\n\t"
      "v_mfma_f32_16x16x32_bf16 v[32:35], a[240:243], a[144:147], v[32:35]\n\t"
      "v_exp_f32 v27, v27\n\t"
      "v_mfma_f32_16x16x32_bf16 v[32:35], a[244:247], a[148:151], v[32:35]\n\t"
      "v_add_f32 v74, v74, v24\n\t"
      "v_mfma_f32_16x16x32_bf16 v[32:35], a[248:251], a[152:155], v[32:35]\n\t"
      "v_cvt_pk_bf16_f32 v82, v24, v25\n\t"
      "v_add_f32 v74, v74, v25\n\t"
      "v_mfma_f32_16x16x32_bf16 v[32:35], a[252:255], a[156:159], v[32:35]\n\t"
      "v_add_f32 v74, v74, v26\n\t"
      "v_cvt_pk_bf16_f32 v83, v26, v27\n\t"
      "v_mfma_f32_16x16x32_bf16 v[36:39], a[224:227], a[160:163], v[64:67]\n\t"
      "v_add_f32 v74, v74, v27\n\t"
      "ds_write_b64 v11, v[82:83] offset:20480\n\t"
      "v_mfma_f32_16x16x32_bf16 v[36:39], a[228:231], a[164:167], v[36:39]\n\t"
      "v_exp_f32 v28, v28\n\t"
      "v_mfma_f32_16x16x32_bf16 v[36:39], a[232:235], a[168:171], v[36:39]\n\t"
      "s_waitcnt vmcnt(18)\n\t"
      "v_mfma_f32_16x16x32_bf16 v[36:39], a[236:239], a[172:175], v[36:39]\n\t"
      "ds_read_b64_tr_b16 v[104:105], v6 offset:0\n\t"
      "ds_read_b64_tr_b16 v[106:107], v6 offset:2048\n\t"
      "v_mfma_f32_16x16x32_bf16 v[36:39], a[240:243], a[176:179], v[36:39]\n\t"
      "ds_read_b64_tr_b16 v[108:109], v6 offset:4096\n\t"
      "ds_read_b64_tr_b16 v[110:111], v6 offset:6144\n\t"
      "v_mfma_f32_16x16x32_bf16 v[36:39], a[244:247], a[180:183], v[36:39]\n\t"
      "ds_read_b64_tr_b16 v[112:113], v7 offset:0\n\t"
      "ds_read_b64_tr_b16 v[114:115], v7 offset:2048\n\t"
      "v_mfma_f32_16x16x32_bf16 v[36:39], a[248:251], a[184:187], v[36:39]\n\t"
      "ds_read_b64_tr_b16 v[116:117], v7 offset:4096\n\t"
      "ds_read_b64_tr_b16 v[118:119], v7 offset:6144\n\t"
      "v_mfma_f32_16x16x32_bf16 v[36:39], a[252:255], a[188:191], v[36:39]\n\t"
      "ds_read_b64_tr_b16 v[120:121], v8 offset:0\n\t"
      "ds_read_b64_tr_b16 v[122:123], v8 offset:2048\n\t"
      "v_mfma_f32_16x16x32_bf16 v[40:43], a[224:227], a[192:195], v[68:71]\n\t"
      "ds_read_b64_tr_b16 v[124:125], v8 offset:4096\n\t"
      "ds_read_b64_tr_b16 v[126:127], v8 offset:6144\n\t"
      "v_mfma_f32_16x16x32_bf16 v[40:43], a[228:231], a[196:199], v[40:43]\n\t"
      "ds_read_b64_tr_b16 v[128:129], v9 offset:0\n\t"
      "ds_read_b64_tr_b16 v[130:131], v9 offset:2048\n\t"
      "v_mfma_f32_16x16x32_bf16 v[40:43], a[232:235], a[200:203], v[40:43]\n\t"
      "ds_read_b64_tr_b16 v[132:133], v9 offset:4096\n\t"
      "ds_read_b64_tr_b16 v[134:135], v9 offset:6144\n\t"
      "v_mfma_f32_16x16x32_bf16 v[40:43], a[236:239], a[204:207], v[40:43]\n\t"
      "v_exp_f32 v29, v29\n\t"
      "v_mfma_f32_16x16x32_bf16 v[40:43], a[240:243], a[208:211], v[40:43]\n\t"
      "v_exp_f32 v30, v30\n\t"
      "v_mfma_f32_16x16x32_bf16 v[40:43], a[244:247], a[212:215], v[40:43]\n\t"
      "v_exp_f32 v31, v31\n\t"
      "v_mfma_f32_16x16x32_bf16 v[40:43], a[248:251], a[216:219], v[40:43]\n\t"
      "v_add_f32 v75, v75, v28\n\t"
      "v_cvt_pk_bf16_f32 v84, v28, v29\n\t"
      "v_mfma_f32_16x16x32_bf16 v[40:43], a[252:255], a[220:223], v[40:43]\n\t"
      "v_add_f32 v75, v75, v29\n\t"
      "v_add_f32 v75, v75, v30\n\t"
      "v_cvt_pk_bf16_f32 v85, v30, v31\n\t"
      "v_add_f32 v75, v75, v31\n\t"
      "ds_write_b64 v11, v[84:85] offset:22528\n\t"
      "s_waitcnt lgkmcnt(15)\n\t"
      "v_mfma_f32_16x16x32_bf16 v[136:139], v[104:107], v[88:91], v[136:139]\n\t"
      "v_exp_f32 v32, v32\n\t"
      "s_waitcnt lgkmcnt(13)\n\t"
      "v_mfma_f32_16x16x32_bf16 v[136:139], v[108:111], v[92:95], v[136:139]\n\t"
      "s_lshl_b32 s92, s100, 7\n\t"
      "s_add_u32 s52, s97, s92\n\t"
      "s_waitcnt lgkmcnt(11)\n\t"
      "v_mfma_f32_16x16x32_bf16 v[140:143], v[112:115], v[88:91], v[140:143]\n\t"
      "s_addc_u32 s53, s98, 0\n\t"
      "s_add_u32 m0, s89, 0\n\t"
      "s_waitcnt lgkmcnt(9)\n\t"
      "v_mfma_f32_16x16x32_bf16 v[140:143], v[116:119], v[92:95], v[140:143]\n\t"
      "buffer_load_dwordx4 v254, s[52:55], s76 offen offset:0 lds\n\t"
      "v_exp_f32 v33, v33\n\t"
      "s_waitcnt lgkmcnt(7)\n\t"
      "v_mfma_f32_16x16x32_bf16 v[144:147], v[120:123], v[88:91], v[144:147]\n\t"
      "v_exp_f32 v34, v34\n\t"
      "s_waitcnt lgkmcnt(5)\n\t"
      "v_mfma_f32_16x16x32_bf16 v[144:147], v[124:127], v[92:95], v[144:147]\n\t"
      "buffer_load_dwordx4 v254, s[52:55], s77 offen offset:1024 lds\n\t"
      "s_waitcnt lgkmcnt(3)\n\t"
      "v_mfma_f32_16x16x32_bf16 v[148:151], v[128:131], v[88:91], v[148:151]\n\t"
      "v_exp_f32 v35, v35\n\t"
      "s_waitcnt lgkmcnt(1)\n\t"
      "v_mfma_f32_16x16x32_bf16 v[148:151], v[132:135], v[92:95], v[148:151]\n\t"
      "ds_read_b128 v[88:91], v10 offset:4096\n\t"
      "ds_read_b128 v[92:95], v10 offset:5120\n\t"
      "v_mfma_f32_16x16x32_bf16 v[152:155], v[104:107], v[96:99], v[152:155]\n\t"
      "v_add_f32 v76, v76, v32\n\t"
      "v_cvt_pk_bf16_f32 v82, v32, v33\n\t"
      "v_mfma_f32_16x16x32_bf16 v[152:155], v[108:111], v[100:103], v[152:155]\n\t"
      "buffer_load_dwordx4 v254, s[52:55], s78 offen offset:2048 lds\n\t"
      "v_add_f32 v76, v76, v33\n\t"
      "v_mfma_f32_16x16x32_bf16 v[156:159], v[112:115], v[96:99], v[156:159]\n\t"
      "v_add_f32 v76, v76, v34\n\t"
      "v_cvt_pk_bf16_f32 v83, v34, v35\n\t"
      "v_mfma_f32_16x16x32_bf16 v[156:159], v[116:119], v[100:103], v[156:159]\n\t"
      "v_add_f32 v76, v76, v35\n\t"
      "ds_write_b64 v11, v[82:83] offset:24576\n\t"
      "v_mfma_f32_16x16x32_bf16 v[160:163], v[120:123], v[96:99], v[160:163]\n\t"
      "v_exp_f32 v36, v36\n\t"
      "v_mfma_f32_16x16x32_bf16 v[160:163], v[124:127], v[100:103], v[160:163]\n\t"
      "buffer_load_dwordx4 v254, s[52:55], s79 offen offset:3072 lds\n\t"
      "v_mfma_f32_16x16x32_bf16 v[164:167], v[128:131], v[96:99], v[164:167]\n\t"
      "v_exp_f32 v37, v37\n\t"
      "v_mfma_f32_16x16x32_bf16 v[164:167], v[132:135], v[100:103], v[164:167]\n\t"
      "ds_read_b128 v[96:99], v10 offset:6144\n\t"
      "ds_read_b128 v[100:103], v10 offset:7168\n\t"
      "s_waitcnt lgkmcnt(4)\n\t"
      "v_mfma_f32_16x16x32_bf16 v[168:171], v[104:107], v[88:91], v[168:171]\n\t"
      "v_exp_f32 v38, v38\n\t"
      "s_waitcnt lgkmcnt(3)\n\t"
      "v_mfma_f32_16x16x32_bf16 v[168:171], v[108:111], v[92:95], v[168:171]\n\t"
      "s_add_u32 m0, s89, 4096\n\t"
      "s_nop 0\n\t"
      "buffer_load_dwordx4 v254, s[52:55], s80 offen offset:0 lds\n\t"
      "v_mfma_f32_16x16x32_bf16 v[172:175], v[112:115], v[88:91], v[172:175]\n\t"
      "v_exp_f32 v39, v39\n\t"
      "v_mfma_f32_16x16x32_bf16 v[172:175], v[116:119], v[92:95], v[172:175]\n\t"
      "v_add_f32 v77, v77, v36\n\t"
      "v_cvt_pk_bf16_f32 v84, v36, v37\n\t"
      "v_mfma_f32_16x16x32_bf16 v[176:179], v[120:123], v[88:91], v[176:179]\n\t"
      "v_add_f32 v77, v77, v37\n\t"
      "v_add_f32 v77, v77, v38\n\t"
      "v_mfma_f32_16x16x32_bf16 v[176:179], v[124:127], v[92:95], v[176:179]\n\t"
      "buffer_load_dwordx4 v254, s[52:55], s81 offen offset:1024 lds\n\t"
      "v_cvt_pk_bf16_f32 v85, v38, v39\n\t"
      "v_mfma_f32_16x16x32_bf16 v[180:183], v[128:131], v[88:91], v[180:183]\n\t"
      "v_add_f32 v77, v77, v39\n\t"
      "ds_write_b64 v11, v[84:85] offset:26624\n\t"
      "v_mfma_f32_16x16x32_bf16 v[180:183], v[132:135], v[92:95], v[180:183]\n\t"
      "ds_read_b128 v[88:91], v10 offset:8192\n\t"
      "s_waitcnt lgkmcnt(3)\n\t"
      "v_mfma_f32_16x16x32_bf16 v[184:187], v[104:107], v[96:99], v[184:187]\n\t"
      "ds_read_b128 v[92:95], v10 offset:9216\n\t"
      "v_exp_f32 v40, v40\n\t"
      "s_waitcnt lgkmcnt(3)\n\t"
      "v_mfma_f32_16x16x32_bf16 v[184:187], v[108:111], v[100:103], v[184:187]\n\t"
      "buffer_load_dwordx4 v254, s[52:55], s82 offen offset:2048 lds\n\t"
      "v_mfma_f32_16x16x32_bf16 v[188:191], v[112:115], v[96:99], v[188:191]\n\t"
      "v_exp_f32 v41, v41\n\t"
      "v_mfma_f32_16x16x32_bf16 v[188:191], v[116:119], v[100:103], v[188:191]\n\t"
      "v_exp_f32 v42, v42\n\t"
      "v_mfma_f32_16x16x32_bf16 v[192:195], v[120:123], v[96:99], v[192:195]\n\t"
      "v_exp_f32 v43, v43\n\t"
      "v_mfma_f32_16x16x32_bf16 v[192:195], v[124:127], v[100:103], v[192:195]\n\t"
      "buffer_load_dwordx4 v254, s[52:55], s83 offen offset:3072 lds\n\t"
      "s_add_u32 s100, s100, 1\n\t"
      "v_mfma_f32_16x16x32_bf16 v[196:199], v[128:131], v[96:99], v[196:199]\n\t"
      "s_cmp_eq_u32 s100, 7\n\t"
      "s_cselect_b32 s100, 0, s100\n\t"
      "v_mfma_f32_16x16x32_bf16 v[196:199], v[132:135], v[100:103], v[196:199]\n\t"
      "ds_read_b128 v[96:99], v10 offset:10240\n\t"
      "ds_read_b128 v[100:103], v10 offset:11264\n\t"
      "s_waitcnt lgkmcnt(3)\n\t"
      "v_mfma_f32_16x16x32_bf16 v[200:203], v[104:107], v[88:91], v[200:203]\n\t"
      "v_add_f32 v78, v78, v40\n\t"
      "v_cvt_pk_bf16_f32 v82, v40, v41\n\t"
      "s_waitcnt lgkmcnt(2)\n\t"
      "v_mfma_f32_16x16x32_bf16 v[200:203], v[108:111], v[92:95], v[200:203]\n\t"
      "v_add_f32 v78, v78, v41\n\t"
      "v_add_f32 v78, v78, v42\n\t"
      "v_mfma_f32_16x16x32_bf16 v[204:207], v[112:115], v[88:91], v[204:207]\n\t"
      "v_cvt_pk_bf16_f32 v83, v42, v43\n\t"
      "v_add_f32 v78, v78, v43\n\t"
      "v_mfma_f32_16x16x32_bf16 v[204:207], v[116:119], v[92:95], v[204:207]\n\t"
      "ds_write_b64 v11, v[82:83] offset:28672\n\t"
      "v_mfma_f32_16x16x32_bf16 v[208:211], v[120:123], v[88:91], v[208:211]\n\t"
      "v_mov_b32 v16, v72\n\t"
      "v_mov_b32 v20, v73\n\t"
      "v_mfma_f32_16x16x32_bf16 v[208:211], v[124:127], v[92:95], v[208:211]\n\t"
      "v_mov_b32 v24, v74\n\t"
      "v_mov_b32 v28, v75\n\t"
      "v_mfma_f32_16x16x32_bf16 v[212:215], v[128:131], v[88:91], v[212:215]\n\t"
      "v_mov_b32 v32, v76\n\t"
      "v_mov_b32 v36, v77\n\t"
      "v_mfma_f32_16x16x32_bf16 v[212:215], v[132:135], v[92:95], v[212:215]\n\t"
      "ds_read_b128 v[88:91], v10 offset:12288\n\t"
      "ds_read_b128 v[92:95], v10 offset:13312\n\t"
      "s_waitcnt lgkmcnt(4)\n\t"
      "v_mfma_f32_16x16x32_bf16 v[216:219], v[104:107], v[96:99], v[216:219]\n\t"
      "v_mov_b32 v40, v78\n\t"
      "v_permlane32_swap_b32 v72, v16\n\t"
      "s_waitcnt lgkmcnt(3)\n\t"
      "v_mfma_f32_16x16x32_bf16 v[216:219], v[108:111], v[100:103], v[216:219]\n\t"
      "v_permlane32_swap_b32 v73, v20\n\t"
      "v_permlane32_swap_b32 v74, v24\n\t"
      "v_mfma_f32_16x16x32_bf16 v[220:223], v[112:115], v[96:99], v[220:223]\n\t"
      "v_permlane32_swap_b32 v75, v28\n\t"
      "v_permlane32_swap_b32 v76, v32\n\t"
      "v_mfma_f32_16x16x32_bf16 v[220:223], v[116:119], v[100:103], v[220:223]\n\t"
      "v_permlane32_swap_b32 v77, v36\n\t"
      "v_permlane32_swap_b32 v78, v40\n\t"
      "v_mfma_f32_16x16x32_bf16 v[224:227], v[120:123], v[96:99], v[224:227]\n\t"
      "v_add_f32 v72, v72, v16\n\t"
      "v_add_f32 v73, v73, v20\n\t"
      "v_mfma_f32_16x16x32_bf16 v[224:227], v[124:127], v[100:103], v[224:227]\n\t"
      "v_add_f32 v74, v74, v24\n\t"
      "v_add_f32 v75, v75, v28\n\t"
      "v_mfma_f32_16x16x32_bf16 v[228:231], v[128:131], v[96:99], v[228:231]\n\t"
      "v_add_f32 v76, v76, v32\n\t"
      "v_add_f32 v77, v77, v36\n\t"
      "v_mfma_f32_16x16x32_bf16 v[228:231], v[132:135], v[100:103], v[228:231]\n\t"
      "v_add_f32 v78, v78, v40\n\t"
      "v_mov_b32 v16, v72\n\t"
      "s_waitcnt lgkmcnt(1)\n\t"
      "v_mfma_f32_16x16x32_bf16 v[232:235], v[104:107], v[88:91], v[232:235]\n\t"
      "v_mov_b32 v20, v73\n\t"
      "v_mov_b32 v24, v74\n\t"
      "s_waitcnt lgkmcnt(0)\n\t"
      "v_mfma_f32_16x16x32_bf16 v[232:235], v[108:111], v[92:95], v[232:235]\n\t"
      "v_mov_b32 v28, v75\n\t"
      "v_mov_b32 v32, v76\n\t"
      "v_mfma_f32_16x16x32_bf16 v[236:239], v[112:115], v[88:91], v[236:239]\n\t"
      "v_mov_b32 v36, v77\n\t"
      "v_mov_b32 v40, v78\n\t"
      "v_mfma_f32_16x16x32_bf16 v[236:239], v[116:119], v[92:95], v[236:239]\n\t"
      "v_permlane16_swap_b32 v72, v16\n\t"
      "v_permlane16_swap_b32 v73, v20\n\t"
      "v_mfma_f32_16x16x32_bf16 v[240:243], v[120:123], v[88:91], v[240:243]\n\t"
      "v_permlane16_swap_b32 v74, v24\n\t"
      "v_permlane16_swap_b32 v75, v28\n\t"
      "v_mfma_f32_16x16x32_bf16 v[240:243], v[124:127], v[92:95], v[240:243]\n\t"
      "v_permlane16_swap_b32 v76, v32\n\t"
      "v_permlane16_swap_b32 v77, v36\n\t"
      "v_mfma_f32_16x16x32_bf16 v[244:247], v[128:131], v[88:91], v[244:247]\n\t"
      "v_permlane16_swap_b32 v78, v40\n\t"
      "v_add_f32 v72, v72, v16\n\t"
      "v_mfma_f32_16x16x32_bf16 v[244:247], v[132:135], v[92:95], v[244:247]\n\t"
      "v_add_f32 v73, v73, v20\n\t"
      "v_add_f32 v74, v74, v24\n\t"
      "v_add_f32 v75, v75, v28\n\t"
      "v_add_f32 v76, v76, v32\n\t"
      "v_add_f32 v77, v77, v36\n\t"
      "v_add_f32 v78, v78, v40\n\t"
      "ds_write_b32 v79, v72 offset:0\n\t"
      "ds_write_b32 v79, v73 offset:256\n\t"
      "ds_write_b32 v79, v74 offset:512\n\t"
      "ds_write_b32 v79, v75 offset:768\n\t"
      "ds_write_b32 v79, v76 offset:1024\n\t"
      "ds_write_b32 v79, v77 offset:1280\n\t"
      "ds_write_b32 v79, v78 offset:1536\n\t"
      "v_mbcnt_lo_u32_b32 v0, -1, 0\n\t"
      "v_mbcnt_hi_u32_b32 v0, -1, v0\n\t"
      "v_and_b32 v1, 15, v0\n\t"
      "v_lshrrev_b32 v2, 4, v0\n\t"
      "v_lshrrev_b32 v17, 2, v1\n\t"
      "v_and_b32 v18, 3, v0\n\t"
      "v_lshlrev_b32 v3, 2, v2\n\t"
      "v_add_u32 v3, v3, v17\n\t"
      "v_lshlrev_b32 v21, 7, v3\n\t"
      "v_lshrrev_b32 v19, 1, v3\n\t"
      "v_and_b32 v19, 3, v19\n\t"
      "v_lshlrev_b32 v19, 1, v19\n\t"
      "v_and_b32 v3, 1, v18\n\t"
      "v_lshlrev_b32 v3, 3, v3\n\t"
      "v_add_u32 v21, v21, v3\n\t"
      "v_lshrrev_b32 v18, 1, v18\n\t"
      "s_lshl_b32 s94, s88, 1\n\t"
      "v_add_u32 v3, s94, v18\n\t"
      "v_xor_b32 v3, v3, v19\n\t"
      "v_lshlrev_b32 v3, 4, v3\n\t"
      "v_add_u32 v44, v21, v3\n\t"
      "v_add_u32 v45, 131072, v44\n\t"
      "v_lshlrev_b32 v47, 4, v0\n\t"
      "v_lshlrev_b32 v48, 3, v0\n\t"
      "s_lshl_b32 s94, s88, 11\n\t"
      "s_add_u32 s94, s94, 65536\n\t"
      "v_add_u32 v49, s94, v47\n\t"
      "v_add_u32 v50, 65536, v47\n\t"
      "s_waitcnt vmcnt(24)\n\t"
      "s_mov_b32 s92, 0\n\t"
      "s_cmp_lg_u32 s88, 0\n\t"
      "s_cbranch_scc1 NOK%=\n\t"
      "NSP%=:\n\t"
      "v_cmp_eq_u32 vcc, v250, v81\n\t"
      "s_nop 4\n\t"
      "s_cbranch_vccnz NGOT%=\n\t"
      "s_add_u32 s92, s92, 1\n\t"
      "s_cmp_gt_u32 s92, 0x8000\n\t"
      "s_cbranch_scc1 NGOT%=\n\t"
      "s_sleep 4\n\t"
      "v_mbcnt_lo_u32_b32 v250, -1, 0\n\t"
      "v_mbcnt_hi_u32_b32 v250, -1, v250\n\t"
      "v_and_b32 v250, 15, v250\n\t"
      "v_lshlrev_b32 v250, 2, v250\n\t"
      "s_mov_b32 s46, s29\n\t"
      "buffer_load_dword v248, v250, s[44:47], 0 offen offset:512 sc1\n\t"
      "s_waitcnt vmcnt(0)\n\t"
      "v_lshlrev_b32 v251, 1, v250\n\t"
      "v_mov_b32 v249, v86\n\t"
      "s_mov_b32 s46, s29\n\t"
      "buffer_store_dwordx2 v[248:249], v251, s[44:47], 0 offen offset:1024 sc1\n\t"
      "s_mov_b32 s46, s28\n\t"
      "buffer_load_dwordx2 v[250:251], v87, s[44:47], 0 offen offset:0 sc1\n\t"
      "s_waitcnt vmcnt(0)\n\t"
      "s_branch NSP%=\n\t"
      "NGOT%=:\n\t"
      "v_mov_b32 v252, 147264\n\t"
      "ds_write_b32 v252, v251 offset:0\n\t"
      "NOK%=:\n\t"
      "s_waitcnt lgkmcnt(0)\n\t"
      "s_barrier\n\t"
      "s_waitcnt vmcnt(16)\n\t"
      "ds_read_b64_tr_b16 v[104:105], v6 offset:32768\n\t"
      "ds_read_b64_tr_b16 v[106:107], v6 offset:34816\n\t"
      "ds_read_b64_tr_b16 v[108:109], v6 offset:36864\n\t"
      "ds_read_b64_tr_b16 v[110:111], v6 offset:38912\n\t"
      "ds_read_b64_tr_b16 v[112:113], v7 offset:32768\n\t"
      "ds_read_b64_tr_b16 v[114:115], v7 offset:34816\n\t"
      "ds_read_b64_tr_b16 v[116:117], v7 offset:36864\n\t"
      "ds_read_b64_tr_b16 v[118:119], v7 offset:38912\n\t"
      "ds_read_b64_tr_b16 v[120:121], v8 offset:32768\n\t"
      "ds_read_b64_tr_b16 v[122:123], v8 offset:34816\n\t"
      "ds_read_b64_tr_b16 v[124:125], v8 offset:36864\n\t"
      "ds_read_b64_tr_b16 v[126:127], v8 offset:38912\n\t"
      "ds_read_b64_tr_b16 v[128:129], v9 offset:32768\n\t"
      "ds_read_b64_tr_b16 v[130:131], v9 offset:34816\n\t"
      "ds_read_b64_tr_b16 v[132:133], v9 offset:36864\n\t"
      "ds_read_b64_tr_b16 v[134:135], v9 offset:38912\n\t"
      "ds_read_b128 v[88:91], v10 offset:16384\n\t"
      "ds_read_b128 v[92:95], v10 offset:17408\n\t"
      "ds_read_b128 v[96:99], v10 offset:18432\n\t"
      "ds_read_b128 v[100:103], v10 offset:19456\n\t"
      "s_waitcnt vmcnt(8)\n\t"
      "s_waitcnt lgkmcnt(4)\n\t"
      "s_barrier\n\t"
      "s_waitcnt lgkmcnt(3)\n\t"
      "v_mfma_f32_16x16x32_bf16 v[136:139], v[104:107], v[88:91], v[136:139]\n\t"
      "ds_read_b64_tr_b16 a[224:225], v44 offset:32768\n\t"
      "ds_read_b64_tr_b16 a[226:227], v44 offset:34816\n\t"
      "s_waitcnt lgkmcnt(4)\n\t"
      "v_mfma_f32_16x16x32_bf16 v[136:139], v[108:111], v[92:95], v[136:139]\n\t"
      "ds_read_b64_tr_b16 a[228:229], v44 offset:36864\n\t"
      "ds_read_b64_tr_b16 a[230:231], v44 offset:38912\n\t"
      "v_mfma_f32_16x16x32_bf16 v[140:143], v[112:115], v[88:91], v[140:143]\n\t"
      "ds_read_b64_tr_b16 a[232:233], v44 offset:40960\n\t"
      "ds_read_b64_tr_b16 a[234:235], v44 offset:43008\n\t"
      "v_mfma_f32_16x16x32_bf16 v[140:143], v[116:119], v[92:95], v[140:143]\n\t"
      "ds_read_b64_tr_b16 a[236:237], v44 offset:45056\n\t"
      "ds_read_b64_tr_b16 a[238:239], v44 offset:47104\n\t"
      "v_mfma_f32_16x16x32_bf16 v[144:147], v[120:123], v[88:91], v[144:147]\n\t"
      "ds_read_b64_tr_b16 a[240:241], v44 offset:49152\n\t"
      "ds_read_b64_tr_b16 a[242:243], v44 offset:51200\n\t"
      "v_mfma_f32_16x16x32_bf16 v[144:147], v[124:127], v[92:95], v[144:147]\n\t"
      "ds_read_b64_tr_b16 a[244:245], v44 offset:53248\n\t"
      "ds_read_b64_tr_b16 a[246:247], v44 offset:55296\n\t"
      "v_mfma_f32_16x16x32_bf16 v[148:151], v[128:131], v[88:91], v[148:151]\n\t"
      "ds_read_b64_tr_b16 a[248:249], v44 offset:57344\n\t"
      "ds_read_b64_tr_b16 a[250:251], v44 offset:59392\n\t"
      "v_mfma_f32_16x16x32_bf16 v[148:151], v[132:135], v[92:95], v[148:151]\n\t"
      "ds_read_b128 v[88:91], v10 offset:20480\n\t"
      "ds_read_b128 v[92:95], v10 offset:21504\n\t"
      "s_waitcnt lgkmcnt(15)\n\t"
      "v_mfma_f32_16x16x32_bf16 v[152:155], v[104:107], v[96:99], v[152:155]\n\t"
      "ds_read_b64_tr_b16 a[252:253], v44 offset:61440\n\t"
      "ds_read_b64_tr_b16 a[254:255], v44 offset:63488\n\t"
      "v_mfma_f32_16x16x32_bf16 v[152:155], v[108:111], v[100:103], v[152:155]\n\t"
      "v_cvt_pk_bf16_f32 v56, v136, v137\n\t"
      "v_cvt_pk_bf16_f32 v57, v138, v139\n\t"
      "v_mfma_f32_16x16x32_bf16 v[156:159], v[112:115], v[96:99], v[156:159]\n\t"
      "ds_read_b128 v[16:19], v80 offset:0\n\t"
      "ds_read_b128 v[20:23], v80 offset:256\n\t"
      "v_mfma_f32_16x16x32_bf16 v[156:159], v[116:119], v[100:103], v[156:159]\n\t"
      "ds_read_b128 v[24:27], v80 offset:512\n\t"
      "ds_read_b128 v[28:31], v80 offset:768\n\t"
      "v_mfma_f32_16x16x32_bf16 v[160:163], v[120:123], v[96:99], v[160:163]\n\t"
      "ds_read_b128 v[32:35], v80 offset:1024\n\t"
      "ds_read_b128 v[36:39], v80 offset:1280\n\t"
      "v_mfma_f32_16x16x32_bf16 v[160:163], v[124:127], v[100:103], v[160:163]\n\t"
      "ds_read_b128 v[40:43], v80 offset:1536\n\t"
      "s_waitcnt lgkmcnt(6)\n\t"
      "v_add_f32 v16, v16, v17\n\t"
      "v_mfma_f32_16x16x32_bf16 v[164:167], v[128:131], v[96:99], v[164:167]\n\t"
      "v_add_f32 v18, v18, v19\n\t"
      "v_add_f32 v16, v16, v18\n\t"
      "v_mfma_f32_16x16x32_bf16 v[164:167], v[132:135], v[100:103], v[164:167]\n\t"
      "ds_read_b128 v[96:99], v10 offset:22528\n\t"
      "ds_read_b128 v[100:103], v10 offset:23552\n\t"
      "v_mfma_f32_16x16x32_bf16 v[168:171], v[104:107], v[88:91], v[168:171]\n\t"
      "v_rcp_f32 v72, v16\n\t"
      "v_mfma_f32_16x16x32_bf16 v[168:171], v[108:111], v[92:95], v[168:171]\n\t"
      "s_waitcnt lgkmcnt(7)\n\t"
      "v_add_f32 v20, v20, v21\n\t"
      "v_add_f32 v22, v22, v23\n\t"
      "v_mfma_f32_16x16x32_bf16 v[172:175], v[112:115], v[88:91], v[172:175]\n\t"
      "v_add_f32 v20, v20, v22\n\t"
      "v_rcp_f32 v73, v20\n\t"
      "v_mfma_f32_16x16x32_bf16 v[172:175], v[116:119], v[92:95], v[172:175]\n\t"
      "s_waitcnt lgkmcnt(6)\n\t"
      "v_add_f32 v24, v24, v25\n\t"
      "v_mfma_f32_16x16x32_bf16 v[176:179], v[120:123], v[88:91], v[176:179]\n\t"
      "v_add_f32 v26, v26, v27\n\t"
      "v_add_f32 v24, v24, v26\n\t"
      "v_mfma_f32_16x16x32_bf16 v[176:179], v[124:127], v[92:95], v[176:179]\n\t"
      "v_rcp_f32 v74, v24\n\t"
      "v_mfma_f32_16x16x32_bf16 v[180:183], v[128:131], v[88:91], v[180:183]\n\t"
      "s_waitcnt lgkmcnt(5)\n\t"
      "v_add_f32 v28, v28, v29\n\t"
      "v_add_f32 v30, v30, v31\n\t"
      "v_mfma_f32_16x16x32_bf16 v[180:183], v[132:135], v[92:95], v[180:183]\n\t"
      "ds_read_b128 v[88:91], v10 offset:24576\n\t"
      "ds_read_b128 v[92:95], v10 offset:25600\n\t"
      "s_waitcnt lgkmcnt(3)\n\t"
      "v_mfma_f32_16x16x32_bf16 v[184:187], v[104:107], v[96:99], v[184:187]\n\t"
      "v_add_f32 v28, v28, v30\n\t"
      "v_rcp_f32 v75, v28\n\t"
      "s_waitcnt lgkmcnt(2)\n\t"
      "v_mfma_f32_16x16x32_bf16 v[184:187], v[108:111], v[100:103], v[184:187]\n\t"
      "v_add_f32 v32, v32, v33\n\t"
      "v_mfma_f32_16x16x32_bf16 v[188:191], v[112:115], v[96:99], v[188:191]\n\t"
      "v_add_f32 v34, v34, v35\n\t"
      "v_add_f32 v32, v32, v34\n\t"
      "v_mfma_f32_16x16x32_bf16 v[188:191], v[116:119], v[100:103], v[188:191]\n\t"
      "v_rcp_f32 v76, v32\n\t"
      "v_mfma_f32_16x16x32_bf16 v[192:195], v[120:123], v[96:99], v[192:195]\n\t"
      "v_add_f32 v36, v36, v37\n\t"
      "v_add_f32 v38, v38, v39\n\t"
      "v_mfma_f32_16x16x32_bf16 v[192:195], v[124:127], v[100:103], v[192:195]\n\t"
      "v_add_f32 v36, v36, v38\n\t"
      "v_rcp_f32 v77, v36\n\t"
      "v_mfma_f32_16x16x32_bf16 v[196:199], v[128:131], v[96:99], v[196:199]\n\t"
      "v_add_f32 v40, v40, v41\n\t"
      "v_mfma_f32_16x16x32_bf16 v[196:199], v[132:135], v[100:103], v[196:199]\n\t"
      "ds_read_b128 v[96:99], v10 offset:26624\n\t"
      "ds_read_b128 v[100:103], v10 offset:27648\n\t"
      "s_waitcnt lgkmcnt(3)\n\t"
      "v_mfma_f32_16x16x32_bf16 v[200:203], v[104:107], v[88:91], v[200:203]\n\t"
      "v_add_f32 v42, v42, v43\n\t"
      "v_add_f32 v40, v40, v42\n\t"
      "s_waitcnt lgkmcnt(2)\n\t"
      "v_mfma_f32_16x16x32_bf16 v[200:203], v[108:111], v[92:95], v[200:203]\n\t"
      "v_rcp_f32 v78, v40\n\t"
      "v_mfma_f32_16x16x32_bf16 v[204:207], v[112:115], v[88:91], v[204:207]\n\t"
      "v_add_f32 v253, v16, v20\n\t"
      "v_add_f32 v253, v253, v24\n\t"
      "v_mfma_f32_16x16x32_bf16 v[204:207], v[116:119], v[92:95], v[204:207]\n\t"
      "v_add_f32 v253, v253, v28\n\t"
      "v_add_f32 v253, v253, v32\n\t"
      "v_mfma_f32_16x16x32_bf16 v[208:211], v[120:123], v[88:91], v[208:211]\n\t"
      "v_add_f32 v253, v253, v36\n\t"
      "v_add_f32 v253, v253, v40\n\t"
      "v_mfma_f32_16x16x32_bf16 v[208:211], v[124:127], v[92:95], v[208:211]\n\t"
      "v_mov_b32 v252, 0x53800000\n\t"
      "v_cmp_nle_f32 vcc, v253, v252\n\t"
      "v_mfma_f32_16x16x32_bf16 v[212:215], v[128:131], v[88:91], v[212:215]\n\t"
      "s_nop 0\n\t"
      "s_mov_b64 s[30:31], vcc\n\t"
      "v_mfma_f32_16x16x32_bf16 v[212:215], v[132:135], v[92:95], v[212:215]\n\t"
      "ds_read_b128 v[88:91], v10 offset:28672\n\t"
      "ds_read_b128 v[92:95], v10 offset:29696\n\t"
      "s_waitcnt lgkmcnt(3)\n\t"
      "v_mfma_f32_16x16x32_bf16 v[216:219], v[104:107], v[96:99], v[216:219]\n\t"
      "v_mov_b32 v252, 147264\n\t"
      "ds_read_b32 v86, v252 offset:0\n\t"
      "s_waitcnt lgkmcnt(3)\n\t"
      "v_mfma_f32_16x16x32_bf16 v[216:219], v[108:111], v[100:103], v[216:219]\n\t"
      "v_cvt_pk_bf16_f32 v58, v140, v141\n\t"
      "v_cvt_pk_bf16_f32 v59, v142, v143\n\t"
      "v_mfma_f32_16x16x32_bf16 v[220:223], v[112:115], v[96:99], v[220:223]\n\t"
      "ds_write_b128 v49, v[56:59] offset:0\n\t"
      "v_mfma_f32_16x16x32_bf16 v[220:223], v[116:119], v[100:103], v[220:223]\n\t"
      "v_cvt_pk_bf16_f32 v60, v144, v145\n\t"
      "v_mfma_f32_16x16x32_bf16 v[224:227], v[120:123], v[96:99], v[224:227]\n\t"
      "v_cvt_pk_bf16_f32 v61, v146, v147\n\t"
      "v_cvt_pk_bf16_f32 v62, v148, v149\n\t"
      "v_mfma_f32_16x16x32_bf16 v[224:227], v[124:127], v[100:103], v[224:227]\n\t"
      "v_cvt_pk_bf16_f32 v63, v150, v151\n\t"
      "ds_write_b128 v49, v[60:63] offset:1024\n\t"
      "v_mfma_f32_16x16x32_bf16 v[228:231], v[128:131], v[96:99], v[228:231]\n\t"
      "v_mfma_f32_16x16x32_bf16 v[228:231], v[132:135], v[100:103], v[228:231]\n\t"
      "v_cvt_pk_bf16_f32 v64, v152, v153\n\t"
      "v_cvt_pk_bf16_f32 v65, v154, v155\n\t"
      "s_waitcnt lgkmcnt(4)\n\t"
      "v_mfma_f32_16x16x32_bf16 v[232:235], v[104:107], v[88:91], v[232:235]\n\t"
      "v_cvt_pk_bf16_f32 v66, v156, v157\n\t"
      "v_cvt_pk_bf16_f32 v67, v158, v159\n\t"
      "s_waitcnt lgkmcnt(3)\n\t"
      "v_mfma_f32_16x16x32_bf16 v[232:235], v[108:111], v[92:95], v[232:235]\n\t"
      "ds_write_b128 v49, v[64:67] offset:8192\n\t"
      "v_mfma_f32_16x16x32_bf16 v[236:239], v[112:115], v[88:91], v[236:239]\n\t"
      "v_cvt_pk_bf16_f32 v68, v160, v161\n\t"
      "v_mfma_f32_16x16x32_bf16 v[236:239], v[116:119], v[92:95], v[236:239]\n\t"
      "v_cvt_pk_bf16_f32 v69, v162, v163\n\t"
      "v_cvt_pk_bf16_f32 v70, v164, v165\n\t"
      "v_mfma_f32_16x16x32_bf16 v[240:243], v[120:123], v[88:91], v[240:243]\n\t"
      "v_cvt_pk_bf16_f32 v71, v166, v167\n\t"
      "ds_write_b128 v49, v[68:71] offset:9216\n\t"
      "v_mfma_f32_16x16x32_bf16 v[240:243], v[124:127], v[92:95], v[240:243]\n\t"
      "v_mfma_f32_16x16x32_bf16 v[244:247], v[128:131], v[88:91], v[244:247]\n\t"
      "v_cvt_pk_bf16_f32 v56, v168, v169\n\t"
      "v_cvt_pk_bf16_f32 v57, v170, v171\n\t"
      "v_mfma_f32_16x16x32_bf16 v[244:247], v[132:135], v[92:95], v[244:247]\n\t"
      "v_cvt_pk_bf16_f32 v58, v172, v173\n\t"
      "v_cvt_pk_bf16_f32 v59, v174, v175\n\t"
      "ds_write_b128 v49, v[56:59] offset:16384\n\t"
      "v_cvt_pk_bf16_f32 v60, v176, v177\n\t"
      "v_cvt_pk_bf16_f32 v61, v178, v179\n\t"
      "v_cvt_pk_bf16_f32 v62, v180, v181\n\t"
      "v_cvt_pk_bf16_f32 v63, v182, v183\n\t"
      "ds_write_b128 v49, v[60:63] offset:17408\n\t"
      "v_cvt_pk_bf16_f32 v64, v184, v185\n\t"
      "v_cvt_pk_bf16_f32 v65, v186, v187\n\t"
      "v_cvt_pk_bf16_f32 v66, v188, v189\n\t"
      "v_cvt_pk_bf16_f32 v67, v190, v191\n\t"
      "ds_write_b128 v49, v[64:67] offset:24576\n\t"
      "v_cvt_pk_bf16_f32 v68, v192, v193\n\t"
      "v_cvt_pk_bf16_f32 v69, v194, v195\n\t"
      "v_cvt_pk_bf16_f32 v70, v196, v197\n\t"
      "v_cvt_pk_bf16_f32 v71, v198, v199\n\t"
      "ds_write_b128 v49, v[68:71] offset:25600\n\t"
      "v_cvt_pk_bf16_f32 v56, v200, v201\n\t"
      "v_cvt_pk_bf16_f32 v57, v202, v203\n\t"
      "v_cvt_pk_bf16_f32 v58, v204, v205\n\t"
      "v_cvt_pk_bf16_f32 v59, v206, v207\n\t"
      "ds_write_b128 v49, v[56:59] offset:32768\n\t"
      "v_cvt_pk_bf16_f32 v60, v208, v209\n\t"
      "v_cvt_pk_bf16_f32 v61, v210, v211\n\t"
      "v_cvt_pk_bf16_f32 v62, v212, v213\n\t"
      "v_cvt_pk_bf16_f32 v63, v214, v215\n\t"
      "ds_write_b128 v49, v[60:63] offset:33792\n\t"
      "v_cvt_pk_bf16_f32 v64, v216, v217\n\t"
      "v_cvt_pk_bf16_f32 v65, v218, v219\n\t"
      "v_cvt_pk_bf16_f32 v66, v220, v221\n\t"
      "v_cvt_pk_bf16_f32 v67, v222, v223\n\t"
      "ds_write_b128 v49, v[64:67] offset:40960\n\t"
      "v_cvt_pk_bf16_f32 v68, v224, v225\n\t"
      "v_cvt_pk_bf16_f32 v69, v226, v227\n\t"
      "v_cvt_pk_bf16_f32 v70, v228, v229\n\t"
      "v_cvt_pk_bf16_f32 v71, v230, v231\n\t"
      "ds_write_b128 v49, v[68:71] offset:41984\n\t"
      "v_cvt_pk_bf16_f32 v56, v232, v233\n\t"
      "v_cvt_pk_bf16_f32 v57, v234, v235\n\t"
      "v_cvt_pk_bf16_f32 v58, v236, v237\n\t"
      "v_cvt_pk_bf16_f32 v59, v238, v239\n\t"
      "ds_write_b128 v49, v[56:59] offset:49152\n\t"
      "v_cvt_pk_bf16_f32 v60, v240, v241\n\t"
      "v_cvt_pk_bf16_f32 v61, v242, v243\n\t"
      "v_cvt_pk_bf16_f32 v62, v244, v245\n\t"
      "v_cvt_pk_bf16_f32 v63, v246, v247\n\t"
      "ds_write_b128 v49, v[60:63] offset:50176\n\t"
      "s_waitcnt vmcnt(0)\n\t"
      "s_waitcnt lgkmcnt(0)\n\t"
      "s_barrier\n\t"
      "s_cmp_lg_u32 s99, 0\n\t"
      "s_cbranch_scc1 LOK%=\n\t"
      "s_cmp_lg_u64 s[30:31], 0\n\t"
      "s_cbranch_scc1 FB%=\n\t"
      "LOK%=:\n\t"
      "ds_read_b128 a[0:3], v50 offset:0\n\t"
      "ds_read_b128 a[4:7], v50 offset:1024\n\t"
      "ds_read_b128 a[8:11], v50 offset:2048\n\t"
      "ds_read_b128 a[12:15], v50 offset:3072\n\t"
      "ds_read_b128 a[16:19], v50 offset:4096\n\t"
      "ds_read_b128 a[20:23], v50 offset:5120\n\t"
      "ds_read_b128 a[24:27], v50 offset:6144\n\t"
      "ds_read_b128 a[28:31], v50 offset:7168\n\t"
      "ds_read_b128 a[32:35], v50 offset:8192\n\t"
      "ds_read_b128 a[36:39], v50 offset:9216\n\t"
      "ds_read_b128 a[40:43], v50 offset:10240\n\t"
      "ds_read_b128 a[44:47], v50 offset:11264\n\t"
      "s_waitcnt lgkmcnt(11)\n\t"
      "v_mfma_f32_16x16x32_bf16 v[16:19], a[224:227], a[0:3], 0\n\t"
      "ds_read_b128 a[48:51], v50 offset:12288\n\t"
      "ds_read_b128 a[52:55], v50 offset:13312\n\t"
      "s_waitcnt lgkmcnt(12)\n\t"
      "v_mfma_f32_16x16x32_bf16 v[16:19], a[228:231], a[4:7], v[16:19]\n\t"
      "ds_read_b128 a[56:59], v50 offset:14336\n\t"
      "ds_read_b128 a[60:63], v50 offset:15360\n\t"
      "s_waitcnt lgkmcnt(13)\n\t"
      "v_mfma_f32_16x16x32_bf16 v[16:19], a[232:235], a[8:11], v[16:19]\n\t"
      "s_waitcnt lgkmcnt(12)\n\t"
      "v_mfma_f32_16x16x32_bf16 v[16:19], a[236:239], a[12:15], v[16:19]\n\t"
      "s_waitcnt lgkmcnt(11)\n\t"
      "v_mfma_f32_16x16x32_bf16 v[16:19], a[240:243], a[16:19], v[16:19]\n\t"
      "ds_read_b128 a[64:67], v50 offset:16384\n\t"
      "ds_read_b128 a[68:71], v50 offset:17408\n\t"
      "s_waitcnt lgkmcnt(12)\n\t"
      "v_mfma_f32_16x16x32_bf16 v[16:19], a[244:247], a[20:23], v[16:19]\n\t"
      "ds_read_b128 a[72:75], v50 offset:18432\n\t"
      "ds_read_b128 a[76:79], v50 offset:19456\n\t"
      "s_waitcnt lgkmcnt(13)\n\t"
      "v_mfma_f32_16x16x32_bf16 v[16:19], a[248:251], a[24:27], v[16:19]\n\t"
      "ds_read_b128 a[80:83], v50 offset:20480\n\t"
      "ds_read_b128 a[84:87], v50 offset:21504\n\t"
      "s_waitcnt lgkmcnt(14)\n\t"
      "v_mfma_f32_16x16x32_bf16 v[16:19], a[252:255], a[28:31], v[16:19]\n\t"
      "ds_read_b128 a[88:91], v50 offset:22528\n\t"
      "ds_read_b128 a[92:95], v50 offset:23552\n\t"
      "s_waitcnt lgkmcnt(15)\n\t"
      "v_mfma_f32_16x16x32_bf16 v[20:23], a[224:227], a[32:35], 0\n\t"
      "s_lshl_b32 s92, s100, 7\n\t"
      "s_add_u32 s52, s97, s92\n\t"
      "s_waitcnt lgkmcnt(14)\n\t"
      "v_mfma_f32_16x16x32_bf16 v[20:23], a[228:231], a[36:39], v[20:23]\n\t"
      "s_addc_u32 s53, s98, 0\n\t"
      "s_add_u32 m0, s89, 131072\n\t"
      "s_waitcnt lgkmcnt(13)\n\t"
      "v_mfma_f32_16x16x32_bf16 v[20:23], a[232:235], a[40:43], v[20:23]\n\t"
      "buffer_load_dwordx4 v254, s[52:55], s76 offen offset:0 lds\n\t"
      "buffer_load_dwordx4 v254, s[52:55], s77 offen offset:1024 lds\n\t"
      "s_waitcnt lgkmcnt(12)\n\t"
      "v_mfma_f32_16x16x32_bf16 v[20:23], a[236:239], a[44:47], v[20:23]\n\t"
      "v_mul_f32 v16, v16, v72\n\t"
      "v_mul_f32 v17, v17, v72\n\t"
      "s_waitcnt lgkmcnt(11)\n\t"
      "v_mfma_f32_16x16x32_bf16 v[20:23], a[240:243], a[48:51], v[20:23]\n\t"
      "ds_read_b128 a[96:99], v50 offset:24576\n\t"
      "ds_read_b128 a[100:103], v50 offset:25600\n\t"
      "s_waitcnt lgkmcnt(12)\n\t"
      "v_mfma_f32_16x16x32_bf16 v[20:23], a[244:247], a[52:55], v[20:23]\n\t"
      "ds_read_b128 a[104:107], v50 offset:26624\n\t"
      "ds_read_b128 a[108:111], v50 offset:27648\n\t"
      "s_waitcnt lgkmcnt(13)\n\t"
      "v_mfma_f32_16x16x32_bf16 v[20:23], a[248:251], a[56:59], v[20:23]\n\t"
      "ds_read_b128 a[112:115], v50 offset:28672\n\t"
      "ds_read_b128 a[116:119], v50 offset:29696\n\t"
      "s_waitcnt lgkmcnt(14)\n\t"
      "v_mfma_f32_16x16x32_bf16 v[20:23], a[252:255], a[60:63], v[20:23]\n\t"
      "ds_read_b128 a[120:123], v50 offset:30720\n\t"
      "ds_read_b128 a[124:127], v50 offset:31744\n\t"
      "s_waitcnt lgkmcnt(15)\n\t"
      "v_mfma_f32_16x16x32_bf16 v[24:27], a[224:227], a[64:67], 0\n\t"
      "buffer_load_dwordx4 v254, s[52:55], s78 offen offset:2048 lds\n\t"
      "buffer_load_dwordx4 v254, s[52:55], s79 offen offset:3072 lds\n\t"
      "s_waitcnt lgkmcnt(14)\n\t"
      "v_mfma_f32_16x16x32_bf16 v[24:27], a[228:231], a[68:71], v[24:27]\n\t"
      "v_mul_f32 v18, v18, v72\n\t"
      "v_mul_f32 v19, v19, v72\n\t"
      "s_waitcnt lgkmcnt(13)\n\t"
      "v_mfma_f32_16x16x32_bf16 v[24:27], a[232:235], a[72:75], v[24:27]\n\t"
      "v_cvt_pk_bf16_f32 v56, v16, v17\n\t"
      "v_cvt_pk_bf16_f32 v57, v18, v19\n\t"
      "s_waitcnt lgkmcnt(12)\n\t"
      "v_mfma_f32_16x16x32_bf16 v[24:27], a[236:239], a[76:79], v[24:27]\n\t"
      "v_mul_f32 v20, v20, v73\n\t"
      "v_mul_f32 v21, v21, v73\n\t"
      "s_waitcnt lgkmcnt(11)\n\t"
      "v_mfma_f32_16x16x32_bf16 v[24:27], a[240:243], a[80:83], v[24:27]\n\t"
      "ds_read_b128 a[128:131], v50 offset:32768\n\t"
      "ds_read_b128 a[132:135], v50 offset:33792\n\t"
      "s_waitcnt lgkmcnt(12)\n\t"
      "v_mfma_f32_16x16x32_bf16 v[24:27], a[244:247], a[84:87], v[24:27]\n\t"
      "ds_read_b128 a[136:139], v50 offset:34816\n\t"
      "ds_read_b128 a[140:143], v50 offset:35840\n\t"
      "s_waitcnt lgkmcnt(13)\n\t"
      "v_mfma_f32_16x16x32_bf16 v[24:27], a[248:251], a[88:91], v[24:27]\n\t"
      "ds_read_b128 a[144:147], v50 offset:36864\n\t"
      "ds_read_b128 a[148:151], v50 offset:37888\n\t"
      "s_waitcnt lgkmcnt(14)\n\t"
      "v_mfma_f32_16x16x32_bf16 v[24:27], a[252:255], a[92:95], v[24:27]\n\t"
      "ds_read_b128 a[152:155], v50 offset:38912\n\t"
      "ds_read_b128 a[156:159], v50 offset:39936\n\t"
      "s_waitcnt lgkmcnt(15)\n\t"
      "v_mfma_f32_16x16x32_bf16 v[28:31], a[224:227], a[96:99], 0\n\t"
      "s_add_u32 m0, s89, 135168\n\t"
      "s_nop 0\n\t"
      "buffer_load_dwordx4 v254, s[52:55], s80 offen offset:0 lds\n\t"
      "s_waitcnt lgkmcnt(14)\n\t"
      "v_mfma_f32_16x16x32_bf16 v[28:31], a[228:231], a[100:103], v[28:31]\n\t"
      "buffer_load_dwordx4 v254, s[52:55], s81 offen offset:1024 lds\n\t"
      "v_mul_f32 v22, v22, v73\n\t"
      "s_waitcnt lgkmcnt(13)\n\t"
      "v_mfma_f32_16x16x32_bf16 v[28:31], a[232:235], a[104:107], v[28:31]\n\t"
      "v_mul_f32 v23, v23, v73\n\t"
      "v_cvt_pk_bf16_f32 v58, v20, v21\n\t"
      "s_waitcnt lgkmcnt(12)\n\t"
      "v_mfma_f32_16x16x32_bf16 v[28:31], a[236:239], a[108:111], v[28:31]\n\t"
      "v_cvt_pk_bf16_f32 v59, v22, v23\n\t"
      "s_lshl_b32 s95, s101, 14\n\t"
      "s_waitcnt lgkmcnt(11)\n\t"
      "v_mfma_f32_16x16x32_bf16 v[28:31], a[240:243], a[112:115], v[28:31]\n\t"
      "ds_read_b128 a[160:163], v50 offset:40960\n\t"
      "ds_read_b128 a[164:167], v50 offset:41984\n\t"
      "s_waitcnt lgkmcnt(12)\n\t"
      "v_mfma_f32_16x16x32_bf16 v[28:31], a[244:247], a[116:119], v[28:31]\n\t"
      "ds_read_b128 a[168:171], v50 offset:43008\n\t"
      "ds_read_b128 a[172:175], v50 offset:44032\n\t"
      "s_waitcnt lgkmcnt(13)\n\t"
      "v_mfma_f32_16x16x32_bf16 v[28:31], a[248:251], a[120:123], v[28:31]\n\t"
      "ds_read_b128 a[176:179], v50 offset:45056\n\t"
      "ds_read_b128 a[180:183], v50 offset:46080\n\t"
      "s_waitcnt lgkmcnt(14)\n\t"
      "v_mfma_f32_16x16x32_bf16 v[28:31], a[252:255], a[124:127], v[28:31]\n\t"
      "ds_read_b128 a[184:187], v50 offset:47104\n\t"
      "ds_read_b128 a[188:191], v50 offset:48128\n\t"
      "s_waitcnt lgkmcnt(15)\n\t"
      "v_mfma_f32_16x16x32_bf16 v[32:35], a[224:227], a[128:131], 0\n\t"
      "buffer_load_dwordx4 v254, s[52:55], s82 offen offset:2048 lds\n\t"
      "buffer_load_dwordx4 v254, s[52:55], s83 offen offset:3072 lds\n\t"
      "s_waitcnt lgkmcnt(14)\n\t"
      "v_mfma_f32_16x16x32_bf16 v[32:35], a[228:231], a[132:135], v[32:35]\n\t"
      "s_add_u32 s100, s100, 1\n\t"
      "s_cmp_eq_u32 s100, 7\n\t"
      "s_waitcnt lgkmcnt(13)\n\t"
      "v_mfma_f32_16x16x32_bf16 v[32:35], a[232:235], a[136:139], v[32:35]\n\t"
      "s_cselect_b32 s100, 0, s100\n\t"
      "ds_read_b64_tr_b16 v[104:105], v44 offset:0\n\t"
      "s_waitcnt lgkmcnt(13)\n\t"
      "v_mfma_f32_16x16x32_bf16 v[32:35], a[236:239], a[140:143], v[32:35]\n\t"
      "ds_read_b64_tr_b16 v[106:107], v44 offset:2048\n\t"
      "ds_read_b64_tr_b16 v[108:109], v44 offset:4096\n\t"
      "s_waitcnt lgkmcnt(14)\n\t"
      "v_mfma_f32_16x16x32_bf16 v[32:35], a[240:243], a[144:147], v[32:35]\n\t"
      "ds_read_b128 a[192:195], v50 offset:49152\n\t"
      "ds_read_b128 a[196:199], v50 offset:50176\n\t"
      "s_waitcnt lgkmcnt(15)\n\t"
      "v_mfma_f32_16x16x32_bf16 v[32:35], a[244:247], a[148:151], v[32:35]\n\t"
      "ds_read_b128 a[200:203], v50 offset:51200\n\t"
      "ds_read_b128 a[204:207], v50 offset:52224\n\t"
      "s_waitcnt lgkmcnt(15)\n\t"
      "v_mfma_f32_16x16x32_bf16 v[32:35], a[248:251], a[152:155], v[32:35]\n\t"
      "ds_read_b128 a[208:211], v50 offset:53248\n\t"
      "ds_read_b128 a[212:215], v50 offset:54272\n\t"
      "v_mfma_f32_16x16x32_bf16 v[32:35], a[252:255], a[156:159], v[32:35]\n\t"
      "ds_read_b128 a[216:219], v50 offset:55296\n\t"
      "ds_read_b128 a[220:223], v50 offset:56320\n\t"
      "s_waitcnt lgkmcnt(15)\n\t"
      "v_mfma_f32_16x16x32_bf16 v[36:39], a[224:227], a[160:163], 0\n\t"
      "ds_read_b64_tr_b16 v[110:111], v44 offset:6144\n\t"
      "ds_read_b64_tr_b16 v[112:113], v44 offset:8192\n\t"
      "v_mfma_f32_16x16x32_bf16 v[36:39], a[228:231], a[164:167], v[36:39]\n\t"
      "ds_read_b64_tr_b16 v[114:115], v44 offset:10240\n\t"
      "ds_read_b64_tr_b16 v[116:117], v44 offset:12288\n\t"
      "v_mfma_f32_16x16x32_bf16 v[36:39], a[232:235], a[168:171], v[36:39]\n\t"
      "ds_read_b64_tr_b16 v[118:119], v44 offset:14336\n\t"
      "ds_read_b64_tr_b16 v[120:121], v44 offset:16384\n\t"
      "v_mfma_f32_16x16x32_bf16 v[36:39], a[236:239], a[172:175], v[36:39]\n\t"
      "ds_read_b64_tr_b16 v[122:123], v44 offset:18432\n\t"
      "ds_read_b64_tr_b16 v[124:125], v44 offset:20480\n\t"
      "s_waitcnt lgkmcnt(15)\n\t"
      "v_mfma_f32_16x16x32_bf16 v[36:39], a[240:243], a[176:179], v[36:39]\n\t"
      "ds_read_b64_tr_b16 v[126:127], v44 offset:22528\n\t"
      "ds_read_b64_tr_b16 v[128:129], v44 offset:24576\n\t"
      "v_mfma_f32_16x16x32_bf16 v[36:39], a[244:247], a[180:183], v[36:39]\n\t"
      "ds_read_b64_tr_b16 v[130:131], v44 offset:26624\n\t"
      "ds_read_b64_tr_b16 v[132:133], v44 offset:28672\n\t"
      "v_mfma_f32_16x16x32_bf16 v[36:39], a[248:251], a[184:187], v[36:39]\n\t"
      "ds_read_b64_tr_b16 v[134:135], v44 offset:30720\n\t"
      "s_lshl_b32 s96, s88, 10\n\t"
      "v_mfma_f32_16x16x32_bf16 v[36:39], a[252:255], a[188:191], v[36:39]\n\t"
      "s_add_u32 s95, s95, s96\n\t"
      "s_add_u32 s96, s95, 0\n\t"
      "v_mfma_f32_16x16x32_bf16 v[40:43], a[224:227], a[192:195], 0\n\t"
      "buffer_store_dwordx4 v[56:59], v47, s[56:59], s96 offen offset:0 sc1\n\t"
      "v_mul_f32 v24, v24, v74\n\t"
      "s_waitcnt lgkmcnt(15)\n\t"
      "v_mfma_f32_16x16x32_bf16 v[40:43], a[228:231], a[196:199], v[40:43]\n\t"
      "v_mul_f32 v25, v25, v74\n\t"
      "v_mul_f32 v26, v26, v74\n\t"
      "v_mfma_f32_16x16x32_bf16 v[40:43], a[232:235], a[200:203], v[40:43]\n\t"
      "v_mul_f32 v27, v27, v74\n\t"
      "v_cvt_pk_bf16_f32 v60, v24, v25\n\t"
      "v_mfma_f32_16x16x32_bf16 v[40:43], a[236:239], a[204:207], v[40:43]\n\t"
      "v_cvt_pk_bf16_f32 v61, v26, v27\n\t"
      "v_mul_f32 v28, v28, v75\n\t"
      "v_mfma_f32_16x16x32_bf16 v[40:43], a[240:243], a[208:211], v[40:43]\n\t"
      "s_waitcnt lgkmcnt(13)\n\t"
      "s_waitcnt lgkmcnt(0)\n\t"
      "v_mfma_f32_16x16x32_bf16 v[40:43], a[244:247], a[212:215], v[40:43]\n\t"
      "s_barrier\n\t"
      "s_lshl_b32 s92, s100, 7\n\t"
      "v_mfma_f32_16x16x32_bf16 v[40:43], a[248:251], a[216:219], v[40:43]\n\t"
      "s_add_u32 s52, s97, s92\n\t"
      "s_addc_u32 s53, s98, 0\n\t"
      "v_mfma_f32_16x16x32_bf16 v[40:43], a[252:255], a[220:223], v[40:43]\n\t"
      "s_add_u32 m0, s89, 32768\n\t"
      "s_nop 0\n\t"
      "buffer_load_dwordx4 v254, s[52:55], s76 offen offset:0 lds\n\t"
      "buffer_load_dwordx4 v254, s[52:55], s77 offen offset:1024 lds\n\t"
      "buffer_load_dwordx4 v254, s[52:55], s78 offen offset:2048 lds\n\t"
      "buffer_load_dwordx4 v254, s[52:55], s79 offen offset:3072 lds\n\t"
      "s_add_u32 m0, s89, 36864\n\t"
      "s_nop 0\n\t"
      "buffer_load_dwordx4 v254, s[52:55], s80 offen offset:0 lds\n\t"
      "buffer_load_dwordx4 v254, s[52:55], s81 offen offset:1024 lds\n\t"
      "buffer_load_dwordx4 v254, s[52:55], s82 offen offset:2048 lds\n\t"
      "buffer_load_dwordx4 v254, s[52:55], s83 offen offset:3072 lds\n\t"
      "s_add_u32 s100, s100, 1\n\t"
      "s_cmp_eq_u32 s100, 7\n\t"
      "s_cselect_b32 s100, 0, s100\n\t"
      "v_mul_f32 v29, v29, v75\n\t"
      "v_mul_f32 v30, v30, v75\n\t"
      "v_mul_f32 v31, v31, v75\n\t"
      "v_cvt_pk_bf16_f32 v62, v28, v29\n\t"
      "v_cvt_pk_bf16_f32 v63, v30, v31\n\t"
      "s_add_u32 s96, s95, 4096\n\t"
      "buffer_store_dwordx4 v[60:63], v47, s[56:59], s96 offen offset:0 sc1\n\t"
      "v_mul_f32 v32, v32, v76\n\t"
      "v_mul_f32 v33, v33, v76\n\t"
      "v_mul_f32 v34, v34, v76\n\t"
      "v_mul_f32 v35, v35, v76\n\t"
      "v_cvt_pk_bf16_f32 v56, v32, v33\n\t"
      "v_cvt_pk_bf16_f32 v57, v34, v35\n\t"
      "v_mul_f32 v36, v36, v77\n\t"
      "v_mul_f32 v37, v37, v77\n\t"
      "v_mul_f32 v38, v38, v77\n\t"
      "v_mul_f32 v39, v39, v77\n\t"
      "v_cvt_pk_bf16_f32 v58, v36, v37\n\t"
      "v_cvt_pk_bf16_f32 v59, v38, v39\n\t"
      "s_add_u32 s96, s95, 8192\n\t"
      "buffer_store_dwordx4 v[56:59], v47, s[56:59], s96 offen offset:0 sc1\n\t"
      "v_mfma_f32_16x16x32_bf16 v[16:19], v[104:107], a[0:3], 0\n\t"
      "v_mfma_f32_16x16x32_bf16 v[16:19], v[108:111], a[4:7], v[16:19]\n\t"
      "v_mfma_f32_16x16x32_bf16 v[16:19], v[112:115], a[8:11], v[16:19]\n\t"
      "v_mul_f32 v40, v40, v78\n\t"
      "v_mul_f32 v41, v41, v78\n\t"
      "v_mfma_f32_16x16x32_bf16 v[16:19], v[116:119], a[12:15], v[16:19]\n\t"
      "v_mul_f32 v42, v42, v78\n\t"
      "v_mul_f32 v43, v43, v78\n\t"
      "v_mfma_f32_16x16x32_bf16 v[16:19], v[120:123], a[16:19], v[16:19]\n\t"
      "v_cvt_pk_bf16_f32 v60, v40, v41\n\t"
      "v_cvt_pk_bf16_f32 v61, v42, v43\n\t"
      "v_mfma_f32_16x16x32_bf16 v[16:19], v[124:127], a[20:23], v[16:19]\n\t"
      "s_add_u32 s96, s95, 12288\n\t"
      "buffer_store_dwordx2 v[60:61], v48, s[56:59], s96 offen offset:0 sc1\n\t"
      "v_mfma_f32_16x16x32_bf16 v[16:19], v[128:131], a[24:27], v[16:19]\n\t"
      "s_add_u32 s101, s101, 1\n\t"
      "s_cmp_eq_u32 s101, 7\n\t"
      "v_mfma_f32_16x16x32_bf16 v[16:19], v[132:135], a[28:31], v[16:19]\n\t"
      "s_cselect_b32 s101, 0, s101\n\t"
      "v_mfma_f32_16x16x32_bf16 v[20:23], v[104:107], a[32:35], 0\n\t"
      "v_mfma_f32_16x16x32_bf16 v[20:23], v[108:111], a[36:39], v[20:23]\n\t"
      "v_mfma_f32_16x16x32_bf16 v[20:23], v[112:115], a[40:43], v[20:23]\n\t"
      "v_mfma_f32_16x16x32_bf16 v[20:23], v[116:119], a[44:47], v[20:23]\n\t"
      "s_nop 2\n\t"
      "v_mul_f32 v16, v16, v72\n\t"
      "v_mul_f32 v17, v17, v72\n\t"
      "v_mfma_f32_16x16x32_bf16 v[20:23], v[120:123], a[48:51], v[20:23]\n\t"
      "v_mul_f32 v18, v18, v72\n\t"
      "v_mul_f32 v19, v19, v72\n\t"
      "v_mfma_f32_16x16x32_bf16 v[20:23], v[124:127], a[52:55], v[20:23]\n\t"
      "v_cvt_pk_bf16_f32 v56, v16, v17\n\t"
      "v_cvt_pk_bf16_f32 v57, v18, v19\n\t"
      "v_mfma_f32_16x16x32_bf16 v[20:23], v[128:131], a[56:59], v[20:23]\n\t"
      "v_mfma_f32_16x16x32_bf16 v[20:23], v[132:135], a[60:63], v[20:23]\n\t"
      "v_mfma_f32_16x16x32_bf16 v[24:27], v[104:107], a[64:67], 0\n\t"
      "v_mfma_f32_16x16x32_bf16 v[24:27], v[108:111], a[68:71], v[24:27]\n\t"
      "v_mfma_f32_16x16x32_bf16 v[24:27], v[112:115], a[72:75], v[24:27]\n\t"
      "v_mfma_f32_16x16x32_bf16 v[24:27], v[116:119], a[76:79], v[24:27]\n\t"
      "s_nop 3\n\t"
      "v_mul_f32 v20, v20, v73\n\t"
      "v_mul_f32 v21, v21, v73\n\t"
      "v_mfma_f32_16x16x32_bf16 v[24:27], v[120:123], a[80:83], v[24:27]\n\t"
      "s_waitcnt vmcnt(12)\n\t"
      "s_barrier\n\t"
      "v_mfma_f32_16x16x32_bf16 v[24:27], v[124:127], a[84:87], v[24:27]\n\t"
      "v_mul_f32 v22, v22, v73\n\t"
      "v_mul_f32 v23, v23, v73\n\t"
      "v_mfma_f32_16x16x32_bf16 v[24:27], v[128:131], a[88:91], v[24:27]\n\t"
      "ds_read_b64_tr_b16 a[224:225], v45 offset:0\n\t"
      "ds_read_b64_tr_b16 a[226:227], v45 offset:2048\n\t"
      "v_mfma_f32_16x16x32_bf16 v[24:27], v[132:135], a[92:95], v[24:27]\n\t"
      "ds_read_b64_tr_b16 a[228:229], v45 offset:4096\n\t"
      "ds_read_b64_tr_b16 a[230:231], v45 offset:6144\n\t"
      "v_mfma_f32_16x16x32_bf16 v[28:31], v[104:107], a[96:99], 0\n\t"
      "ds_read_b64_tr_b16 a[232:233], v45 offset:8192\n\t"
      "ds_read_b64_tr_b16 a[234:235], v45 offset:10240\n\t"
      "v_mfma_f32_16x16x32_bf16 v[28:31], v[108:111], a[100:103], v[28:31]\n\t"
      "ds_read_b64_tr_b16 a[236:237], v45 offset:12288\n\t"
      "ds_read_b64_tr_b16 a[238:239], v45 offset:14336\n\t"
      "v_mfma_f32_16x16x32_bf16 v[28:31], v[112:115], a[104:107], v[28:31]\n\t"
      "ds_read_b64_tr_b16 a[240:241], v45 offset:16384\n\t"
      "ds_read_b64_tr_b16 a[242:243], v45 offset:18432\n\t"
      "v_mfma_f32_16x16x32_bf16 v[28:31], v[116:119], a[108:111], v[28:31]\n\t"
      "ds_read_b64_tr_b16 a[244:245], v45 offset:20480\n\t"
      "ds_read_b64_tr_b16 a[246:247], v45 offset:22528\n\t"
      "v_mfma_f32_16x16x32_bf16 v[28:31], v[120:123], a[112:115], v[28:31]\n\t"
      "ds_read_b64_tr_b16 a[248:249], v45 offset:24576\n\t"
      "ds_read_b64_tr_b16 a[250:251], v45 offset:26624\n\t"
      "v_mfma_f32_16x16x32_bf16 v[28:31], v[124:127], a[116:119], v[28:31]\n\t"
      "ds_read_b64_tr_b16 a[252:253], v45 offset:28672\n\t"
      "ds_read_b64_tr_b16 a[254:255], v45 offset:30720\n\t"
      "v_mfma_f32_16x16x32_bf16 v[28:31], v[128:131], a[120:123], v[28:31]\n\t"
      "s_lshl_b32 s92, s100, 7\n\t"
      "s_add_u32 s52, s97, s92\n\t"
      "v_mfma_f32_16x16x32_bf16 v[28:31], v[132:135], a[124:127], v[28:31]\n\t"
      "s_addc_u32 s53, s98, 0\n\t"
      "s_add_u32 m0, s89, 0\n\t"
      "v_mfma_f32_16x16x32_bf16 v[32:35], v[104:107], a[128:131], 0\n\t"
      "buffer_load_dwordx4 v254, s[52:55], s76 offen offset:0 lds\n\t"
      "buffer_load_dwordx4 v254, s[52:55], s77 offen offset:1024 lds\n\t"
      "v_mfma_f32_16x16x32_bf16 v[32:35], v[108:111], a[132:135], v[32:35]\n\t"
      "buffer_load_dwordx4 v254, s[52:55], s78 offen offset:2048 lds\n\t"
      "v_cvt_pk_bf16_f32 v58, v20, v21\n\t"
      "v_mfma_f32_16x16x32_bf16 v[32:35], v[112:115], a[136:139], v[32:35]\n\t"
      "buffer_load_dwordx4 v254, s[52:55], s79 offen offset:3072 lds\n\t"
      "v_cvt_pk_bf16_f32 v59, v22, v23\n\t"
      "v_mfma_f32_16x16x32_bf16 v[32:35], v[116:119], a[140:143], v[32:35]\n\t"
      "s_lshl_b32 s95, s101, 14\n\t"
      "s_lshl_b32 s96, s88, 10\n\t"
      "v_mfma_f32_16x16x32_bf16 v[32:35], v[120:123], a[144:147], v[32:35]\n\t"
      "s_add_u32 s95, s95, s96\n\t"
      "s_add_u32 s96, s95, 0\n\t"
      "v_mfma_f32_16x16x32_bf16 v[32:35], v[124:127], a[148:151], v[32:35]\n\t"
      "buffer_store_dwordx4 v[56:59], v47, s[56:59], s96 offen offset:0 sc1\n\t"
      "v_mul_f32 v24, v24, v74\n\t"
      "v_mfma_f32_16x16x32_bf16 v[32:35], v[128:131], a[152:155], v[32:35]\n\t"
      "s_add_u32 m0, s89, 4096\n\t"
      "s_nop 0\n\t"
      "buffer_load_dwordx4 v254, s[52:55], s80 offen offset:0 lds\n\t"
      "v_mfma_f32_16x16x32_bf16 v[32:35], v[132:135], a[156:159], v[32:35]\n\t"
      "v_mul_f32 v25, v25, v74\n\t"
      "v_mul_f32 v26, v26, v74\n\t"
      "v_mfma_f32_16x16x32_bf16 v[36:39], v[104:107], a[160:163], 0\n\t"
      "v_mul_f32 v27, v27, v74\n\t"
      "v_cvt_pk_bf16_f32 v60, v24, v25\n\t"
      "v_mfma_f32_16x16x32_bf16 v[36:39], v[108:111], a[164:167], v[36:39]\n\t"
      "v_cvt_pk_bf16_f32 v61, v26, v27\n\t"
      "v_mul_f32 v28, v28, v75\n\t"
      "v_mfma_f32_16x16x32_bf16 v[36:39], v[112:115], a[168:171], v[36:39]\n\t"
      "buffer_load_dwordx4 v254, s[52:55], s81 offen offset:1024 lds\n\t"
      "v_mul_f32 v29, v29, v75\n\t"
      "v_mfma_f32_16x16x32_bf16 v[36:39], v[116:119], a[172:175], v[36:39]\n\t"
      "v_mul_f32 v30, v30, v75\n\t"
      "v_mul_f32 v31, v31, v75\n\t"
      "v_mfma_f32_16x16x32_bf16 v[36:39], v[120:123], a[176:179], v[36:39]\n\t"
      "v_cvt_pk_bf16_f32 v62, v28, v29\n\t"
      "v_cvt_pk_bf16_f32 v63, v30, v31\n\t"
      "v_mfma_f32_16x16x32_bf16 v[36:39], v[124:127], a[180:183], v[36:39]\n\t"
      "s_add_u32 s96, s95, 4096\n\t"
      "buffer_store_dwordx4 v[60:63], v47, s[56:59], s96 offen offset:0 sc1\n\t"
      "v_mfma_f32_16x16x32_bf16 v[36:39], v[128:131], a[184:187], v[36:39]\n\t"
      "buffer_load_dwordx4 v254, s[52:55], s82 offen offset:2048 lds\n\t"
      "v_mul_f32 v32, v32, v76\n\t"
      "v_mfma_f32_16x16x32_bf16 v[36:39], v[132:135], a[188:191], v[36:39]\n\t"
      "v_mul_f32 v33, v33, v76\n\t"
      "v_mul_f32 v34, v34, v76\n\t"
      "v_mfma_f32_16x16x32_bf16 v[40:43], v[104:107], a[192:195], 0\n\t"
      "v_mul_f32 v35, v35, v76\n\t"
      "v_cvt_pk_bf16_f32 v56, v32, v33\n\t"
      "v_mfma_f32_16x16x32_bf16 v[40:43], v[108:111], a[196:199], v[40:43]\n\t"
      "v_cvt_pk_bf16_f32 v57, v34, v35\n\t"
      "v_mfma_f32_16x16x32_bf16 v[40:43], v[112:115], a[200:203], v[40:43]\n\t"
      "buffer_load_dwordx4 v254, s[52:55], s83 offen offset:3072 lds\n\t"
      "s_add_u32 s100, s100, 1\n\t"
      "v_mfma_f32_16x16x32_bf16 v[40:43], v[116:119], a[204:207], v[40:43]\n\t"
      "s_cmp_eq_u32 s100, 7\n\t"
      "s_cselect_b32 s100, 0, s100\n\t"
      "v_mfma_f32_16x16x32_bf16 v[40:43], v[120:123], a[208:211], v[40:43]\n\t"
      "v_mul_f32 v36, v36, v77\n\t"
      "v_mul_f32 v37, v37, v77\n\t"
      "v_mfma_f32_16x16x32_bf16 v[40:43], v[124:127], a[212:215], v[40:43]\n\t"
      "v_mul_f32 v38, v38, v77\n\t"
      "v_mul_f32 v39, v39, v77\n\t"
      "v_mfma_f32_16x16x32_bf16 v[40:43], v[128:131], a[216:219], v[40:43]\n\t"
      "v_cvt_pk_bf16_f32 v58, v36, v37\n\t"
      "v_cvt_pk_bf16_f32 v59, v38, v39\n\t"
      "v_mfma_f32_16x16x32_bf16 v[40:43], v[132:135], a[220:223], v[40:43]\n\t"
      "s_add_u32 s96, s95, 8192\n\t"
      "buffer_store_dwordx4 v[56:59], v47, s[56:59], s96 offen offset:0 sc1\n\t"
      "s_waitcnt lgkmcnt(14)\n\t"
      "v_mfma_f32_16x16x32_bf16 v[16:19], a[224:227], a[0:3], 0\n\t"
      "s_waitcnt lgkmcnt(12)\n\t"
      "v_mfma_f32_16x16x32_bf16 v[16:19], a[228:231], a[4:7], v[16:19]\n\t"
      "s_waitcnt lgkmcnt(10)\n\t"
      "v_mfma_f32_16x16x32_bf16 v[16:19], a[232:235], a[8:11], v[16:19]\n\t"
      "v_mul_f32 v40, v40, v78\n\t"
      "v_mul_f32 v41, v41, v78\n\t"
      "s_waitcnt lgkmcnt(8)\n\t"
      "v_mfma_f32_16x16x32_bf16 v[16:19], a[236:239], a[12:15], v[16:19]\n\t"
      "v_mul_f32 v42, v42, v78\n\t"
      "v_mul_f32 v43, v43, v78\n\t"
      "s_waitcnt lgkmcnt(6)\n\t"
      "v_mfma_f32_16x16x32_bf16 v[16:19], a[240:243], a[16:19], v[16:19]\n\t"
      "v_cvt_pk_bf16_f32 v60, v40, v41\n\t"
      "v_cvt_pk_bf16_f32 v61, v42, v43\n\t"
      "s_waitcnt lgkmcnt(4)\n\t"
      "v_mfma_f32_16x16x32_bf16 v[16:19], a[244:247], a[20:23], v[16:19]\n\t"
      "s_add_u32 s96, s95, 12288\n\t"
      "buffer_store_dwordx2 v[60:61], v48, s[56:59], s96 offen offset:0 sc1\n\t"
      "s_waitcnt lgkmcnt(2)\n\t"
      "v_mfma_f32_16x16x32_bf16 v[16:19], a[248:251], a[24:27], v[16:19]\n\t"
      "s_add_u32 s101, s101, 1\n\t"
      "s_cmp_eq_u32 s101, 7\n\t"
      "s_waitcnt lgkmcnt(0)\n\t"
      "v_mfma_f32_16x16x32_bf16 v[16:19], a[252:255], a[28:31], v[16:19]\n\t"
      "s_cselect_b32 s101, 0, s101\n\t"
      "v_mfma_f32_16x16x32_bf16 v[20:23], a[224:227], a[32:35], 0\n\t"
      "v_mfma_f32_16x16x32_bf16 v[20:23], a[228:231], a[36:39], v[20:23]\n\t"
      "v_mfma_f32_16x16x32_bf16 v[20:23], a[232:235], a[40:43], v[20:23]\n\t"
      "v_mfma_f32_16x16x32_bf16 v[20:23], a[236:239], a[44:47], v[20:23]\n\t"
      "s_nop 2\n\t"
      "v_mul_f32 v16, v16, v72\n\t"
      "v_mul_f32 v17, v17, v72\n\t"
      "v_mfma_f32_16x16x32_bf16 v[20:23], a[240:243], a[48:51], v[20:23]\n\t"
      "v_mul_f32 v18, v18, v72\n\t"
      "v_mul_f32 v19, v19, v72\n\t"
      "v_mfma_f32_16x16x32_bf16 v[20:23], a[244:247], a[52:55], v[20:23]\n\t"
      "v_cvt_pk_bf16_f32 v56, v16, v17\n\t"
      "v_cvt_pk_bf16_f32 v57, v18, v19\n\t"
      "v_mfma_f32_16x16x32_bf16 v[20:23], a[248:251], a[56:59], v[20:23]\n\t"
      "v_mfma_f32_16x16x32_bf16 v[20:23], a[252:255], a[60:63], v[20:23]\n\t"
      "v_mfma_f32_16x16x32_bf16 v[24:27], a[224:227], a[64:67], 0\n\t"
      "v_mfma_f32_16x16x32_bf16 v[24:27], a[228:231], a[68:71], v[24:27]\n\t"
      "v_mfma_f32_16x16x32_bf16 v[24:27], a[232:235], a[72:75], v[24:27]\n\t"
      "v_mfma_f32_16x16x32_bf16 v[24:27], a[236:239], a[76:79], v[24:27]\n\t"
      "s_nop 3\n\t"
      "v_mul_f32 v20, v20, v73\n\t"
      "v_mul_f32 v21, v21, v73\n\t"
      "v_mfma_f32_16x16x32_bf16 v[24:27], a[240:243], a[80:83], v[24:27]\n\t"
      "s_waitcnt vmcnt(15)\n\t"
      "s_waitcnt vmcnt(12)\n\t"
      "v_mfma_f32_16x16x32_bf16 v[24:27], a[244:247], a[84:87], v[24:27]\n\t"
      "s_barrier\n\t"
      "s_lshl_b32 s3, s93, 6\n\t"
      "s_lshl_b32 s2, s7, 2\n\t"
      "s_add_u32 s3, s3, s2\n\t"
      "v_mov_b32 v87, s3\n\t"
      "s_mov_b32 s46, s28\n\t"
      "buffer_store_dword v86, v87, s[44:47], 0 offen offset:0 sc1\n\t"
      "s_add_u32 s93, s93, 1\n\t"
      "s_cmp_eq_u32 s93, 7\n\t"
      "s_cselect_b32 s93, 0, s93\n\t"
      "v_mfma_f32_16x16x32_bf16 v[24:27], a[248:251], a[88:91], v[24:27]\n\t"
      "v_mfma_f32_16x16x32_bf16 v[24:27], a[252:255], a[92:95], v[24:27]\n\t"
      "v_mfma_f32_16x16x32_bf16 v[28:31], a[224:227], a[96:99], 0\n\t"
      "v_mfma_f32_16x16x32_bf16 v[28:31], a[228:231], a[100:103], v[28:31]\n\t"
      "v_mfma_f32_16x16x32_bf16 v[28:31], a[232:235], a[104:107], v[28:31]\n\t"
      "ds_read_b64_tr_b16 v[104:105], v44 offset:32768\n\t"
      "ds_read_b64_tr_b16 v[106:107], v44 offset:34816\n\t"
      "v_mfma_f32_16x16x32_bf16 v[28:31], a[236:239], a[108:111], v[28:31]\n\t"
      "ds_read_b64_tr_b16 v[108:109], v44 offset:36864\n\t"
      "ds_read_b64_tr_b16 v[110:111], v44 offset:38912\n\t"
      "v_mfma_f32_16x16x32_bf16 v[28:31], a[240:243], a[112:115], v[28:31]\n\t"
      "ds_read_b64_tr_b16 v[112:113], v44 offset:40960\n\t"
      "ds_read_b64_tr_b16 v[114:115], v44 offset:43008\n\t"
      "v_mfma_f32_16x16x32_bf16 v[28:31], a[244:247], a[116:119], v[28:31]\n\t"
      "ds_read_b64_tr_b16 v[116:117], v44 offset:45056\n\t"
      "ds_read_b64_tr_b16 v[118:119], v44 offset:47104\n\t"
      "v_mfma_f32_16x16x32_bf16 v[28:31], a[248:251], a[120:123], v[28:31]\n\t"
      "ds_read_b64_tr_b16 v[120:121], v44 offset:49152\n\t"
      "ds_read_b64_tr_b16 v[122:123], v44 offset:51200\n\t"
      "v_mfma_f32_16x16x32_bf16 v[28:31], a[252:255], a[124:127], v[28:31]\n\t"
      "ds_read_b64_tr_b16 v[124:125], v44 offset:53248\n\t"
      "ds_read_b64_tr_b16 v[126:127], v44 offset:55296\n\t"
      "v_mfma_f32_16x16x32_bf16 v[32:35], a[224:227], a[128:131], 0\n\t"
      "ds_read_b64_tr_b16 v[128:129], v44 offset:57344\n\t"
      "ds_read_b64_tr_b16 v[130:131], v44 offset:59392\n\t"
      "v_mfma_f32_16x16x32_bf16 v[32:35], a[228:231], a[132:135], v[32:35]\n\t"
      "ds_read_b64_tr_b16 v[132:133], v44 offset:61440\n\t"
      "ds_read_b64_tr_b16 v[134:135], v44 offset:63488\n\t"
      "v_mfma_f32_16x16x32_bf16 v[32:35], a[232:235], a[136:139], v[32:35]\n\t"
      "s_lshl_b32 s92, s100, 7\n\t"
      "s_add_u32 s52, s97, s92\n\t"
      "v_mfma_f32_16x16x32_bf16 v[32:35], a[236:239], a[140:143], v[32:35]\n\t"
      "s_addc_u32 s53, s98, 0\n\t"
      "s_add_u32 m0, s89, 131072\n\t"
      "v_mfma_f32_16x16x32_bf16 v[32:35], a[240:243], a[144:147], v[32:35]\n\t"
      "buffer_load_dwordx4 v254, s[52:55], s76 offen offset:0 lds\n\t"
      "buffer_load_dwordx4 v254, s[52:55], s77 offen offset:1024 lds\n\t"
      "v_mfma_f32_16x16x32_bf16 v[32:35], a[244:247], a[148:151], v[32:35]\n\t"
      "buffer_load_dwordx4 v254, s[52:55], s78 offen offset:2048 lds\n\t"
      "buffer_load_dwordx4 v254, s[52:55], s79 offen offset:3072 lds\n\t"
      "v_mfma_f32_16x16x32_bf16 v[32:35], a[248:251], a[152:155], v[32:35]\n\t"
      "s_add_u32 m0, s89, 135168\n\t"
      "s_nop 0\n\t"
      "buffer_load_dwordx4 v254, s[52:55], s80 offen offset:0 lds\n\t"
      "v_mfma_f32_16x16x32_bf16 v[32:35], a[252:255], a[156:159], v[32:35]\n\t"
      "v_mul_f32 v22, v22, v73\n\t"
      "v_mul_f32 v23, v23, v73\n\t"
      "v_mfma_f32_16x16x32_bf16 v[36:39], a[224:227], a[160:163], 0\n\t"
      "v_cvt_pk_bf16_f32 v58, v20, v21\n\t"
      "v_cvt_pk_bf16_f32 v59, v22, v23\n\t"
      "v_mfma_f32_16x16x32_bf16 v[36:39], a[228:231], a[164:167], v[36:39]\n\t"
      "s_lshl_b32 s95, s101, 14\n\t"
      "s_lshl_b32 s96, s88, 10\n\t"
      "v_mfma_f32_16x16x32_bf16 v[36:39], a[232:235], a[168:171], v[36:39]\n\t"
      "buffer_load_dwordx4 v254, s[52:55], s81 offen offset:1024 lds\n\t"
      "s_add_u32 s95, s95, s96\n\t"
      "v_mfma_f32_16x16x32_bf16 v[36:39], a[236:239], a[172:175], v[36:39]\n\t"
      "s_add_u32 s96, s95, 0\n\t"
      "buffer_store_dwordx4 v[56:59], v47, s[56:59], s96 offen offset:0 sc1\n\t"
      "v_mfma_f32_16x16x32_bf16 v[36:39], a[240:243], a[176:179], v[36:39]\n\t"
      "v_mul_f32 v24, v24, v74\n\t"
      "v_mul_f32 v25, v25, v74\n\t"
      "v_mfma_f32_16x16x32_bf16 v[36:39], a[244:247], a[180:183], v[36:39]\n\t"
      "v_mul_f32 v26, v26, v74\n\t"
      "v_mul_f32 v27, v27, v74\n\t"
      "v_mfma_f32_16x16x32_bf16 v[36:39], a[248:251], a[184:187], v[36:39]\n\t"
      "buffer_load_dwordx4 v254, s[52:55], s82 offen offset:2048 lds\n\t"
      "v_cvt_pk_bf16_f32 v60, v24, v25\n\t"
      "v_mfma_f32_16x16x32_bf16 v[36:39], a[252:255], a[188:191], v[36:39]\n\t"
      "v_cvt_pk_bf16_f32 v61, v26, v27\n\t"
      "v_mul_f32 v28, v28, v75\n\t"
      "v_mfma_f32_16x16x32_bf16 v[40:43], a[224:227], a[192:195], 0\n\t"
      "v_mul_f32 v29, v29, v75\n\t"
      "v_mul_f32 v30, v30, v75\n\t"
      "v_mfma_f32_16x16x32_bf16 v[40:43], a[228:231], a[196:199], v[40:43]\n\t"
      "v_mul_f32 v31, v31, v75\n\t"
      "v_cvt_pk_bf16_f32 v62, v28, v29\n\t"
      "v_mfma_f32_16x16x32_bf16 v[40:43], a[232:235], a[200:203], v[40:43]\n\t"
      "buffer_load_dwordx4 v254, s[52:55], s83 offen offset:3072 lds\n\t"
      "s_add_u32 s100, s100, 1\n\t"
      "v_mfma_f32_16x16x32_bf16 v[40:43], a[236:239], a[204:207], v[40:43]\n\t"
      "s_cmp_eq_u32 s100, 7\n\t"
      "s_cselect_b32 s100, 0, s100\n\t"
      "v_mfma_f32_16x16x32_bf16 v[40:43], a[240:243], a[208:211], v[40:43]\n\t"
      "v_cvt_pk_bf16_f32 v63, v30, v31\n\t"
      "s_add_u32 s96, s95, 4096\n\t"
      "v_mfma_f32_16x16x32_bf16 v[40:43], a[244:247], a[212:215], v[40:43]\n\t"
      "buffer_store_dwordx4 v[60:63], v47, s[56:59], s96 offen offset:0 sc1\n\t"
      "v_mul_f32 v32, v32, v76\n\t"
      "v_mfma_f32_16x16x32_bf16 v[40:43], a[248:251], a[216:219], v[40:43]\n\t"
      "v_mul_f32 v33, v33, v76\n\t"
      "v_mul_f32 v34, v34, v76\n\t"
      "v_mfma_f32_16x16x32_bf16 v[40:43], a[252:255], a[220:223], v[40:43]\n\t"
      "v_mul_f32 v35, v35, v76\n\t"
      "v_cvt_pk_bf16_f32 v56, v32, v33\n\t"
      "v_cvt_pk_bf16_f32 v57, v34, v35\n\t"
      "v_mul_f32 v36, v36, v77\n\t"
      "v_mul_f32 v37, v37, v77\n\t"
      "v_mul_f32 v38, v38, v77\n\t"
      "v_mul_f32 v39, v39, v77\n\t"
      "v_cvt_pk_bf16_f32 v58, v36, v37\n\t"
      "v_cvt_pk_bf16_f32 v59, v38, v39\n\t"
      "s_add_u32 s96, s95, 8192\n\t"
      "buffer_store_dwordx4 v[56:59], v47, s[56:59], s96 offen offset:0 sc1\n\t"
      "s_waitcnt lgkmcnt(14)\n\t"
      "v_mfma_f32_16x16x32_bf16 v[16:19], v[104:107], a[0:3], 0\n\t"
      "s_waitcnt lgkmcnt(12)\n\t"
      "v_mfma_f32_16x16x32_bf16 v[16:19], v[108:111], a[4:7], v[16:19]\n\t"
      "s_waitcnt lgkmcnt(10)\n\t"
      "v_mfma_f32_16x16x32_bf16 v[16:19], v[112:115], a[8:11], v[16:19]\n\t"
      "v_mul_f32 v40, v40, v78\n\t"
      "v_mul_f32 v41, v41, v78\n\t"
      "s_waitcnt lgkmcnt(8)\n\t"
      "v_mfma_f32_16x16x32_bf16 v[16:19], v[116:119], a[12:15], v[16:19]\n\t"
      "v_mul_f32 v42, v42, v78\n\t"
      "v_mul_f32 v43, v43, v78\n\t"
      "s_waitcnt lgkmcnt(6)\n\t"
      "v_mfma_f32_16x16x32_bf16 v[16:19], v[120:123], a[16:19], v[16:19]\n\t"
      "v_cvt_pk_bf16_f32 v60, v40, v41\n\t"
      "v_cvt_pk_bf16_f32 v61, v42, v43\n\t"
      "s_waitcnt lgkmcnt(4)\n\t"
      "v_mfma_f32_16x16x32_bf16 v[16:19], v[124:127], a[20:23], v[16:19]\n\t"
      "s_add_u32 s96, s95, 12288\n\t"
      "buffer_store_dwordx2 v[60:61], v48, s[56:59], s96 offen offset:0 sc1\n\t"
      "s_waitcnt lgkmcnt(2)\n\t"
      "v_mfma_f32_16x16x32_bf16 v[16:19], v[128:131], a[24:27], v[16:19]\n\t"
      "s_add_u32 s101, s101, 1\n\t"
      "s_cmp_eq_u32 s101, 7\n\t"
      "s_waitcnt lgkmcnt(0)\n\t"
      "v_mfma_f32_16x16x32_bf16 v[16:19], v[132:135], a[28:31], v[16:19]\n\t"
      "s_cselect_b32 s101, 0, s101\n\t"
      "v_mfma_f32_16x16x32_bf16 v[20:23], v[104:107], a[32:35], 0\n\t"
      "v_mfma_f32_16x16x32_bf16 v[20:23], v[108:111], a[36:39], v[20:23]\n\t"
      "v_mfma_f32_16x16x32_bf16 v[20:23], v[112:115], a[40:43], v[20:23]\n\t"
      "v_mfma_f32_16x16x32_bf16 v[20:23], v[116:119], a[44:47], v[20:23]\n\t"
      "s_nop 2\n\t"
      "v_mul_f32 v16, v16, v72\n\t"
      "v_mul_f32 v17, v17, v72\n\t"
      "v_mfma_f32_16x16x32_bf16 v[20:23], v[120:123], a[48:51], v[20:23]\n\t"
      "v_mul_f32 v18, v18, v72\n\t"
      "v_mul_f32 v19, v19, v72\n\t"
      "v_mfma_f32_16x16x32_bf16 v[20:23], v[124:127], a[52:55], v[20:23]\n\t"
      "v_cvt_pk_bf16_f32 v56, v16, v17\n\t"
      "v_cvt_pk_bf16_f32 v57, v18, v19\n\t"
      "v_mfma_f32_16x16x32_bf16 v[20:23], v[128:131], a[56:59], v[20:23]\n\t"
      "v_mfma_f32_16x16x32_bf16 v[20:23], v[132:135], a[60:63], v[20:23]\n\t"
      "v_mfma_f32_16x16x32_bf16 v[24:27], v[104:107], a[64:67], 0\n\t"
      "v_mfma_f32_16x16x32_bf16 v[24:27], v[108:111], a[68:71], v[24:27]\n\t"
      "v_mfma_f32_16x16x32_bf16 v[24:27], v[112:115], a[72:75], v[24:27]\n\t"
      "v_mfma_f32_16x16x32_bf16 v[24:27], v[116:119], a[76:79], v[24:27]\n\t"
      "s_nop 3\n\t"
      "v_mul_f32 v20, v20, v73\n\t"
      "v_mul_f32 v21, v21, v73\n\t"
      "v_mfma_f32_16x16x32_bf16 v[24:27], v[120:123], a[80:83], v[24:27]\n\t"
      "s_waitcnt vmcnt(15)\n\t"
      "s_waitcnt vmcnt(13)\n\t"
      "v_mfma_f32_16x16x32_bf16 v[24:27], v[124:127], a[84:87], v[24:27]\n\t"
      "s_barrier\n\t"
      "s_lshl_b32 s3, s93, 6\n\t"
      "s_lshl_b32 s2, s7, 2\n\t"
      "s_add_u32 s3, s3, s2\n\t"
      "v_mov_b32 v87, s3\n\t"
      "s_mov_b32 s46, s28\n\t"
      "buffer_store_dword v86, v87, s[44:47], 0 offen offset:0 sc1\n\t"
      "s_add_u32 s93, s93, 1\n\t"
      "s_cmp_eq_u32 s93, 7\n\t"
      "s_cselect_b32 s93, 0, s93\n\t"
      "v_mfma_f32_16x16x32_bf16 v[24:27], v[128:131], a[88:91], v[24:27]\n\t"
      "v_mfma_f32_16x16x32_bf16 v[24:27], v[132:135], a[92:95], v[24:27]\n\t"
      "v_mfma_f32_16x16x32_bf16 v[28:31], v[104:107], a[96:99], 0\n\t"
      "v_mfma_f32_16x16x32_bf16 v[28:31], v[108:111], a[100:103], v[28:31]\n\t"
      "v_mfma_f32_16x16x32_bf16 v[28:31], v[112:115], a[104:107], v[28:31]\n\t"
      "ds_read_b64_tr_b16 a[224:225], v44 offset:0\n\t"
      "ds_read_b64_tr_b16 a[226:227], v44 offset:2048\n\t"
      "v_mfma_f32_16x16x32_bf16 v[28:31], v[116:119], a[108:111], v[28:31]\n\t"
      "ds_read_b64_tr_b16 a[228:229], v44 offset:4096\n\t"
      "ds_read_b64_tr_b16 a[230:231], v44 offset:6144\n\t"
      "v_mfma_f32_16x16x32_bf16 v[28:31], v[120:123], a[112:115], v[28:31]\n\t"
      "ds_read_b64_tr_b16 a[232:233], v44 offset:8192\n\t"
      "ds_read_b64_tr_b16 a[234:235], v44 offset:10240\n\t"
      "v_mfma_f32_16x16x32_bf16 v[28:31], v[124:127], a[116:119], v[28:31]\n\t"
      "ds_read_b64_tr_b16 a[236:237], v44 offset:12288\n\t"
      "ds_read_b64_tr_b16 a[238:239], v44 offset:14336\n\t"
      "v_mfma_f32_16x16x32_bf16 v[28:31], v[128:131], a[120:123], v[28:31]\n\t"
      "ds_read_b64_tr_b16 a[240:241], v44 offset:16384\n\t"
      "ds_read_b64_tr_b16 a[242:243], v44 offset:18432\n\t"
      "v_mfma_f32_16x16x32_bf16 v[28:31], v[132:135], a[124:127], v[28:31]\n\t"
      "ds_read_b64_tr_b16 a[244:245], v44 offset:20480\n\t"
      "ds_read_b64_tr_b16 a[246:247], v44 offset:22528\n\t"
      "v_mfma_f32_16x16x32_bf16 v[32:35], v[104:107], a[128:131], 0\n\t"
      "ds_read_b64_tr_b16 a[248:249], v44 offset:24576\n\t"
      "ds_read_b64_tr_b16 a[250:251], v44 offset:26624\n\t"
      "v_mfma_f32_16x16x32_bf16 v[32:35], v[108:111], a[132:135], v[32:35]\n\t"
      "ds_read_b64_tr_b16 a[252:253], v44 offset:28672\n\t"
      "ds_read_b64_tr_b16 a[254:255], v44 offset:30720\n\t"
      "v_mfma_f32_16x16x32_bf16 v[32:35], v[112:115], a[136:139], v[32:35]\n\t"
      "s_lshl_b32 s92, s100, 7\n\t"
      "s_add_u32 s52, s97, s92\n\t"
      "v_mfma_f32_16x16x32_bf16 v[32:35], v[116:119], a[140:143], v[32:35]\n\t"
      "s_addc_u32 s53, s98, 0\n\t"
      "s_add_u32 m0, s89, 32768\n\t"
      "v_mfma_f32_16x16x32_bf16 v[32:35], v[120:123], a[144:147], v[32:35]\n\t"
      "buffer_load_dwordx4 v254, s[52:55], s76 offen offset:0 lds\n\t"
      "buffer_load_dwordx4 v254, s[52:55], s77 offen offset:1024 lds\n\t"
      "v_mfma_f32_16x16x32_bf16 v[32:35], v[124:127], a[148:151], v[32:35]\n\t"
      "buffer_load_dwordx4 v254, s[52:55], s78 offen offset:2048 lds\n\t"
      "buffer_load_dwordx4 v254, s[52:55], s79 offen offset:3072 lds\n\t"
      "v_mfma_f32_16x16x32_bf16 v[32:35], v[128:131], a[152:155], v[32:35]\n\t"
      "s_add_u32 m0, s89, 36864\n\t"
      "s_nop 0\n\t"
      "buffer_load_dwordx4 v254, s[52:55], s80 offen offset:0 lds\n\t"
      "v_mfma_f32_16x16x32_bf16 v[32:35], v[132:135], a[156:159], v[32:35]\n\t"
      "v_mul_f32 v22, v22, v73\n\t"
      "v_mul_f32 v23, v23, v73\n\t"
      "v_mfma_f32_16x16x32_bf16 v[36:39], v[104:107], a[160:163], 0\n\t"
      "v_cvt_pk_bf16_f32 v58, v20, v21\n\t"
      "v_cvt_pk_bf16_f32 v59, v22, v23\n\t"
      "v_mfma_f32_16x16x32_bf16 v[36:39], v[108:111], a[164:167], v[36:39]\n\t"
      "s_lshl_b32 s95, s101, 14\n\t"
      "s_lshl_b32 s96, s88, 10\n\t"
      "v_mfma_f32_16x16x32_bf16 v[36:39], v[112:115], a[168:171], v[36:39]\n\t"
      "buffer_load_dwordx4 v254, s[52:55], s81 offen offset:1024 lds\n\t"
      "s_add_u32 s95, s95, s96\n\t"
      "v_mfma_f32_16x16x32_bf16 v[36:39], v[116:119], a[172:175], v[36:39]\n\t"
      "s_add_u32 s96, s95, 0\n\t"
      "buffer_store_dwordx4 v[56:59], v47, s[56:59], s96 offen offset:0 sc1\n\t"
      "v_mfma_f32_16x16x32_bf16 v[36:39], v[120:123], a[176:179], v[36:39]\n\t"
      "v_mul_f32 v24, v24, v74\n\t"
      "v_mul_f32 v25, v25, v74\n\t"
      "v_mfma_f32_16x16x32_bf16 v[36:39], v[124:127], a[180:183], v[36:39]\n\t"
      "v_mul_f32 v26, v26, v74\n\t"
      "v_mul_f32 v27, v27, v74\n\t"
      "v_mfma_f32_16x16x32_bf16 v[36:39], v[128:131], a[184:187], v[36:39]\n\t"
      "buffer_load_dwordx4 v254, s[52:55], s82 offen offset:2048 lds\n\t"
      "v_cvt_pk_bf16_f32 v60, v24, v25\n\t"
      "v_mfma_f32_16x16x32_bf16 v[36:39], v[132:135], a[188:191], v[36:39]\n\t"
      "v_cvt_pk_bf16_f32 v61, v26, v27\n\t"
      "v_mul_f32 v28, v28, v75\n\t"
      "v_mfma_f32_16x16x32_bf16 v[40:43], v[104:107], a[192:195], 0\n\t"
      "v_mul_f32 v29, v29, v75\n\t"
      "v_mul_f32 v30, v30, v75\n\t"
      "v_mfma_f32_16x16x32_bf16 v[40:43], v[108:111], a[196:199], v[40:43]\n\t"
      "v_mul_f32 v31, v31, v75\n\t"
      "v_cvt_pk_bf16_f32 v62, v28, v29\n\t"
      "v_mfma_f32_16x16x32_bf16 v[40:43], v[112:115], a[200:203], v[40:43]\n\t"
      "buffer_load_dwordx4 v254, s[52:55], s83 offen offset:3072 lds\n\t"
      "s_add_u32 s100, s100, 1\n\t"
      "v_mfma_f32_16x16x32_bf16 v[40:43], v[116:119], a[204:207], v[40:43]\n\t"
      "s_cmp_eq_u32 s100, 7\n\t"
      "s_cselect_b32 s100, 0, s100\n\t"
      "v_mfma_f32_16x16x32_bf16 v[40:43], v[120:123], a[208:211], v[40:43]\n\t"
      "v_cvt_pk_bf16_f32 v63, v30, v31\n\t"
      "s_add_u32 s96, s95, 4096\n\t"
      "v_mfma_f32_16x16x32_bf16 v[40:43], v[124:127], a[212:215], v[40:43]\n\t"
      "buffer_store_dwordx4 v[60:63], v47, s[56:59], s96 offen offset:0 sc1\n\t"
      "v_mul_f32 v32, v32, v76\n\t"
      "v_mfma_f32_16x16x32_bf16 v[40:43], v[128:131], a[216:219], v[40:43]\n\t"
      "v_mul_f32 v33, v33, v76\n\t"
      "v_mul_f32 v34, v34, v76\n\t"
      "v_mfma_f32_16x16x32_bf16 v[40:43], v[132:135], a[220:223], v[40:43]\n\t"
      "v_mul_f32 v35, v35, v76\n\t"
      "v_cvt_pk_bf16_f32 v56, v32, v33\n\t"
      "v_cvt_pk_bf16_f32 v57, v34, v35\n\t"
      "v_mul_f32 v36, v36, v77\n\t"
      "v_mul_f32 v37, v37, v77\n\t"
      "v_mul_f32 v38, v38, v77\n\t"
      "v_mul_f32 v39, v39, v77\n\t"
      "v_cvt_pk_bf16_f32 v58, v36, v37\n\t"
      "v_cvt_pk_bf16_f32 v59, v38, v39\n\t"
      "s_add_u32 s96, s95, 8192\n\t"
      "buffer_store_dwordx4 v[56:59], v47, s[56:59], s96 offen offset:0 sc1\n\t"
      "s_waitcnt lgkmcnt(14)\n\t"
      "v_mfma_f32_16x16x32_bf16 v[16:19], a[224:227], a[0:3], 0\n\t"
      "s_waitcnt lgkmcnt(12)\n\t"
      "v_mfma_f32_16x16x32_bf16 v[16:19], a[228:231], a[4:7], v[16:19]\n\t"
      "s_waitcnt lgkmcnt(10)\n\t"
      "v_mfma_f32_16x16x32_bf16 v[16:19], a[232:235], a[8:11], v[16:19]\n\t"
      "v_mul_f32 v40, v40, v78\n\t"
      "v_mul_f32 v41, v41, v78\n\t"
      "s_waitcnt lgkmcnt(8)\n\t"
      "v_mfma_f32_16x16x32_bf16 v[16:19], a[236:239], a[12:15], v[16:19]\n\t"
      "v_mul_f32 v42, v42, v78\n\t"
      "v_mul_f32 v43, v43, v78\n\t"
      "s_waitcnt lgkmcnt(6)\n\t"
      "v_mfma_f32_16x16x32_bf16 v[16:19], a[240:243], a[16:19], v[16:19]\n\t"
      "v_cvt_pk_bf16_f32 v60, v40, v41\n\t"
      "v_cvt_pk_bf16_f32 v61, v42, v43\n\t"
      "s_waitcnt lgkmcnt(4)\n\t"
      "v_mfma_f32_16x16x32_bf16 v[16:19], a[244:247], a[20:23], v[16:19]\n\t"
      "s_add_u32 s96, s95, 12288\n\t"
      "buffer_store_dwordx2 v[60:61], v48, s[56:59], s96 offen offset:0 sc1\n\t"
      "s_waitcnt lgkmcnt(2)\n\t"
      "v_mfma_f32_16x16x32_bf16 v[16:19], a[248:251], a[24:27], v[16:19]\n\t"
      "s_add_u32 s101, s101, 1\n\t"
      "s_cmp_eq_u32 s101, 7\n\t"
      "s_waitcnt lgkmcnt(0)\n\t"
      "v_mfma_f32_16x16x32_bf16 v[16:19], a[252:255], a[28:31], v[16:19]\n\t"
      "s_cselect_b32 s101, 0, s101\n\t"
      "v_mfma_f32_16x16x32_bf16 v[20:23], a[224:227], a[32:35], 0\n\t"
      "v_mfma_f32_16x16x32_bf16 v[20:23], a[228:231], a[36:39], v[20:23]\n\t"
      "v_mfma_f32_16x16x32_bf16 v[20:23], a[232:235], a[40:43], v[20:23]\n\t"
      "v_mfma_f32_16x16x32_bf16 v[20:23], a[236:239], a[44:47], v[20:23]\n\t"
      "s_nop 2\n\t"
      "v_mul_f32 v16, v16, v72\n\t"
      "v_mul_f32 v17, v17, v72\n\t"
      "v_mfma_f32_16x16x32_bf16 v[20:23], a[240:243], a[48:51], v[20:23]\n\t"
      "v_mul_f32 v18, v18, v72\n\t"
      "v_mul_f32 v19, v19, v72\n\t"
      "v_mfma_f32_16x16x32_bf16 v[20:23], a[244:247], a[52:55], v[20:23]\n\t"
      "v_cvt_pk_bf16_f32 v56, v16, v17\n\t"
      "v_cvt_pk_bf16_f32 v57, v18, v19\n\t"
      "v_mfma_f32_16x16x32_bf16 v[20:23], a[248:251], a[56:59], v[20:23]\n\t"
      "v_mfma_f32_16x16x32_bf16 v[20:23], a[252:255], a[60:63], v[20:23]\n\t"
      "v_mfma_f32_16x16x32_bf16 v[24:27], a[224:227], a[64:67], 0\n\t"
      "v_mfma_f32_16x16x32_bf16 v[24:27], a[228:231], a[68:71], v[24:27]\n\t"
      "v_mfma_f32_16x16x32_bf16 v[24:27], a[232:235], a[72:75], v[24:27]\n\t"
      "v_mfma_f32_16x16x32_bf16 v[24:27], a[236:239], a[76:79], v[24:27]\n\t"
      "s_nop 3\n\t"
      "v_mul_f32 v20, v20, v73\n\t"
      "v_mul_f32 v21, v21, v73\n\t"
      "v_mfma_f32_16x16x32_bf16 v[24:27], a[240:243], a[80:83], v[24:27]\n\t"
      "s_waitcnt vmcnt(16)\n\t"
      "s_waitcnt vmcnt(13)\n\t"
      "v_mfma_f32_16x16x32_bf16 v[24:27], a[244:247], a[84:87], v[24:27]\n\t"
      "s_barrier\n\t"
      "s_lshl_b32 s3, s93, 6\n\t"
      "s_lshl_b32 s2, s7, 2\n\t"
      "s_add_u32 s3, s3, s2\n\t"
      "v_mov_b32 v87, s3\n\t"
      "s_mov_b32 s46, s28\n\t"
      "buffer_store_dword v86, v87, s[44:47], 0 offen offset:0 sc1\n\t"
      "s_add_u32 s93, s93, 1\n\t"
      "s_cmp_eq_u32 s93, 7\n\t"
      "s_cselect_b32 s93, 0, s93\n\t"
      "v_mfma_f32_16x16x32_bf16 v[24:27], a[248:251], a[88:91], v[24:27]\n\t"
      "v_mfma_f32_16x16x32_bf16 v[24:27], a[252:255], a[92:95], v[24:27]\n\t"
      "v_mfma_f32_16x16x32_bf16 v[28:31], a[224:227], a[96:99], 0\n\t"
      "v_mfma_f32_16x16x32_bf16 v[28:31], a[228:231], a[100:103], v[28:31]\n\t"
      "v_mfma_f32_16x16x32_bf16 v[28:31], a[232:235], a[104:107], v[28:31]\n\t"
      "ds_read_b64_tr_b16 v[104:105], v45 offset:0\n\t"
      "ds_read_b64_tr_b16 v[106:107], v45 offset:2048\n\t"
      "v_mfma_f32_16x16x32_bf16 v[28:31], a[236:239], a[108:111], v[28:31]\n\t"
      "ds_read_b64_tr_b16 v[108:109], v45 offset:4096\n\t"
      "ds_read_b64_tr_b16 v[110:111], v45 offset:6144\n\t"
      "v_mfma_f32_16x16x32_bf16 v[28:31], a[240:243], a[112:115], v[28:31]\n\t"
      "ds_read_b64_tr_b16 v[112:113], v45 offset:8192\n\t"
      "ds_read_b64_tr_b16 v[114:115], v45 offset:10240\n\t"
      "v_mfma_f32_16x16x32_bf16 v[28:31], a[244:247], a[116:119], v[28:31]\n\t"
      "ds_read_b64_tr_b16 v[116:117], v45 offset:12288\n\t"
      "ds_read_b64_tr_b16 v[118:119], v45 offset:14336\n\t"
      "v_mfma_f32_16x16x32_bf16 v[28:31], a[248:251], a[120:123], v[28:31]\n\t"
      "ds_read_b64_tr_b16 v[120:121], v45 offset:16384\n\t"
      "ds_read_b64_tr_b16 v[122:123], v45 offset:18432\n\t"
      "v_mfma_f32_16x16x32_bf16 v[28:31], a[252:255], a[124:127], v[28:31]\n\t"
      "ds_read_b64_tr_b16 v[124:125], v45 offset:20480\n\t"
      "ds_read_b64_tr_b16 v[126:127], v45 offset:22528\n\t"
      "v_mfma_f32_16x16x32_bf16 v[32:35], a[224:227], a[128:131], 0\n\t"
      "ds_read_b64_tr_b16 v[128:129], v45 offset:24576\n\t"
      "ds_read_b64_tr_b16 v[130:131], v45 offset:26624\n\t"
      "v_mfma_f32_16x16x32_bf16 v[32:35], a[228:231], a[132:135], v[32:35]\n\t"
      "ds_read_b64_tr_b16 v[132:133], v45 offset:28672\n\t"
      "ds_read_b64_tr_b16 v[134:135], v45 offset:30720\n\t"
      "v_mfma_f32_16x16x32_bf16 v[32:35], a[232:235], a[136:139], v[32:35]\n\t"
      "v_mul_f32 v22, v22, v73\n\t"
      "v_mul_f32 v23, v23, v73\n\t"
      "v_mfma_f32_16x16x32_bf16 v[32:35], a[236:239], a[140:143], v[32:35]\n\t"
      "v_cvt_pk_bf16_f32 v58, v20, v21\n\t"
      "v_cvt_pk_bf16_f32 v59, v22, v23\n\t"
      "v_mfma_f32_16x16x32_bf16 v[32:35], a[240:243], a[144:147], v[32:35]\n\t"
      "s_lshl_b32 s95, s101, 14\n\t"
      "s_lshl_b32 s96, s88, 10\n\t"
      "v_mfma_f32_16x16x32_bf16 v[32:35], a[244:247], a[148:151], v[32:35]\n\t"
      "s_add_u32 s95, s95, s96\n\t"
      "s_add_u32 s96, s95, 0\n\t"
      "v_mfma_f32_16x16x32_bf16 v[32:35], a[248:251], a[152:155], v[32:35]\n\t"
      "buffer_store_dwordx4 v[56:59], v47, s[56:59], s96 offen offset:0 sc1\n\t"
      "v_mul_f32 v24, v24, v74\n\t"
      "v_mfma_f32_16x16x32_bf16 v[32:35], a[252:255], a[156:159], v[32:35]\n\t"
      "v_mul_f32 v25, v25, v74\n\t"
      "v_mul_f32 v26, v26, v74\n\t"
      "v_mfma_f32_16x16x32_bf16 v[36:39], a[224:227], a[160:163], 0\n\t"
      "v_mul_f32 v27, v27, v74\n\t"
      "v_cvt_pk_bf16_f32 v60, v24, v25\n\t"
      "v_mfma_f32_16x16x32_bf16 v[36:39], a[228:231], a[164:167], v[36:39]\n\t"
      "v_cvt_pk_bf16_f32 v61, v26, v27\n\t"
      "v_mul_f32 v28, v28, v75\n\t"
      "v_mfma_f32_16x16x32_bf16 v[36:39], a[232:235], a[168:171], v[36:39]\n\t"
      "v_mul_f32 v29, v29, v75\n\t"
      "v_mul_f32 v30, v30, v75\n\t"
      "v_mfma_f32_16x16x32_bf16 v[36:39], a[236:239], a[172:175], v[36:39]\n\t"
      "v_mul_f32 v31, v31, v75\n\t"
      "v_cvt_pk_bf16_f32 v62, v28, v29\n\t"
      "v_mfma_f32_16x16x32_bf16 v[36:39], a[240:243], a[176:179], v[36:39]\n\t"
      "v_cvt_pk_bf16_f32 v63, v30, v31\n\t"
      "s_add_u32 s96, s95, 4096\n\t"
      "v_mfma_f32_16x16x32_bf16 v[36:39], a[244:247], a[180:183], v[36:39]\n\t"
      "buffer_store_dwordx4 v[60:63], v47, s[56:59], s96 offen offset:0 sc1\n\t"
      "v_mul_f32 v32, v32, v76\n\t"
      "v_mfma_f32_16x16x32_bf16 v[36:39], a[248:251], a[184:187], v[36:39]\n\t"
      "v_mul_f32 v33, v33, v76\n\t"
      "v_mul_f32 v34, v34, v76\n\t"
      "v_mfma_f32_16x16x32_bf16 v[36:39], a[252:255], a[188:191], v[36:39]\n\t"
      "v_mul_f32 v35, v35, v76\n\t"
      "v_cvt_pk_bf16_f32 v56, v32, v33\n\t"
      "v_mfma_f32_16x16x32_bf16 v[40:43], a[224:227], a[192:195], 0\n\t"
      "v_cvt_pk_bf16_f32 v57, v34, v35\n\t"
      "v_mfma_f32_16x16x32_bf16 v[40:43], a[228:231], a[196:199], v[40:43]\n\t"
      "v_mfma_f32_16x16x32_bf16 v[40:43], a[232:235], a[200:203], v[40:43]\n\t"
      "v_mfma_f32_16x16x32_bf16 v[40:43], a[236:239], a[204:207], v[40:43]\n\t"
      "s_nop 0\n\t"
      "v_mul_f32 v36, v36, v77\n\t"
      "v_mul_f32 v37, v37, v77\n\t"
      "v_mfma_f32_16x16x32_bf16 v[40:43], a[240:243], a[208:211], v[40:43]\n\t"
      "v_mul_f32 v38, v38, v77\n\t"
      "v_mul_f32 v39, v39, v77\n\t"
      "v_mfma_f32_16x16x32_bf16 v[40:43], a[244:247], a[212:215], v[40:43]\n\t"
      "v_cvt_pk_bf16_f32 v58, v36, v37\n\t"
      "v_cvt_pk_bf16_f32 v59, v38, v39\n\t"
      "v_mfma_f32_16x16x32_bf16 v[40:43], a[248:251], a[216:219], v[40:43]\n\t"
      "s_add_u32 s96, s95, 8192\n\t"
      "buffer_store_dwordx4 v[56:59], v47, s[56:59], s96 offen offset:0 sc1\n\t"
      "v_mfma_f32_16x16x32_bf16 v[40:43], a[252:255], a[220:223], v[40:43]\n\t"
      "s_waitcnt lgkmcnt(14)\n\t"
      "v_mfma_f32_16x16x32_bf16 v[16:19], v[104:107], a[0:3], 0\n\t"
      "s_waitcnt lgkmcnt(12)\n\t"
      "v_mfma_f32_16x16x32_bf16 v[16:19], v[108:111], a[4:7], v[16:19]\n\t"
      "s_waitcnt lgkmcnt(10)\n\t"
      "v_mfma_f32_16x16x32_bf16 v[16:19], v[112:115], a[8:11], v[16:19]\n\t"
      "s_nop 1\n\t"
      "v_mul_f32 v40, v40, v78\n\t"
      "v_mul_f32 v41, v41, v78\n\t"
      "s_waitcnt lgkmcnt(8)\n\t"
      "v_mfma_f32_16x16x32_bf16 v[16:19], v[116:119], a[12:15], v[16:19]\n\t"
      "v_mul_f32 v42, v42, v78\n\t"
      "v_mul_f32 v43, v43, v78\n\t"
      "s_waitcnt lgkmcnt(6)\n\t"
      "v_mfma_f32_16x16x32_bf16 v[16:19], v[120:123], a[16:19], v[16:19]\n\t"
      "v_cvt_pk_bf16_f32 v60, v40, v41\n\t"
      "v_cvt_pk_bf16_f32 v61, v42, v43\n\t"
      "s_waitcnt lgkmcnt(4)\n\t"
      "v_mfma_f32_16x16x32_bf16 v[16:19], v[124:127], a[20:23], v[16:19]\n\t"
      "s_add_u32 s96, s95, 12288\n\t"
      "buffer_store_dwordx2 v[60:61], v48, s[56:59], s96 offen offset:0 sc1\n\t"
      "s_waitcnt lgkmcnt(2)\n\t"
      "v_mfma_f32_16x16x32_bf16 v[16:19], v[128:131], a[24:27], v[16:19]\n\t"
      "s_add_u32 s101, s101, 1\n\t"
      "s_cmp_eq_u32 s101, 7\n\t"
      "s_waitcnt lgkmcnt(0)\n\t"
      "v_mfma_f32_16x16x32_bf16 v[16:19], v[132:135], a[28:31], v[16:19]\n\t"
      "s_cselect_b32 s101, 0, s101\n\t"
      "v_mfma_f32_16x16x32_bf16 v[20:23], v[104:107], a[32:35], 0\n\t"
      "v_mfma_f32_16x16x32_bf16 v[20:23], v[108:111], a[36:39], v[20:23]\n\t"
      "v_mfma_f32_16x16x32_bf16 v[20:23], v[112:115], a[40:43], v[20:23]\n\t"
      "v_mfma_f32_16x16x32_bf16 v[20:23], v[116:119], a[44:47], v[20:23]\n\t"
      "s_nop 2\n\t"
      "v_mul_f32 v16, v16, v72\n\t"
      "v_mul_f32 v17, v17, v72\n\t"
      "v_mfma_f32_16x16x32_bf16 v[20:23], v[120:123], a[48:51], v[20:23]\n\t"
      "v_mul_f32 v18, v18, v72\n\t"
      "v_mul_f32 v19, v19, v72\n\t"
      "v_mfma_f32_16x16x32_bf16 v[20:23], v[124:127], a[52:55], v[20:23]\n\t"
      "v_cvt_pk_bf16_f32 v56, v16, v17\n\t"
      "v_cvt_pk_bf16_f32 v57, v18, v19\n\t"
      "v_mfma_f32_16x16x32_bf16 v[20:23], v[128:131], a[56:59], v[20:23]\n\t"
      "v_mfma_f32_16x16x32_bf16 v[20:23], v[132:135], a[60:63], v[20:23]\n\t"
      "v_mfma_f32_16x16x32_bf16 v[24:27], v[104:107], a[64:67], 0\n\t"
      "v_mfma_f32_16x16x32_bf16 v[24:27], v[108:111], a[68:71], v[24:27]\n\t"
      "v_mfma_f32_16x16x32_bf16 v[24:27], v[112:115], a[72:75], v[24:27]\n\t"
      "v_mfma_f32_16x16x32_bf16 v[24:27], v[116:119], a[76:79], v[24:27]\n\t"
      "s_nop 3\n\t"
      "v_mul_f32 v20, v20, v73\n\t"
      "v_mul_f32 v21, v21, v73\n\t"
      "v_mfma_f32_16x16x32_bf16 v[24:27], v[120:123], a[80:83], v[24:27]\n\t"
      "s_waitcnt vmcnt(8)\n\t"
      "s_waitcnt vmcnt(5)\n\t"
      "v_mfma_f32_16x16x32_bf16 v[24:27], v[124:127], a[84:87], v[24:27]\n\t"
      "s_barrier\n\t"
      "s_lshl_b32 s3, s93, 6\n\t"
      "s_lshl_b32 s2, s7, 2\n\t"
      "s_add_u32 s3, s3, s2\n\t"
      "v_mov_b32 v87, s3\n\t"
      "s_mov_b32 s46, s28\n\t"
      "buffer_store_dword v86, v87, s[44:47], 0 offen offset:0 sc1\n\t"
      "s_add_u32 s93, s93, 1\n\t"
      "s_cmp_eq_u32 s93, 7\n\t"
      "s_cselect_b32 s93, 0, s93\n\t"
      "v_mfma_f32_16x16x32_bf16 v[24:27], v[128:131], a[88:91], v[24:27]\n\t"
      "v_mfma_f32_16x16x32_bf16 v[24:27], v[132:135], a[92:95], v[24:27]\n\t"
      "v_mfma_f32_16x16x32_bf16 v[28:31], v[104:107], a[96:99], 0\n\t"
      "v_mfma_f32_16x16x32_bf16 v[28:31], v[108:111], a[100:103], v[28:31]\n\t"
      "v_mfma_f32_16x16x32_bf16 v[28:31], v[112:115], a[104:107], v[28:31]\n\t"
      "ds_read_b64_tr_b16 a[224:225], v44 offset:32768\n\t"
      "ds_read_b64_tr_b16 a[226:227], v44 offset:34816\n\t"
      "v_mfma_f32_16x16x32_bf16 v[28:31], v[116:119], a[108:111], v[28:31]\n\t"
      "ds_read_b64_tr_b16 a[228:229], v44 offset:36864\n\t"
      "ds_read_b64_tr_b16 a[230:231], v44 offset:38912\n\t"
      "v_mfma_f32_16x16x32_bf16 v[28:31], v[120:123], a[112:115], v[28:31]\n\t"
      "ds_read_b64_tr_b16 a[232:233], v44 offset:40960\n\t"
      "ds_read_b64_tr_b16 a[234:235], v44 offset:43008\n\t"
      "v_mfma_f32_16x16x32_bf16 v[28:31], v[124:127], a[116:119], v[28:31]\n\t"
      "ds_read_b64_tr_b16 a[236:237], v44 offset:45056\n\t"
      "ds_read_b64_tr_b16 a[238:239], v44 offset:47104\n\t"
      "v_mfma_f32_16x16x32_bf16 v[28:31], v[128:131], a[120:123], v[28:31]\n\t"
      "ds_read_b64_tr_b16 a[240:241], v44 offset:49152\n\t"
      "ds_read_b64_tr_b16 a[242:243], v44 offset:51200\n\t"
      "v_mfma_f32_16x16x32_bf16 v[28:31], v[132:135], a[124:127], v[28:31]\n\t"
      "ds_read_b64_tr_b16 a[244:245], v44 offset:53248\n\t"
      "ds_read_b64_tr_b16 a[246:247], v44 offset:55296\n\t"
      "v_mfma_f32_16x16x32_bf16 v[32:35], v[104:107], a[128:131], 0\n\t"
      "ds_read_b64_tr_b16 a[248:249], v44 offset:57344\n\t"
      "ds_read_b64_tr_b16 a[250:251], v44 offset:59392\n\t"
      "v_mfma_f32_16x16x32_bf16 v[32:35], v[108:111], a[132:135], v[32:35]\n\t"
      "ds_read_b64_tr_b16 a[252:253], v44 offset:61440\n\t"
      "ds_read_b64_tr_b16 a[254:255], v44 offset:63488\n\t"
      "v_mfma_f32_16x16x32_bf16 v[32:35], v[112:115], a[136:139], v[32:35]\n\t"
      "v_mul_f32 v22, v22, v73\n\t"
      "v_mul_f32 v23, v23, v73\n\t"
      "v_mfma_f32_16x16x32_bf16 v[32:35], v[116:119], a[140:143], v[32:35]\n\t"
      "v_cvt_pk_bf16_f32 v58, v20, v21\n\t"
      "v_cvt_pk_bf16_f32 v59, v22, v23\n\t"
      "v_mfma_f32_16x16x32_bf16 v[32:35], v[120:123], a[144:147], v[32:35]\n\t"
      "s_lshl_b32 s95, s101, 14\n\t"
      "s_lshl_b32 s96, s88, 10\n\t"
      "v_mfma_f32_16x16x32_bf16 v[32:35], v[124:127], a[148:151], v[32:35]\n\t"
      "s_add_u32 s95, s95, s96\n\t"
      "s_add_u32 s96, s95, 0\n\t"
      "v_mfma_f32_16x16x32_bf16 v[32:35], v[128:131], a[152:155], v[32:35]\n\t"
      "buffer_store_dwordx4 v[56:59], v47, s[56:59], s96 offen offset:0 sc1\n\t"
      "v_mul_f32 v24, v24, v74\n\t"
      "v_mfma_f32_16x16x32_bf16 v[32:35], v[132:135], a[156:159], v[32:35]\n\t"
      "v_mul_f32 v25, v25, v74\n\t"
      "v_mul_f32 v26, v26, v74\n\t"
      "v_mfma_f32_16x16x32_bf16 v[36:39], v[104:107], a[160:163], 0\n\t"
      "v_mul_f32 v27, v27, v74\n\t"
      "v_cvt_pk_bf16_f32 v60, v24, v25\n\t"
      "v_mfma_f32_16x16x32_bf16 v[36:39], v[108:111], a[164:167], v[36:39]\n\t"
      "v_cvt_pk_bf16_f32 v61, v26, v27\n\t"
      "v_mul_f32 v28, v28, v75\n\t"
      "v_mfma_f32_16x16x32_bf16 v[36:39], v[112:115], a[168:171], v[36:39]\n\t"
      "v_mul_f32 v29, v29, v75\n\t"
      "v_mul_f32 v30, v30, v75\n\t"
      "v_mfma_f32_16x16x32_bf16 v[36:39], v[116:119], a[172:175], v[36:39]\n\t"
      "v_mul_f32 v31, v31, v75\n\t"
      "v_cvt_pk_bf16_f32 v62, v28, v29\n\t"
      "v_mfma_f32_16x16x32_bf16 v[36:39], v[120:123], a[176:179], v[36:39]\n\t"
      "v_cvt_pk_bf16_f32 v63, v30, v31\n\t"
      "s_add_u32 s96, s95, 4096\n\t"
      "v_mfma_f32_16x16x32_bf16 v[36:39], v[124:127], a[180:183], v[36:39]\n\t"
      "buffer_store_dwordx4 v[60:63], v47, s[56:59], s96 offen offset:0 sc1\n\t"
      "v_mul_f32 v32, v32, v76\n\t"
      "v_mfma_f32_16x16x32_bf16 v[36:39], v[128:131], a[184:187], v[36:39]\n\t"
      "v_mul_f32 v33, v33, v76\n\t"
      "v_mul_f32 v34, v34, v76\n\t"
      "v_mfma_f32_16x16x32_bf16 v[36:39], v[132:135], a[188:191], v[36:39]\n\t"
      "v_mul_f32 v35, v35, v76\n\t"
      "v_cvt_pk_bf16_f32 v56, v32, v33\n\t"
      "v_mfma_f32_16x16x32_bf16 v[40:43], v[104:107], a[192:195], 0\n\t"
      "v_cvt_pk_bf16_f32 v57, v34, v35\n\t"
      "s_mov_b32 s48, s18\n\t"
      "v_mfma_f32_16x16x32_bf16 v[40:43], v[108:111], a[196:199], v[40:43]\n\t"
      "s_and_b32 s49, s19, 0xffff\n\t"
      "s_mov_b32 s50, 1835008\n\t"
      "v_mfma_f32_16x16x32_bf16 v[40:43], v[112:115], a[200:203], v[40:43]\n\t"
      "s_mov_b32 s51, 0x20000\n\t"
      "s_mov_b32 s84, s20\n\t"
      "v_mfma_f32_16x16x32_bf16 v[40:43], v[116:119], a[204:207], v[40:43]\n\t"
      "v_mul_f32 v36, v36, v77\n\t"
      "v_mul_f32 v37, v37, v77\n\t"
      "v_mfma_f32_16x16x32_bf16 v[40:43], v[120:123], a[208:211], v[40:43]\n\t"
      "v_mul_f32 v38, v38, v77\n\t"
      "v_mul_f32 v39, v39, v77\n\t"
      "v_mfma_f32_16x16x32_bf16 v[40:43], v[124:127], a[212:215], v[40:43]\n\t"
      "v_cvt_pk_bf16_f32 v58, v36, v37\n\t"
      "v_cvt_pk_bf16_f32 v59, v38, v39\n\t"
      "v_mfma_f32_16x16x32_bf16 v[40:43], v[128:131], a[216:219], v[40:43]\n\t"
      "s_add_u32 s96, s95, 8192\n\t"
      "buffer_store_dwordx4 v[56:59], v47, s[56:59], s96 offen offset:0 sc1\n\t"
      "v_mfma_f32_16x16x32_bf16 v[40:43], v[132:135], a[220:223], v[40:43]\n\t"
      "s_and_b32 s85, s21, 0xffff\n\t"
      "s_mov_b32 s86, 100352\n\t"
      "s_mov_b32 s87, 0x20000\n\t"
      "v_mbcnt_lo_u32_b32 v0, -1, 0\n\t"
      "v_mbcnt_hi_u32_b32 v0, -1, v0\n\t"
      "v_and_b32 v1, 15, v0\n\t"
      "v_lshrrev_b32 v2, 4, v0\n\t"
      "s_lshr_b32 s64, s7, 2\n\t"
      "s_mul_i32 s64, s64, 28672\n\t"
      "s_and_b32 s65, s7, 3\n\t"
      "s_lshl_b32 s65, s65, 5\n\t"
      "s_add_u32 s64, s64, s65\n\t"
      "v_mul_u32_u24 v3, 896, v1\n\t"
      "v_lshlrev_b32 v2, 3, v2\n\t"
      "v_add_u32 v3, v3, v2\n\t"
      "v_add_u32 v64, s64, v3\n\t"
      "s_cmp_lt_u32 s7, 12\n\t"
      "s_cselect_b32 s64, 14336, 0x40000000\n\t"
      "v_add_u32 v65, s64, v64\n\t"
      "v_and_b32 v66, 15, v0\n\t"
      "v_lshlrev_b32 v66, 2, v66\n\t"
      "s_add_u32 s62, s88, s6\n\t"
      "s_cmp_ge_u32 s62, 7\n\t"
      "s_cselect_b32 s64, 7, 0\n\t"
      "s_sub_u32 s62, s62, s64\n\t"
      "s_cmp_ge_u32 s62, 7\n\t"
      "s_cselect_b32 s64, 7, 0\n\t"
      "s_sub_u32 s62, s62, s64\n\t"
      "s_add_u32 s63, s88, s6\n\t"
      "s_add_u32 s63, s63, 4\n\t"
      "s_cmp_ge_u32 s63, 7\n\t"
      "s_cselect_b32 s64, 7, 0\n\t"
      "s_sub_u32 s63, s63, s64\n\t"
      "s_cmp_ge_u32 s63, 7\n\t"
      "s_cselect_b32 s64, 7, 0\n\t"
      "s_sub_u32 s63, s63, s64\n\t"
      "s_cmp_lt_u32 s7, 12\n\t"
      "s_cselect_b32 s64, 4, 3\n\t"
      "v_lshlrev_b32 v68, s64, v0\n\t"
      "s_waitcnt lgkmcnt(14)\n\t"
      "v_mfma_f32_16x16x32_bf16 v[16:19], a[224:227], a[0:3], 0\n\t"
      "s_waitcnt lgkmcnt(12)\n\t"
      "v_mfma_f32_16x16x32_bf16 v[16:19], a[228:231], a[4:7], v[16:19]\n\t"
      "s_waitcnt lgkmcnt(10)\n\t"
      "v_mfma_f32_16x16x32_bf16 v[16:19], a[232:235], a[8:11], v[16:19]\n\t"
      "v_mul_f32 v40, v40, v78\n\t"
      "v_mul_f32 v41, v41, v78\n\t"
      "s_waitcnt lgkmcnt(8)\n\t"
      "v_mfma_f32_16x16x32_bf16 v[16:19], a[236:239], a[12:15], v[16:19]\n\t"
      "v_mul_f32 v42, v42, v78\n\t"
      "v_mul_f32 v43, v43, v78\n\t"
      "s_waitcnt lgkmcnt(6)\n\t"
      "v_mfma_f32_16x16x32_bf16 v[16:19], a[240:243], a[16:19], v[16:19]\n\t"
      "v_cvt_pk_bf16_f32 v60, v40, v41\n\t"
      "v_cvt_pk_bf16_f32 v61, v42, v43\n\t"
      "s_waitcnt lgkmcnt(4)\n\t"
      "v_mfma_f32_16x16x32_bf16 v[16:19], a[244:247], a[20:23], v[16:19]\n\t"
      "s_add_u32 s96, s95, 12288\n\t"
      "buffer_store_dwordx2 v[60:61], v48, s[56:59], s96 offen offset:0 sc1\n\t"
      "s_waitcnt lgkmcnt(2)\n\t"
      "v_mfma_f32_16x16x32_bf16 v[16:19], a[248:251], a[24:27], v[16:19]\n\t"
      "s_add_u32 s101, s101, 1\n\t"
      "s_cmp_eq_u32 s101, 7\n\t"
      "s_waitcnt lgkmcnt(0)\n\t"
      "v_mfma_f32_16x16x32_bf16 v[16:19], a[252:255], a[28:31], v[16:19]\n\t"
      "s_cselect_b32 s101, 0, s101\n\t"
      "v_mfma_f32_16x16x32_bf16 v[20:23], a[224:227], a[32:35], 0\n\t"
      "v_mfma_f32_16x16x32_bf16 v[20:23], a[228:231], a[36:39], v[20:23]\n\t"
      "v_mfma_f32_16x16x32_bf16 v[20:23], a[232:235], a[40:43], v[20:23]\n\t"
      "v_mfma_f32_16x16x32_bf16 v[20:23], a[236:239], a[44:47], v[20:23]\n\t"
      "s_nop 2\n\t"
      "v_mul_f32 v16, v16, v72\n\t"
      "v_mul_f32 v17, v17, v72\n\t"
      "v_mfma_f32_16x16x32_bf16 v[20:23], a[240:243], a[48:51], v[20:23]\n\t"
      "v_mul_f32 v18, v18, v72\n\t"
      "v_mul_f32 v19, v19, v72\n\t"
      "v_mfma_f32_16x16x32_bf16 v[20:23], a[244:247], a[52:55], v[20:23]\n\t"
      "v_cvt_pk_bf16_f32 v56, v16, v17\n\t"
      "v_cvt_pk_bf16_f32 v57, v18, v19\n\t"
      "v_mfma_f32_16x16x32_bf16 v[20:23], a[248:251], a[56:59], v[20:23]\n\t"
      "v_mfma_f32_16x16x32_bf16 v[20:23], a[252:255], a[60:63], v[20:23]\n\t"
      "v_mfma_f32_16x16x32_bf16 v[24:27], a[224:227], a[64:67], 0\n\t"
      "v_mfma_f32_16x16x32_bf16 v[24:27], a[228:231], a[68:71], v[24:27]\n\t"
      "v_mfma_f32_16x16x32_bf16 v[24:27], a[232:235], a[72:75], v[24:27]\n\t"
      "v_mfma_f32_16x16x32_bf16 v[24:27], a[236:239], a[76:79], v[24:27]\n\t"
      "s_nop 3\n\t"
      "v_mul_f32 v20, v20, v73\n\t"
      "v_mul_f32 v21, v21, v73\n\t"
      "v_mfma_f32_16x16x32_bf16 v[24:27], a[240:243], a[80:83], v[24:27]\n\t"
      "s_waitcnt vmcnt(5)\n\t"
      "s_barrier\n\t"
      "v_mfma_f32_16x16x32_bf16 v[24:27], a[244:247], a[84:87], v[24:27]\n\t"
      "s_lshl_b32 s3, s93, 6\n\t"
      "s_lshl_b32 s2, s7, 2\n\t"
      "s_add_u32 s3, s3, s2\n\t"
      "v_mov_b32 v87, s3\n\t"
      "s_mov_b32 s46, s28\n\t"
      "buffer_store_dword v86, v87, s[44:47], 0 offen offset:0 sc1\n\t"
      "s_add_u32 s93, s93, 1\n\t"
      "s_cmp_eq_u32 s93, 7\n\t"
      "s_cselect_b32 s93, 0, s93\n\t"
      "v_mfma_f32_16x16x32_bf16 v[24:27], a[248:251], a[88:91], v[24:27]\n\t"
      "v_mfma_f32_16x16x32_bf16 v[24:27], a[252:255], a[92:95], v[24:27]\n\t"
      "v_mfma_f32_16x16x32_bf16 v[28:31], a[224:227], a[96:99], 0\n\t"
      "v_mfma_f32_16x16x32_bf16 v[28:31], a[228:231], a[100:103], v[28:31]\n\t"
      "v_mul_f32 v22, v22, v73\n\t"
      "v_mfma_f32_16x16x32_bf16 v[28:31], a[232:235], a[104:107], v[28:31]\n\t"
      "v_mul_f32 v23, v23, v73\n\t"
      "v_cvt_pk_bf16_f32 v58, v20, v21\n\t"
      "v_mfma_f32_16x16x32_bf16 v[28:31], a[236:239], a[108:111], v[28:31]\n\t"
      "v_cvt_pk_bf16_f32 v59, v22, v23\n\t"
      "s_lshl_b32 s95, s101, 14\n\t"
      "v_mfma_f32_16x16x32_bf16 v[28:31], a[240:243], a[112:115], v[28:31]\n\t"
      "s_lshl_b32 s96, s88, 10\n\t"
      "s_add_u32 s95, s95, s96\n\t"
      "v_mfma_f32_16x16x32_bf16 v[28:31], a[244:247], a[116:119], v[28:31]\n\t"
      "s_add_u32 s96, s95, 0\n\t"
      "buffer_store_dwordx4 v[56:59], v47, s[56:59], s96 offen offset:0 sc1\n\t"
      "v_mfma_f32_16x16x32_bf16 v[28:31], a[248:251], a[120:123], v[28:31]\n\t"
      "v_mul_f32 v24, v24, v74\n\t"
      "v_mul_f32 v25, v25, v74\n\t"
      "v_mfma_f32_16x16x32_bf16 v[28:31], a[252:255], a[124:127], v[28:31]\n\t"
      "v_mul_f32 v26, v26, v74\n\t"
      "v_mul_f32 v27, v27, v74\n\t"
      "v_mfma_f32_16x16x32_bf16 v[32:35], a[224:227], a[128:131], 0\n\t"
      "v_cvt_pk_bf16_f32 v60, v24, v25\n\t"
      "v_cvt_pk_bf16_f32 v61, v26, v27\n\t"
      "v_mfma_f32_16x16x32_bf16 v[32:35], a[228:231], a[132:135], v[32:35]\n\t"
      "v_mfma_f32_16x16x32_bf16 v[32:35], a[232:235], a[136:139], v[32:35]\n\t"
      "v_mfma_f32_16x16x32_bf16 v[32:35], a[236:239], a[140:143], v[32:35]\n\t"
      "v_mul_f32 v28, v28, v75\n\t"
      "v_mul_f32 v29, v29, v75\n\t"
      "v_mfma_f32_16x16x32_bf16 v[32:35], a[240:243], a[144:147], v[32:35]\n\t"
      "v_mul_f32 v30, v30, v75\n\t"
      "v_mul_f32 v31, v31, v75\n\t"
      "v_mfma_f32_16x16x32_bf16 v[32:35], a[244:247], a[148:151], v[32:35]\n\t"
      "v_cvt_pk_bf16_f32 v62, v28, v29\n\t"
      "v_cvt_pk_bf16_f32 v63, v30, v31\n\t"
      "v_mfma_f32_16x16x32_bf16 v[32:35], a[248:251], a[152:155], v[32:35]\n\t"
      "s_add_u32 s96, s95, 4096\n\t"
      "buffer_store_dwordx4 v[60:63], v47, s[56:59], s96 offen offset:0 sc1\n\t"
      "v_mfma_f32_16x16x32_bf16 v[32:35], a[252:255], a[156:159], v[32:35]\n\t"
      "v_mfma_f32_16x16x32_bf16 v[36:39], a[224:227], a[160:163], 0\n\t"
      "v_mfma_f32_16x16x32_bf16 v[36:39], a[228:231], a[164:167], v[36:39]\n\t"
      "v_mfma_f32_16x16x32_bf16 v[36:39], a[232:235], a[168:171], v[36:39]\n\t"
      "s_lshl_b32 s64, s62, 6\n\t"
      "s_mov_b32 s46, 2048\n\t"
      "buffer_load_dword v67, v66, s[44:47], s64 offen offset:0 sc1\n\t"
      "v_mfma_f32_16x16x32_bf16 v[36:39], a[236:239], a[172:175], v[36:39]\n\t"
      "s_nop 0\n\t"
      "v_mul_f32 v32, v32, v76\n\t"
      "v_mfma_f32_16x16x32_bf16 v[36:39], a[240:243], a[176:179], v[36:39]\n\t"
      "v_mul_f32 v33, v33, v76\n\t"
      "v_mul_f32 v34, v34, v76\n\t"
      "v_mfma_f32_16x16x32_bf16 v[36:39], a[244:247], a[180:183], v[36:39]\n\t"
      "v_mul_f32 v35, v35, v76\n\t"
      "v_cvt_pk_bf16_f32 v56, v32, v33\n\t"
      "v_mfma_f32_16x16x32_bf16 v[36:39], a[248:251], a[184:187], v[36:39]\n\t"
      "v_cvt_pk_bf16_f32 v57, v34, v35\n\t"
      "v_mfma_f32_16x16x32_bf16 v[36:39], a[252:255], a[188:191], v[36:39]\n\t"
      "v_mfma_f32_16x16x32_bf16 v[40:43], a[224:227], a[192:195], 0\n\t"
      "v_mfma_f32_16x16x32_bf16 v[40:43], a[228:231], a[196:199], v[40:43]\n\t"
      "v_mfma_f32_16x16x32_bf16 v[40:43], a[232:235], a[200:203], v[40:43]\n\t"
      "s_waitcnt vmcnt(4)\n\t"
      "s_barrier\n\t"
      "v_mfma_f32_16x16x32_bf16 v[40:43], a[236:239], a[204:207], v[40:43]\n\t"
      "s_lshl_b32 s3, s93, 6\n\t"
      "s_lshl_b32 s2, s7, 2\n\t"
      "s_add_u32 s3, s3, s2\n\t"
      "v_mov_b32 v87, s3\n\t"
      "s_mov_b32 s46, s28\n\t"
      "buffer_store_dword v86, v87, s[44:47], 0 offen offset:0 sc1\n\t"
      "s_add_u32 s93, s93, 1\n\t"
      "s_cmp_eq_u32 s93, 7\n\t"
      "s_cselect_b32 s93, 0, s93\n\t"
      "v_mfma_f32_16x16x32_bf16 v[40:43], a[240:243], a[208:211], v[40:43]\n\t"
      "v_mfma_f32_16x16x32_bf16 v[40:43], a[244:247], a[212:215], v[40:43]\n\t"
      "v_mfma_f32_16x16x32_bf16 v[40:43], a[248:251], a[216:219], v[40:43]\n\t"
      "v_mfma_f32_16x16x32_bf16 v[40:43], a[252:255], a[220:223], v[40:43]\n\t"
      "v_mul_f32 v36, v36, v77\n\t"
      "v_mul_f32 v37, v37, v77\n\t"
      "v_mul_f32 v38, v38, v77\n\t"
      "v_mul_f32 v39, v39, v77\n\t"
      "v_cvt_pk_bf16_f32 v58, v36, v37\n\t"
      "v_cvt_pk_bf16_f32 v59, v38, v39\n\t"
      "s_add_u32 s96, s95, 8192\n\t"
      "buffer_store_dwordx4 v[56:59], v47, s[56:59], s96 offen offset:0 sc1\n\t"
      "v_mul_f32 v40, v40, v78\n\t"
      "v_mul_f32 v41, v41, v78\n\t"
      "v_mul_f32 v42, v42, v78\n\t"
      "v_mul_f32 v43, v43, v78\n\t"
      "v_cvt_pk_bf16_f32 v60, v40, v41\n\t"
      "v_cvt_pk_bf16_f32 v61, v42, v43\n\t"
      "s_add_u32 s96, s95, 12288\n\t"
      "buffer_store_dwordx2 v[60:61], v48, s[56:59], s96 offen offset:0 sc1\n\t"
      "s_add_u32 s101, s101, 1\n\t"
      "s_cmp_eq_u32 s101, 7\n\t"
      "s_cselect_b32 s101, 0, s101\n\t"
      "s_waitcnt vmcnt(3)\n\t"
      "s_mov_b32 s92, 0\n\t"
      "PSP0%=:\n\t"
      "v_cmp_eq_u32 vcc, v67, v86\n\t"
      "s_nop 0\n\t"
      "s_cmp_eq_u64 vcc, -1\n\t"
      "s_cbranch_scc1 PGO0%=\n\t"
      "s_add_u32 s92, s92, 1\n\t"
      "s_cmp_gt_u32 s92, 0x8000\n\t"
      "s_cbranch_scc1 PGO0%=\n\t"
      "s_sleep 1\n\t"
      "s_lshl_b32 s64, s62, 6\n\t"
      "s_mov_b32 s46, 2048\n\t"
      "buffer_load_dword v67, v66, s[44:47], s64 offen offset:0 sc1\n\t"
      "v_mbcnt_lo_u32_b32 v250, -1, 0\n\t"
      "v_mbcnt_hi_u32_b32 v250, -1, v250\n\t"
      "v_and_b32 v250, 15, v250\n\t"
      "v_lshlrev_b32 v250, 2, v250\n\t"
      "s_mov_b32 s46, s29\n\t"
      "buffer_load_dword v248, v250, s[44:47], 0 offen offset:512 sc1\n\t"
      "s_waitcnt vmcnt(0)\n\t"
      "v_lshlrev_b32 v251, 1, v250\n\t"
      "v_mov_b32 v249, v86\n\t"
      "s_mov_b32 s46, s29\n\t"
      "buffer_store_dwordx2 v[248:249], v251, s[44:47], 0 offen offset:1024 sc1\n\t"
      "s_branch PSP0%=\n\t"
      "PGO0%=:\n\t"
      "s_lshl_b32 s65, s62, 4\n\t"
      "s_add_u32 s65, s65, s7\n\t"
      "s_lshl_b32 s65, s65, 10\n\t"
      "buffer_load_dwordx4 v[136:139], v68, s[48:51], s65 offen offset:0 sc1\n\t"
      "s_add_u32 s65, s65, 114688\n\t"
      "buffer_load_dwordx4 v[140:143], v68, s[48:51], s65 offen offset:0 sc1\n\t"
      "s_add_u32 s65, s65, 114688\n\t"
      "buffer_load_dwordx4 v[144:147], v68, s[48:51], s65 offen offset:0 sc1\n\t"
      "s_add_u32 s65, s65, 114688\n\t"
      "buffer_load_dwordx4 v[148:151], v68, s[48:51], s65 offen offset:0 sc1\n\t"
      "s_add_u32 s65, s65, 114688\n\t"
      "buffer_load_dwordx4 v[152:155], v68, s[48:51], s65 offen offset:0 sc1\n\t"
      "s_add_u32 s65, s65, 114688\n\t"
      "buffer_load_dwordx4 v[156:159], v68, s[48:51], s65 offen offset:0 sc1\n\t"
      "s_add_u32 s65, s65, 114688\n\t"
      "buffer_load_dwordx4 v[160:163], v68, s[48:51], s65 offen offset:0 sc1\n\t"
      "s_waitcnt vmcnt(7)\n\t"
      "s_barrier\n\t"
      "s_lshl_b32 s3, s93, 6\n\t"
      "s_lshl_b32 s2, s7, 2\n\t"
      "s_add_u32 s3, s3, s2\n\t"
      "v_mov_b32 v87, s3\n\t"
      "s_mov_b32 s46, s28\n\t"
      "buffer_store_dword v86, v87, s[44:47], 0 offen offset:0 sc1\n\t"
      "s_add_u32 s93, s93, 1\n\t"
      "s_cmp_eq_u32 s93, 7\n\t"
      "s_cselect_b32 s93, 0, s93\n\t"
      "s_add_u32 s65, s65, 114688\n\t"
      "buffer_load_dwordx4 v[164:167], v68, s[48:51], s65 offen offset:0 sc1\n\t"
      "s_add_u32 s65, s65, 114688\n\t"
      "buffer_load_dwordx4 v[168:171], v68, s[48:51], s65 offen offset:0 sc1\n\t"
      "s_add_u32 s65, s65, 114688\n\t"
      "buffer_load_dwordx4 v[172:175], v68, s[48:51], s65 offen offset:0 sc1\n\t"
      "s_add_u32 s65, s65, 114688\n\t"
      "buffer_load_dwordx4 v[176:179], v68, s[48:51], s65 offen offset:0 sc1\n\t"
      "s_add_u32 s65, s65, 114688\n\t"
      "buffer_load_dwordx4 v[180:183], v68, s[48:51], s65 offen offset:0 sc1\n\t"
      "s_add_u32 s65, s65, 114688\n\t"
      "buffer_load_dwordx4 v[184:187], v68, s[48:51], s65 offen offset:0 sc1\n\t"
      "s_add_u32 s65, s65, 114688\n\t"
      "buffer_load_dwordx4 v[188:191], v68, s[48:51], s65 offen offset:0 sc1\n\t"
      "s_add_u32 s65, s65, 114688\n\t"
      "buffer_load_dwordx4 v[192:195], v68, s[48:51], s65 offen offset:0 sc1\n\t"
      "s_add_u32 s65, s65, 114688\n\t"
      "buffer_load_dwordx4 v[196:199], v68, s[48:51], s65 offen offset:0 sc1\n\t"
      "s_cmp_eq_u32 s88, 3\n\t"
      "s_cbranch_scc1 RB%=\n\t"
      "s_lshl_b32 s64, s63, 6\n\t"
      "s_mov_b32 s46, 2048\n\t"
      "buffer_load_dword v67, v66, s[44:47], s64 offen offset:0 sc1\n\t"
      "s_waitcnt vmcnt(0)\n\t"
      "s_mov_b32 s92, 0\n\t"
      "PSP1%=:\n\t"
      "v_cmp_eq_u32 vcc, v67, v86\n\t"
      "s_nop 0\n\t"
      "s_cmp_eq_u64 vcc, -1\n\t"
      "s_cbranch_scc1 PGO1%=\n\t"
      "s_add_u32 s92, s92, 1\n\t"
      "s_cmp_gt_u32 s92, 0x8000\n\t"
      "s_cbranch_scc1 PGO1%=\n\t"
      "s_sleep 1\n\t"
      "s_lshl_b32 s64, s63, 6\n\t"
      "s_mov_b32 s46, 2048\n\t"
      "buffer_load_dword v67, v66, s[44:47], s64 offen offset:0 sc1\n\t"
      "v_mbcnt_lo_u32_b32 v250, -1, 0\n\t"
      "v_mbcnt_hi_u32_b32 v250, -1, v250\n\t"
      "v_and_b32 v250, 15, v250\n\t"
      "v_lshlrev_b32 v250, 2, v250\n\t"
      "s_mov_b32 s46, s29\n\t"
      "buffer_load_dword v248, v250, s[44:47], 0 offen offset:512 sc1\n\t"
      "s_waitcnt vmcnt(0)\n\t"
      "v_lshlrev_b32 v251, 1, v250\n\t"
      "v_mov_b32 v249, v86\n\t"
      "s_mov_b32 s46, s29\n\t"
      "buffer_store_dwordx2 v[248:249], v251, s[44:47], 0 offen offset:1024 sc1\n\t"
      "s_branch PSP1%=\n\t"
      "PGO1%=:\n\t"
      "s_lshl_b32 s65, s63, 4\n\t"
      "s_add_u32 s65, s65, s7\n\t"
      "s_lshl_b32 s65, s65, 10\n\t"
      "buffer_load_dwordx4 v[200:203], v68, s[48:51], s65 offen offset:0 sc1\n\t"
      "s_add_u32 s65, s65, 114688\n\t"
      "buffer_load_dwordx4 v[204:207], v68, s[48:51], s65 offen offset:0 sc1\n\t"
      "s_add_u32 s65, s65, 114688\n\t"
      "buffer_load_dwordx4 v[208:211], v68, s[48:51], s65 offen offset:0 sc1\n\t"
      "s_add_u32 s65, s65, 114688\n\t"
      "buffer_load_dwordx4 v[212:215], v68, s[48:51], s65 offen offset:0 sc1\n\t"
      "s_add_u32 s65, s65, 114688\n\t"
      "buffer_load_dwordx4 v[216:219], v68, s[48:51], s65 offen offset:0 sc1\n\t"
      "s_add_u32 s65, s65, 114688\n\t"
      "buffer_load_dwordx4 v[220:223], v68, s[48:51], s65 offen offset:0 sc1\n\t"
      "s_add_u32 s65, s65, 114688\n\t"
      "buffer_load_dwordx4 v[224:227], v68, s[48:51], s65 offen offset:0 sc1\n\t"
      "s_add_u32 s65, s65, 114688\n\t"
      "buffer_load_dwordx4 v[228:231], v68, s[48:51], s65 offen offset:0 sc1\n\t"
      "s_add_u32 s65, s65, 114688\n\t"
      "buffer_load_dwordx4 v[232:235], v68, s[48:51], s65 offen offset:0 sc1\n\t"
      "s_add_u32 s65, s65, 114688\n\t"
      "buffer_load_dwordx4 v[236:239], v68, s[48:51], s65 offen offset:0 sc1\n\t"
      "s_add_u32 s65, s65, 114688\n\t"
      "buffer_load_dwordx4 v[240:243], v68, s[48:51], s65 offen offset:0 sc1\n\t"
      "s_add_u32 s65, s65, 114688\n\t"
      "buffer_load_dwordx4 v[244:247], v68, s[48:51], s65 offen offset:0 sc1\n\t"
      "s_add_u32 s65, s65, 114688\n\t"
      "buffer_load_dwordx4 v[104:107], v68, s[48:51], s65 offen offset:0 sc1\n\t"
      "s_add_u32 s65, s65, 114688\n\t"
      "buffer_load_dwordx4 v[108:111], v68, s[48:51], s65 offen offset:0 sc1\n\t"
      "s_add_u32 s65, s65, 114688\n\t"
      "buffer_load_dwordx4 v[112:115], v68, s[48:51], s65 offen offset:0 sc1\n\t"
      "s_add_u32 s65, s65, 114688\n\t"
      "buffer_load_dwordx4 v[116:119], v68, s[48:51], s65 offen offset:0 sc1\n\t"
      "s_branch RC%=\n\t"
      "RB%=:\n\t"
      "s_waitcnt vmcnt(0)\n\t"
      "RC%=:\n\t"
      "s_mov_b32 s66, 0x3f80\n\t"
      "s_mov_b32 s67, 0x3f800000\n\t"
      "v_mov_b32 v16, 0\n\t"
      "v_mov_b32 v17, 0\n\t"
      "v_mov_b32 v18, 0\n\t"
      "v_mov_b32 v19, 0\n\t"
      "v_mov_b32 v20, 0\n\t"
      "v_mov_b32 v21, 0\n\t"
      "v_mov_b32 v22, 0\n\t"
      "v_mov_b32 v23, 0\n\t"
      "v_dot2c_f32_bf16 v16, s66, v136\n\t"
      "v_dot2c_f32_bf16 v17, s67, v136\n\t"
      "v_dot2c_f32_bf16 v18, s66, v137\n\t"
      "v_dot2c_f32_bf16 v19, s67, v137\n\t"
      "v_dot2c_f32_bf16 v20, s66, v138\n\t"
      "v_dot2c_f32_bf16 v21, s67, v138\n\t"
      "v_dot2c_f32_bf16 v22, s66, v139\n\t"
      "v_dot2c_f32_bf16 v23, s67, v139\n\t"
      "v_dot2c_f32_bf16 v16, s66, v140\n\t"
      "v_dot2c_f32_bf16 v17, s67, v140\n\t"
      "v_dot2c_f32_bf16 v18, s66, v141\n\t"
      "v_dot2c_f32_bf16 v19, s67, v141\n\t"
      "v_dot2c_f32_bf16 v20, s66, v142\n\t"
      "v_dot2c_f32_bf16 v21, s67, v142\n\t"
      "v_dot2c_f32_bf16 v22, s66, v143\n\t"
      "v_dot2c_f32_bf16 v23, s67, v143\n\t"
      "v_dot2c_f32_bf16 v16, s66, v144\n\t"
      "v_dot2c_f32_bf16 v17, s67, v144\n\t"
      "v_dot2c_f32_bf16 v18, s66, v145\n\t"
      "v_dot2c_f32_bf16 v19, s67, v145\n\t"
      "v_dot2c_f32_bf16 v20, s66, v146\n\t"
      "v_dot2c_f32_bf16 v21, s67, v146\n\t"
      "v_dot2c_f32_bf16 v22, s66, v147\n\t"
      "v_dot2c_f32_bf16 v23, s67, v147\n\t"
      "v_dot2c_f32_bf16 v16, s66, v148\n\t"
      "v_dot2c_f32_bf16 v17, s67, v148\n\t"
      "v_dot2c_f32_bf16 v18, s66, v149\n\t"
      "v_dot2c_f32_bf16 v19, s67, v149\n\t"
      "v_dot2c_f32_bf16 v20, s66, v150\n\t"
      "v_dot2c_f32_bf16 v21, s67, v150\n\t"
      "v_dot2c_f32_bf16 v22, s66, v151\n\t"
      "v_dot2c_f32_bf16 v23, s67, v151\n\t"
      "v_dot2c_f32_bf16 v16, s66, v152\n\t"
      "v_dot2c_f32_bf16 v17, s67, v152\n\t"
      "v_dot2c_f32_bf16 v18, s66, v153\n\t"
      "v_dot2c_f32_bf16 v19, s67, v153\n\t"
      "v_dot2c_f32_bf16 v20, s66, v154\n\t"
      "v_dot2c_f32_bf16 v21, s67, v154\n\t"
      "v_dot2c_f32_bf16 v22, s66, v155\n\t"
      "v_dot2c_f32_bf16 v23, s67, v155\n\t"
      "v_dot2c_f32_bf16 v16, s66, v156\n\t"
      "v_dot2c_f32_bf16 v17, s67, v156\n\t"
      "v_dot2c_f32_bf16 v18, s66, v157\n\t"
      "v_dot2c_f32_bf16 v19, s67, v157\n\t"
      "v_dot2c_f32_bf16 v20, s66, v158\n\t"
      "v_dot2c_f32_bf16 v21, s67, v158\n\t"
      "v_dot2c_f32_bf16 v22, s66, v159\n\t"
      "v_dot2c_f32_bf16 v23, s67, v159\n\t"
      "v_dot2c_f32_bf16 v16, s66, v160\n\t"
      "v_dot2c_f32_bf16 v17, s67, v160\n\t"
      "v_dot2c_f32_bf16 v18, s66, v161\n\t"
      "v_dot2c_f32_bf16 v19, s67, v161\n\t"
      "v_dot2c_f32_bf16 v20, s66, v162\n\t"
      "v_dot2c_f32_bf16 v21, s67, v162\n\t"
      "v_dot2c_f32_bf16 v22, s66, v163\n\t"
      "v_dot2c_f32_bf16 v23, s67, v163\n\t"
      "v_dot2c_f32_bf16 v16, s66, v164\n\t"
      "v_dot2c_f32_bf16 v17, s67, v164\n\t"
      "v_dot2c_f32_bf16 v18, s66, v165\n\t"
      "v_dot2c_f32_bf16 v19, s67, v165\n\t"
      "v_dot2c_f32_bf16 v20, s66, v166\n\t"
      "v_dot2c_f32_bf16 v21, s67, v166\n\t"
      "v_dot2c_f32_bf16 v22, s66, v167\n\t"
      "v_dot2c_f32_bf16 v23, s67, v167\n\t"
      "v_dot2c_f32_bf16 v16, s66, v168\n\t"
      "v_dot2c_f32_bf16 v17, s67, v168\n\t"
      "v_dot2c_f32_bf16 v18, s66, v169\n\t"
      "v_dot2c_f32_bf16 v19, s67, v169\n\t"
      "v_dot2c_f32_bf16 v20, s66, v170\n\t"
      "v_dot2c_f32_bf16 v21, s67, v170\n\t"
      "v_dot2c_f32_bf16 v22, s66, v171\n\t"
      "v_dot2c_f32_bf16 v23, s67, v171\n\t"
      "v_dot2c_f32_bf16 v16, s66, v172\n\t"
      "v_dot2c_f32_bf16 v17, s67, v172\n\t"
      "v_dot2c_f32_bf16 v18, s66, v173\n\t"
      "v_dot2c_f32_bf16 v19, s67, v173\n\t"
      "v_dot2c_f32_bf16 v20, s66, v174\n\t"
      "v_dot2c_f32_bf16 v21, s67, v174\n\t"
      "v_dot2c_f32_bf16 v22, s66, v175\n\t"
      "v_dot2c_f32_bf16 v23, s67, v175\n\t"
      "v_dot2c_f32_bf16 v16, s66, v176\n\t"
      "v_dot2c_f32_bf16 v17, s67, v176\n\t"
      "v_dot2c_f32_bf16 v18, s66, v177\n\t"
      "v_dot2c_f32_bf16 v19, s67, v177\n\t"
      "v_dot2c_f32_bf16 v20, s66, v178\n\t"
      "v_dot2c_f32_bf16 v21, s67, v178\n\t"
      "v_dot2c_f32_bf16 v22, s66, v179\n\t"
      "v_dot2c_f32_bf16 v23, s67, v179\n\t"
      "v_dot2c_f32_bf16 v16, s66, v180\n\t"
      "v_dot2c_f32_bf16 v17, s67, v180\n\t"
      "v_dot2c_f32_bf16 v18, s66, v181\n\t"
      "v_dot2c_f32_bf16 v19, s67, v181\n\t"
      "v_dot2c_f32_bf16 v20, s66, v182\n\t"
      "v_dot2c_f32_bf16 v21, s67, v182\n\t"
      "v_dot2c_f32_bf16 v22, s66, v183\n\t"
      "v_dot2c_f32_bf16 v23, s67, v183\n\t"
      "v_dot2c_f32_bf16 v16, s66, v184\n\t"
      "v_dot2c_f32_bf16 v17, s67, v184\n\t"
      "v_dot2c_f32_bf16 v18, s66, v185\n\t"
      "v_dot2c_f32_bf16 v19, s67, v185\n\t"
      "v_dot2c_f32_bf16 v20, s66, v186\n\t"
      "v_dot2c_f32_bf16 v21, s67, v186\n\t"
      "v_dot2c_f32_bf16 v22, s66, v187\n\t"
      "v_dot2c_f32_bf16 v23, s67, v187\n\t"
      "v_dot2c_f32_bf16 v16, s66, v188\n\t"
      "v_dot2c_f32_bf16 v17, s67, v188\n\t"
      "v_dot2c_f32_bf16 v18, s66, v189\n\t"
      "v_dot2c_f32_bf16 v19, s67, v189\n\t"
      "v_dot2c_f32_bf16 v20, s66, v190\n\t"
      "v_dot2c_f32_bf16 v21, s67, v190\n\t"
      "v_dot2c_f32_bf16 v22, s66, v191\n\t"
      "v_dot2c_f32_bf16 v23, s67, v191\n\t"
      "v_dot2c_f32_bf16 v16, s66, v192\n\t"
      "v_dot2c_f32_bf16 v17, s67, v192\n\t"
      "v_dot2c_f32_bf16 v18, s66, v193\n\t"
      "v_dot2c_f32_bf16 v19, s67, v193\n\t"
      "v_dot2c_f32_bf16 v20, s66, v194\n\t"
      "v_dot2c_f32_bf16 v21, s67, v194\n\t"
      "v_dot2c_f32_bf16 v22, s66, v195\n\t"
      "v_dot2c_f32_bf16 v23, s67, v195\n\t"
      "v_dot2c_f32_bf16 v16, s66, v196\n\t"
      "v_dot2c_f32_bf16 v17, s67, v196\n\t"
      "v_dot2c_f32_bf16 v18, s66, v197\n\t"
      "v_dot2c_f32_bf16 v19, s67, v197\n\t"
      "v_dot2c_f32_bf16 v20, s66, v198\n\t"
      "v_dot2c_f32_bf16 v21, s67, v198\n\t"
      "v_dot2c_f32_bf16 v22, s66, v199\n\t"
      "v_dot2c_f32_bf16 v23, s67, v199\n\t"
      "s_nop 2\n\t"
      "v_cvt_pk_bf16_f32 v32, v16, v17\n\t"
      "v_cvt_pk_bf16_f32 v33, v18, v19\n\t"
      "v_cvt_pk_bf16_f32 v34, v20, v21\n\t"
      "v_cvt_pk_bf16_f32 v35, v22, v23\n\t"
      "s_lshl_b32 s64, s62, 7\n\t"
      "buffer_store_dwordx2 v[32:33], v64, s[84:87], s64 offen offset:0\n\t"
      "buffer_store_dwordx2 v[34:35], v65, s[84:87], s64 offen offset:0\n\t"
      "s_cmp_eq_u32 s88, 3\n\t"
      "s_cbranch_scc1 RDONE%=\n\t"
      "s_waitcnt vmcnt(0)\n\t"
      "s_mov_b32 s66, 0x3f80\n\t"
      "s_mov_b32 s67, 0x3f800000\n\t"
      "v_mov_b32 v24, 0\n\t"
      "v_mov_b32 v25, 0\n\t"
      "v_mov_b32 v26, 0\n\t"
      "v_mov_b32 v27, 0\n\t"
      "v_mov_b32 v28, 0\n\t"
      "v_mov_b32 v29, 0\n\t"
      "v_mov_b32 v30, 0\n\t"
      "v_mov_b32 v31, 0\n\t"
      "v_dot2c_f32_bf16 v24, s66, v200\n\t"
      "v_dot2c_f32_bf16 v25, s67, v200\n\t"
      "v_dot2c_f32_bf16 v26, s66, v201\n\t"
      "v_dot2c_f32_bf16 v27, s67, v201\n\t"
      "v_dot2c_f32_bf16 v28, s66, v202\n\t"
      "v_dot2c_f32_bf16 v29, s67, v202\n\t"
      "v_dot2c_f32_bf16 v30, s66, v203\n\t"
      "v_dot2c_f32_bf16 v31, s67, v203\n\t"
      "v_dot2c_f32_bf16 v24, s66, v204\n\t"
      "v_dot2c_f32_bf16 v25, s67, v204\n\t"
      "v_dot2c_f32_bf16 v26, s66, v205\n\t"
      "v_dot2c_f32_bf16 v27, s67, v205\n\t"
      "v_dot2c_f32_bf16 v28, s66, v206\n\t"
      "v_dot2c_f32_bf16 v29, s67, v206\n\t"
      "v_dot2c_f32_bf16 v30, s66, v207\n\t"
      "v_dot2c_f32_bf16 v31, s67, v207\n\t"
      "v_dot2c_f32_bf16 v24, s66, v208\n\t"
      "v_dot2c_f32_bf16 v25, s67, v208\n\t"
      "v_dot2c_f32_bf16 v26, s66, v209\n\t"
      "v_dot2c_f32_bf16 v27, s67, v209\n\t"
      "v_dot2c_f32_bf16 v28, s66, v210\n\t"
      "v_dot2c_f32_bf16 v29, s67, v210\n\t"
      "v_dot2c_f32_bf16 v30, s66, v211\n\t"
      "v_dot2c_f32_bf16 v31, s67, v211\n\t"
      "v_dot2c_f32_bf16 v24, s66, v212\n\t"
      "v_dot2c_f32_bf16 v25, s67, v212\n\t"
      "v_dot2c_f32_bf16 v26, s66, v213\n\t"
      "v_dot2c_f32_bf16 v27, s67, v213\n\t"
      "v_dot2c_f32_bf16 v28, s66, v214\n\t"
      "v_dot2c_f32_bf16 v29, s67, v214\n\t"
      "v_dot2c_f32_bf16 v30, s66, v215\n\t"
      "v_dot2c_f32_bf16 v31, s67, v215\n\t"
      "v_dot2c_f32_bf16 v24, s66, v216\n\t"
      "v_dot2c_f32_bf16 v25, s67, v216\n\t"
      "v_dot2c_f32_bf16 v26, s66, v217\n\t"
      "v_dot2c_f32_bf16 v27, s67, v217\n\t"
      "v_dot2c_f32_bf16 v28, s66, v218\n\t"
      "v_dot2c_f32_bf16 v29, s67, v218\n\t"
      "v_dot2c_f32_bf16 v30, s66, v219\n\t"
      "v_dot2c_f32_bf16 v31, s67, v219\n\t"
      "v_dot2c_f32_bf16 v24, s66, v220\n\t"
      "v_dot2c_f32_bf16 v25, s67, v220\n\t"
      "v_dot2c_f32_bf16 v26, s66, v221\n\t"
      "v_dot2c_f32_bf16 v27, s67, v221\n\t"
      "v_dot2c_f32_bf16 v28, s66, v222\n\t"
      "v_dot2c_f32_bf16 v29, s67, v222\n\t"
      "v_dot2c_f32_bf16 v30, s66, v223\n\t"
      "v_dot2c_f32_bf16 v31, s67, v223\n\t"
      "v_dot2c_f32_bf16 v24, s66, v224\n\t"
      "v_dot2c_f32_bf16 v25, s67, v224\n\t"
      "v_dot2c_f32_bf16 v26, s66, v225\n\t"
      "v_dot2c_f32_bf16 v27, s67, v225\n\t"
      "v_dot2c_f32_bf16 v28, s66, v226\n\t"
      "v_dot2c_f32_bf16 v29, s67, v226\n\t"
      "v_dot2c_f32_bf16 v30, s66, v227\n\t"
      "v_dot2c_f32_bf16 v31, s67, v227\n\t"
      "v_dot2c_f32_bf16 v24, s66, v228\n\t"
      "v_dot2c_f32_bf16 v25, s67, v228\n\t"
      "v_dot2c_f32_bf16 v26, s66, v229\n\t"
      "v_dot2c_f32_bf16 v27, s67, v229\n\t"
      "v_dot2c_f32_bf16 v28, s66, v230\n\t"
      "v_dot2c_f32_bf16 v29, s67, v230\n\t"
      "v_dot2c_f32_bf16 v30, s66, v231\n\t"
      "v_dot2c_f32_bf16 v31, s67, v231\n\t"
      "v_dot2c_f32_bf16 v24, s66, v232\n\t"
      "v_dot2c_f32_bf16 v25, s67, v232\n\t"
      "v_dot2c_f32_bf16 v26, s66, v233\n\t"
      "v_dot2c_f32_bf16 v27, s67, v233\n\t"
      "v_dot2c_f32_bf16 v28, s66, v234\n\t"
      "v_dot2c_f32_bf16 v29, s67, v234\n\t"
      "v_dot2c_f32_bf16 v30, s66, v235\n\t"
      "v_dot2c_f32_bf16 v31, s67, v235\n\t"
      "v_dot2c_f32_bf16 v24, s66, v236\n\t"
      "v_dot2c_f32_bf16 v25, s67, v236\n\t"
      "v_dot2c_f32_bf16 v26, s66, v237\n\t"
      "v_dot2c_f32_bf16 v27, s67, v237\n\t"
      "v_dot2c_f32_bf16 v28, s66, v238\n\t"
      "v_dot2c_f32_bf16 v29, s67, v238\n\t"
      "v_dot2c_f32_bf16 v30, s66, v239\n\t"
      "v_dot2c_f32_bf16 v31, s67, v239\n\t"
      "v_dot2c_f32_bf16 v24, s66, v240\n\t"
      "v_dot2c_f32_bf16 v25, s67, v240\n\t"
      "v_dot2c_f32_bf16 v26, s66, v241\n\t"
      "v_dot2c_f32_bf16 v27, s67, v241\n\t"
      "v_dot2c_f32_bf16 v28, s66, v242\n\t"
      "v_dot2c_f32_bf16 v29, s67, v242\n\t"
      "v_dot2c_f32_bf16 v30, s66, v243\n\t"
      "v_dot2c_f32_bf16 v31, s67, v243\n\t"
      "v_dot2c_f32_bf16 v24, s66, v244\n\t"
      "v_dot2c_f32_bf16 v25, s67, v244\n\t"
      "v_dot2c_f32_bf16 v26, s66, v245\n\t"
      "v_dot2c_f32_bf16 v27, s67, v245\n\t"
      "v_dot2c_f32_bf16 v28, s66, v246\n\t"
      "v_dot2c_f32_bf16 v29, s67, v246\n\t"
      "v_dot2c_f32_bf16 v30, s66, v247\n\t"
      "v_dot2c_f32_bf16 v31, s67, v247\n\t"
      "v_dot2c_f32_bf16 v24, s66, v104\n\t"
      "v_dot2c_f32_bf16 v25, s67, v104\n\t"
      "v_dot2c_f32_bf16 v26, s66, v105\n\t"
      "v_dot2c_f32_bf16 v27, s67, v105\n\t"
      "v_dot2c_f32_bf16 v28, s66, v106\n\t"
      "v_dot2c_f32_bf16 v29, s67, v106\n\t"
      "v_dot2c_f32_bf16 v30, s66, v107\n\t"
      "v_dot2c_f32_bf16 v31, s67, v107\n\t"
      "v_dot2c_f32_bf16 v24, s66, v108\n\t"
      "v_dot2c_f32_bf16 v25, s67, v108\n\t"
      "v_dot2c_f32_bf16 v26, s66, v109\n\t"
      "v_dot2c_f32_bf16 v27, s67, v109\n\t"
      "v_dot2c_f32_bf16 v28, s66, v110\n\t"
      "v_dot2c_f32_bf16 v29, s67, v110\n\t"
      "v_dot2c_f32_bf16 v30, s66, v111\n\t"
      "v_dot2c_f32_bf16 v31, s67, v111\n\t"
      "v_dot2c_f32_bf16 v24, s66, v112\n\t"
      "v_dot2c_f32_bf16 v25, s67, v112\n\t"
      "v_dot2c_f32_bf16 v26, s66, v113\n\t"
      "v_dot2c_f32_bf16 v27, s67, v113\n\t"
      "v_dot2c_f32_bf16 v28, s66, v114\n\t"
      "v_dot2c_f32_bf16 v29, s67, v114\n\t"
      "v_dot2c_f32_bf16 v30, s66, v115\n\t"
      "v_dot2c_f32_bf16 v31, s67, v115\n\t"
      "v_dot2c_f32_bf16 v24, s66, v116\n\t"
      "v_dot2c_f32_bf16 v25, s67, v116\n\t"
      "v_dot2c_f32_bf16 v26, s66, v117\n\t"
      "v_dot2c_f32_bf16 v27, s67, v117\n\t"
      "v_dot2c_f32_bf16 v28, s66, v118\n\t"
      "v_dot2c_f32_bf16 v29, s67, v118\n\t"
      "v_dot2c_f32_bf16 v30, s66, v119\n\t"
      "v_dot2c_f32_bf16 v31, s67, v119\n\t"
      "s_nop 2\n\t"
      "v_cvt_pk_bf16_f32 v36, v24, v25\n\t"
      "v_cvt_pk_bf16_f32 v37, v26, v27\n\t"
      "v_cvt_pk_bf16_f32 v38, v28, v29\n\t"
      "v_cvt_pk_bf16_f32 v39, v30, v31\n\t"
      "s_lshl_b32 s64, s63, 7\n\t"
      "buffer_store_dwordx2 v[36:37], v64, s[84:87], s64 offen offset:0\n\t"
      "buffer_store_dwordx2 v[38:39], v65, s[84:87], s64 offen offset:0\n\t"
      "RDONE%=:\n\t"
      "s_branch FIN%=\n\t"
      "FB%=:\n\t"
      "s_waitcnt vmcnt(0)\n\t"
      "s_waitcnt lgkmcnt(0)\n\t"
      "s_barrier\n\t"
      "s_mov_b32 s99, 1\n\t"
      "s_mov_b32 s100, s6\n\t"
      "s_mov_b32 s36, s10\n\t"
      "s_and_b32 s37, s11, 0xffff\n\t"
      "v_mov_b32 v72, 0xff800000\n\t"
      "v_mov_b32 v73, 0xff800000\n\t"
      "v_mov_b32 v74, 0xff800000\n\t"
      "v_mov_b32 v75, 0xff800000\n\t"
      "v_mov_b32 v76, 0xff800000\n\t"
      "v_mov_b32 v77, 0xff800000\n\t"
      "v_mov_b32 v78, 0xff800000\n\t"
      "s_mov_b32 s91, 17\n\t"
      "FBL%=:\n\t"
      "s_cmp_lg_u32 s91, 1\n\t"
      "s_cbranch_scc1 FBN%=\n\t"
      "s_mov_b32 s36, s10\n\t"
      "s_and_b32 s37, s11, 0xffff\n\t"
      "FBN%=:\n\t"
      "s_add_u32 m0, s89, 0\n\t"
      "s_nop 0\n\t"
      "buffer_load_dwordx4 v12, s[36:39], s60 offen offset:0 nt lds\n\t"
      "buffer_load_dwordx4 v13, s[36:39], s61 offen offset:1024 nt lds\n\t"
      "buffer_load_dwordx4 v12, s[36:39], s62 offen offset:2048 nt lds\n\t"
      "buffer_load_dwordx4 v13, s[36:39], s63 offen offset:3072 nt lds\n\t"
      "s_add_u32 m0, s89, 4096\n\t"
      "s_nop 0\n\t"
      "buffer_load_dwordx4 v12, s[36:39], s64 offen offset:0 nt lds\n\t"
      "buffer_load_dwordx4 v13, s[36:39], s65 offen offset:1024 nt lds\n\t"
      "buffer_load_dwordx4 v12, s[36:39], s66 offen offset:2048 nt lds\n\t"
      "buffer_load_dwordx4 v13, s[36:39], s67 offen offset:3072 nt lds\n\t"
      "s_add_u32 s36, s36, 32768\n\t"
      "s_addc_u32 s37, s37, 0\n\t"
      "s_waitcnt vmcnt(0)\n\t"
      "ds_read_b128 a[224:227], v4 offset:0\n\t"
      "ds_read_b128 a[228:231], v5 offset:0\n\t"
      "ds_read_b128 a[232:235], v4 offset:2048\n\t"
      "ds_read_b128 a[236:239], v5 offset:2048\n\t"
      "ds_read_b128 a[240:243], v4 offset:4096\n\t"
      "ds_read_b128 a[244:247], v5 offset:4096\n\t"
      "ds_read_b128 a[248:251], v4 offset:6144\n\t"
      "ds_read_b128 a[252:255], v5 offset:6144\n\t"
      "s_waitcnt lgkmcnt(0)\n\t"
      "v_mfma_f32_16x16x32_bf16 v[16:19], a[224:227], a[0:3], 0\n\t"
      "v_mfma_f32_16x16x32_bf16 v[16:19], a[228:231], a[4:7], v[16:19]\n\t"
      "v_mfma_f32_16x16x32_bf16 v[16:19], a[232:235], a[8:11], v[16:19]\n\t"
      "v_mfma_f32_16x16x32_bf16 v[16:19], a[236:239], a[12:15], v[16:19]\n\t"
      "v_mfma_f32_16x16x32_bf16 v[16:19], a[240:243], a[16:19], v[16:19]\n\t"
      "v_mfma_f32_16x16x32_bf16 v[16:19], a[244:247], a[20:23], v[16:19]\n\t"
      "v_mfma_f32_16x16x32_bf16 v[16:19], a[248:251], a[24:27], v[16:19]\n\t"
      "v_mfma_f32_16x16x32_bf16 v[16:19], a[252:255], a[28:31], v[16:19]\n\t"
      "v_mfma_f32_16x16x32_bf16 v[20:23], a[224:227], a[32:35], 0\n\t"
      "v_mfma_f32_16x16x32_bf16 v[20:23], a[228:231], a[36:39], v[20:23]\n\t"
      "v_mfma_f32_16x16x32_bf16 v[20:23], a[232:235], a[40:43], v[20:23]\n\t"
      "v_mfma_f32_16x16x32_bf16 v[20:23], a[236:239], a[44:47], v[20:23]\n\t"
      "v_mfma_f32_16x16x32_bf16 v[20:23], a[240:243], a[48:51], v[20:23]\n\t"
      "v_mfma_f32_16x16x32_bf16 v[20:23], a[244:247], a[52:55], v[20:23]\n\t"
      "v_mfma_f32_16x16x32_bf16 v[20:23], a[248:251], a[56:59], v[20:23]\n\t"
      "v_mfma_f32_16x16x32_bf16 v[20:23], a[252:255], a[60:63], v[20:23]\n\t"
      "v_mfma_f32_16x16x32_bf16 v[24:27], a[224:227], a[64:67], 0\n\t"
      "v_mfma_f32_16x16x32_bf16 v[24:27], a[228:231], a[68:71], v[24:27]\n\t"
      "v_mfma_f32_16x16x32_bf16 v[24:27], a[232:235], a[72:75], v[24:27]\n\t"
      "v_mfma_f32_16x16x32_bf16 v[24:27], a[236:239], a[76:79], v[24:27]\n\t"
      "v_mfma_f32_16x16x32_bf16 v[24:27], a[240:243], a[80:83], v[24:27]\n\t"
      "v_mfma_f32_16x16x32_bf16 v[24:27], a[244:247], a[84:87], v[24:27]\n\t"
      "v_mfma_f32_16x16x32_bf16 v[24:27], a[248:251], a[88:91], v[24:27]\n\t"
      "v_mfma_f32_16x16x32_bf16 v[24:27], a[252:255], a[92:95], v[24:27]\n\t"
      "v_mfma_f32_16x16x32_bf16 v[28:31], a[224:227], a[96:99], 0\n\t"
      "v_mfma_f32_16x16x32_bf16 v[28:31], a[228:231], a[100:103], v[28:31]\n\t"
      "v_mfma_f32_16x16x32_bf16 v[28:31], a[232:235], a[104:107], v[28:31]\n\t"
      "v_mfma_f32_16x16x32_bf16 v[28:31], a[236:239], a[108:111], v[28:31]\n\t"
      "v_mfma_f32_16x16x32_bf16 v[28:31], a[240:243], a[112:115], v[28:31]\n\t"
      "v_mfma_f32_16x16x32_bf16 v[28:31], a[244:247], a[116:119], v[28:31]\n\t"
      "v_mfma_f32_16x16x32_bf16 v[28:31], a[248:251], a[120:123], v[28:31]\n\t"
      "v_mfma_f32_16x16x32_bf16 v[28:31], a[252:255], a[124:127], v[28:31]\n\t"
      "v_mfma_f32_16x16x32_bf16 v[32:35], a[224:227], a[128:131], 0\n\t"
      "v_mfma_f32_16x16x32_bf16 v[32:35], a[228:231], a[132:135], v[32:35]\n\t"
      "v_mfma_f32_16x16x32_bf16 v[32:35], a[232:235], a[136:139], v[32:35]\n\t"
      "v_mfma_f32_16x16x32_bf16 v[32:35], a[236:239], a[140:143], v[32:35]\n\t"
      "v_mfma_f32_16x16x32_bf16 v[32:35], a[240:243], a[144:147], v[32:35]\n\t"
      "v_mfma_f32_16x16x32_bf16 v[32:35], a[244:247], a[148:151], v[32:35]\n\t"
      "v_mfma_f32_16x16x32_bf16 v[32:35], a[248:251], a[152:155], v[32:35]\n\t"
      "v_mfma_f32_16x16x32_bf16 v[32:35], a[252:255], a[156:159], v[32:35]\n\t"
      "v_mfma_f32_16x16x32_bf16 v[36:39], a[224:227], a[160:163], 0\n\t"
      "v_mfma_f32_16x16x32_bf16 v[36:39], a[228:231], a[164:167], v[36:39]\n\t"
      "v_mfma_f32_16x16x32_bf16 v[36:39], a[232:235], a[168:171], v[36:39]\n\t"
      "v_mfma_f32_16x16x32_bf16 v[36:39], a[236:239], a[172:175], v[36:39]\n\t"
      "v_mfma_f32_16x16x32_bf16 v[36:39], a[240:243], a[176:179], v[36:39]\n\t"
      "v_mfma_f32_16x16x32_bf16 v[36:39], a[244:247], a[180:183], v[36:39]\n\t"
      "v_mfma_f32_16x16x32_bf16 v[36:39], a[248:251], a[184:187], v[36:39]\n\t"
      "v_mfma_f32_16x16x32_bf16 v[36:39], a[252:255], a[188:191], v[36:39]\n\t"
      "v_mfma_f32_16x16x32_bf16 v[40:43], a[224:227], a[192:195], 0\n\t"
      "v_mfma_f32_16x16x32_bf16 v[40:43], a[228:231], a[196:199], v[40:43]\n\t"
      "v_mfma_f32_16x16x32_bf16 v[40:43], a[232:235], a[200:203], v[40:43]\n\t"
      "v_mfma_f32_16x16x32_bf16 v[40:43], a[236:239], a[204:207], v[40:43]\n\t"
      "v_mfma_f32_16x16x32_bf16 v[40:43], a[240:243], a[208:211], v[40:43]\n\t"
      "v_mfma_f32_16x16x32_bf16 v[40:43], a[244:247], a[212:215], v[40:43]\n\t"
      "v_mfma_f32_16x16x32_bf16 v[40:43], a[248:251], a[216:219], v[40:43]\n\t"
      "v_mfma_f32_16x16x32_bf16 v[40:43], a[252:255], a[220:223], v[40:43]\n\t"
      "s_nop 7\n\t"
      "v_max3_f32 v72, v72, v16, v17\n\t"
      "v_max3_f32 v72, v72, v18, v19\n\t"
      "v_max3_f32 v73, v73, v20, v21\n\t"
      "v_max3_f32 v73, v73, v22, v23\n\t"
      "v_max3_f32 v74, v74, v24, v25\n\t"
      "v_max3_f32 v74, v74, v26, v27\n\t"
      "v_max3_f32 v75, v75, v28, v29\n\t"
      "v_max3_f32 v75, v75, v30, v31\n\t"
      "v_max3_f32 v76, v76, v32, v33\n\t"
      "v_max3_f32 v76, v76, v34, v35\n\t"
      "v_max3_f32 v77, v77, v36, v37\n\t"
      "v_max3_f32 v77, v77, v38, v39\n\t"
      "v_max3_f32 v78, v78, v40, v41\n\t"
      "v_max3_f32 v78, v78, v42, v43\n\t"
      "s_sub_u32 s91, s91, 1\n\t"
      "s_cmp_lg_u32 s91, 0\n\t"
      "s_cbranch_scc1 FBL%=\n\t"
      "v_mov_b32 v248, v72\n\t"
      "s_nop 1\n\t"
      "v_permlane32_swap_b32 v72, v248\n\t"
      "v_max_f32 v72, v72, v248\n\t"
      "v_mov_b32 v248, v72\n\t"
      "s_nop 1\n\t"
      "v_permlane16_swap_b32 v72, v248\n\t"
      "v_max_f32 v72, v72, v248\n\t"
      "ds_write_b32 v79, v72 offset:0\n\t"
      "v_mov_b32 v249, v73\n\t"
      "s_nop 1\n\t"
      "v_permlane32_swap_b32 v73, v249\n\t"
      "v_max_f32 v73, v73, v249\n\t"
      "v_mov_b32 v249, v73\n\t"
      "s_nop 1\n\t"
      "v_permlane16_swap_b32 v73, v249\n\t"
      "v_max_f32 v73, v73, v249\n\t"
      "ds_write_b32 v79, v73 offset:256\n\t"
      "v_mov_b32 v248, v74\n\t"
      "s_nop 1\n\t"
      "v_permlane32_swap_b32 v74, v248\n\t"
      "v_max_f32 v74, v74, v248\n\t"
      "v_mov_b32 v248, v74\n\t"
      "s_nop 1\n\t"
      "v_permlane16_swap_b32 v74, v248\n\t"
      "v_max_f32 v74, v74, v248\n\t"
      "ds_write_b32 v79, v74 offset:512\n\t"
      "v_mov_b32 v249, v75\n\t"
      "s_nop 1\n\t"
      "v_permlane32_swap_b32 v75, v249\n\t"
      "v_max_f32 v75, v75, v249\n\t"
      "v_mov_b32 v249, v75\n\t"
      "s_nop 1\n\t"
      "v_permlane16_swap_b32 v75, v249\n\t"
      "v_max_f32 v75, v75, v249\n\t"
      "ds_write_b32 v79, v75 offset:768\n\t"
      "v_mov_b32 v248, v76\n\t"
      "s_nop 1\n\t"
      "v_permlane32_swap_b32 v76, v248\n\t"
      "v_max_f32 v76, v76, v248\n\t"
      "v_mov_b32 v248, v76\n\t"
      "s_nop 1\n\t"
      "v_permlane16_swap_b32 v76, v248\n\t"
      "v_max_f32 v76, v76, v248\n\t"
      "ds_write_b32 v79, v76 offset:1024\n\t"
      "v_mov_b32 v249, v77\n\t"
      "s_nop 1\n\t"
      "v_permlane32_swap_b32 v77, v249\n\t"
      "v_max_f32 v77, v77, v249\n\t"
      "v_mov_b32 v249, v77\n\t"
      "s_nop 1\n\t"
      "v_permlane16_swap_b32 v77, v249\n\t"
      "v_max_f32 v77, v77, v249\n\t"
      "ds_write_b32 v79, v77 offset:1280\n\t"
      "v_mov_b32 v248, v78\n\t"
      "s_nop 1\n\t"
      "v_permlane32_swap_b32 v78, v248\n\t"
      "v_max_f32 v78, v78, v248\n\t"
      "v_mov_b32 v248, v78\n\t"
      "s_nop 1\n\t"
      "v_permlane16_swap_b32 v78, v248\n\t"
      "v_max_f32 v78, v78, v248\n\t"
      "ds_write_b32 v79, v78 offset:1536\n\t"
      "s_add_u32 m0, s89, 0\n\t"
      "s_nop 0\n\t"
      "buffer_load_dwordx4 v12, s[36:39], s60 offen offset:0 nt lds\n\t"
      "buffer_load_dwordx4 v13, s[36:39], s61 offen offset:1024 nt lds\n\t"
      "buffer_load_dwordx4 v12, s[36:39], s62 offen offset:2048 nt lds\n\t"
      "buffer_load_dwordx4 v13, s[36:39], s63 offen offset:3072 nt lds\n\t"
      "s_add_u32 m0, s89, 4096\n\t"
      "s_nop 0\n\t"
      "buffer_load_dwordx4 v12, s[36:39], s64 offen offset:0 nt lds\n\t"
      "buffer_load_dwordx4 v13, s[36:39], s65 offen offset:1024 nt lds\n\t"
      "buffer_load_dwordx4 v12, s[36:39], s66 offen offset:2048 nt lds\n\t"
      "buffer_load_dwordx4 v13, s[36:39], s67 offen offset:3072 nt lds\n\t"
      "s_add_u32 s36, s36, 32768\n\t"
      "s_addc_u32 s37, s37, 0\n\t"
      "s_waitcnt vmcnt(0)\n\t"
      "ds_read_b128 v[104:107], v4 offset:0\n\t"
      "ds_read_b128 v[108:111], v5 offset:0\n\t"
      "ds_read_b128 v[112:115], v4 offset:2048\n\t"
      "ds_read_b128 v[116:119], v5 offset:2048\n\t"
      "ds_read_b128 v[120:123], v4 offset:4096\n\t"
      "ds_read_b128 v[124:127], v5 offset:4096\n\t"
      "ds_read_b128 v[128:131], v4 offset:6144\n\t"
      "ds_read_b128 v[132:135], v5 offset:6144\n\t"
      "s_waitcnt lgkmcnt(0)\n\t"
      "s_add_u32 m0, s89, 32768\n\t"
      "s_nop 0\n\t"
      "buffer_load_dwordx4 v12, s[36:39], s60 offen offset:0 nt lds\n\t"
      "buffer_load_dwordx4 v13, s[36:39], s61 offen offset:1024 nt lds\n\t"
      "buffer_load_dwordx4 v12, s[36:39], s62 offen offset:2048 nt lds\n\t"
      "buffer_load_dwordx4 v13, s[36:39], s63 offen offset:3072 nt lds\n\t"
      "s_add_u32 m0, s89, 36864\n\t"
      "s_nop 0\n\t"
      "buffer_load_dwordx4 v12, s[36:39], s64 offen offset:0 nt lds\n\t"
      "buffer_load_dwordx4 v13, s[36:39], s65 offen offset:1024 nt lds\n\t"
      "buffer_load_dwordx4 v12, s[36:39], s66 offen offset:2048 nt lds\n\t"
      "buffer_load_dwordx4 v13, s[36:39], s67 offen offset:3072 nt lds\n\t"
      "s_add_u32 s36, s36, 32768\n\t"
      "s_addc_u32 s37, s37, 0\n\t"
      "s_add_u32 m0, s89, 0\n\t"
      "s_nop 0\n\t"
      "buffer_load_dwordx4 v12, s[36:39], s60 offen offset:0 nt lds\n\t"
      "buffer_load_dwordx4 v13, s[36:39], s61 offen offset:1024 nt lds\n\t"
      "buffer_load_dwordx4 v12, s[36:39], s62 offen offset:2048 nt lds\n\t"
      "buffer_load_dwordx4 v13, s[36:39], s63 offen offset:3072 nt lds\n\t"
      "s_add_u32 m0, s89, 4096\n\t"
      "s_nop 0\n\t"
      "buffer_load_dwordx4 v12, s[36:39], s64 offen offset:0 nt lds\n\t"
      "buffer_load_dwordx4 v13, s[36:39], s65 offen offset:1024 nt lds\n\t"
      "buffer_load_dwordx4 v12, s[36:39], s66 offen offset:2048 nt lds\n\t"
      "buffer_load_dwordx4 v13, s[36:39], s67 offen offset:3072 nt lds\n\t"
      "s_add_u32 s36, s36, 32768\n\t"
      "s_addc_u32 s37, s37, 0\n\t"
      "s_mov_b32 s40, s12\n\t"
      "s_and_b32 s41, s13, 0xffff\n\t"
      "s_add_u32 m0, s89, 65536\n\t"
      "s_nop 0\n\t"
      "buffer_load_dwordx4 v14, s[40:43], s68 offen offset:0 nt lds\n\t"
      "buffer_load_dwordx4 v14, s[40:43], s69 offen offset:1024 nt lds\n\t"
      "buffer_load_dwordx4 v14, s[40:43], s70 offen offset:2048 nt lds\n\t"
      "buffer_load_dwordx4 v14, s[40:43], s71 offen offset:3072 nt lds\n\t"
      "s_add_u32 m0, s89, 69632\n\t"
      "s_nop 0\n\t"
      "buffer_load_dwordx4 v14, s[40:43], s72 offen offset:0 nt lds\n\t"
      "buffer_load_dwordx4 v14, s[40:43], s73 offen offset:1024 nt lds\n\t"
      "buffer_load_dwordx4 v14, s[40:43], s74 offen offset:2048 nt lds\n\t"
      "buffer_load_dwordx4 v14, s[40:43], s75 offen offset:3072 nt lds\n\t"
      "s_add_u32 s40, s40, 32768\n\t"
      "s_addc_u32 s41, s41, 0\n\t"
      "s_waitcnt vmcnt(0)\n\t"
      "s_waitcnt lgkmcnt(0)\n\t"
      "s_barrier\n\t"
      "s_branch X1P%=\n\t"
      "FIN%=:\n\t"
      :
      : "{s[0:1]}"(__builtin_amdgcn_kernarg_segment_ptr()), "{s2}"(blockIdx.x), "{v0}"(threadIdx.x)
      : "v1", "v2", "v3", "v4", "v5", "v6", "v7", "v8", "v9", "v10", "v11", "v12", "v13", "v14", "v15", "v16", "v17", "v18", "v19", "v20", "v21", "v22", "v23", "v24", "v25", "v26", "v27", "v28", "v29", "v30", "v31", "v32", "v33", "v34", "v35", "v36", "v37", "v38", "v39", "v40", "v41", "v42", "v43", "v44", "v45", "v46", "v47", "v48", "v49", "v50", "v51", "v52", "v53", "v54", "v55", "v56", "v57", "v58", "v59", "v60", "v61", "v62", "v63", "v64", "v65", "v66", "v67", "v68", "v69", "v70", "v71", "v72", "v73", "v74", "v75", "v76", "v77", "v78", "v79", "v80", "v81", "v82", "v83", "v84", "v85", "v86", "v87", "v88", "v89", "v90", "v91", "v92", "v93", "v94", "v95", "v96", "v97", "v98", "v99", "v100", "v101", "v102", "v103", "v104", "v105", "v106", "v107", "v108", "v109", "v110", "v111", "v112", "v113", "v114", "v115", "v116", "v117", "v118", "v119", "v120", "v121", "v122", "v123", "v124", "v125", "v126", "v127", "v128", "v129", "v130", "v131", "v132", "v133", "v134", "v135", "v136", "v137", "v138", "v139", "v140", "v141", "v142", "v143", "v144", "v145", "v146", "v147", "v148", "v149", "v150", "v151", "v152", "v153", "v154", "v155", "v156", "v157", "v158", "v159", "v160", "v161", "v162", "v163", "v164", "v165", "v166", "v167", "v168", "v169", "v170", "v171", "v172", "v173", "v174", "v175", "v176", "v177", "v178", "v179", "v180", "v181", "v182", "v183", "v184", "v185", "v186", "v187", "v188", "v189", "v190", "v191", "v192", "v193", "v194", "v195", "v196", "v197", "v198", "v199", "v200", "v201", "v202", "v203", "v204", "v205", "v206", "v207", "v208", "v209", "v210", "v211", "v212", "v213", "v214", "v215", "v216", "v217", "v218", "v219", "v220", "v221", "v222", "v223", "v224", "v225", "v226", "v227", "v228", "v229", "v230", "v231", "v232", "v233", "v234", "v235", "v236", "v237", "v238", "v239", "v240", "v241", "v242", "v243", "v244", "v245", "v246", "v247", "v248", "v249", "v250", "v251", "v252", "v253", "v254", "v255", "a0", "a1", "a2", "a3", "a4", "a5", "a6", "a7", "a8", "a9", "a10", "a11", "a12", "a13", "a14", "a15", "a16", "a17", "a18", "a19", "a20", "a21", "a22", "a23", "a24", "a25", "a26", "a27", "a28", "a29", "a30", "a31", "a32", "a33", "a34", "a35", "a36", "a37", "a38", "a39", "a40", "a41", "a42", "a43", "a44", "a45", "a46", "a47", "a48", "a49", "a50", "a51", "a52", "a53", "a54", "a55", "a56", "a57", "a58", "a59", "a60", "a61", "a62", "a63", "a64", "a65", "a66", "a67", "a68", "a69", "a70", "a71", "a72", "a73", "a74", "a75", "a76", "a77", "a78", "a79", "a80", "a81", "a82", "a83", "a84", "a85", "a86", "a87", "a88", "a89", "a90", "a91", "a92", "a93", "a94", "a95", "a96", "a97", "a98", "a99", "a100", "a101", "a102", "a103", "a104", "a105", "a106", "a107", "a108", "a109", "a110", "a111", "a112", "a113", "a114", "a115", "a116", "a117", "a118", "a119", "a120", "a121", "a122", "a123", "a124", "a125", "a126", "a127", "a128", "a129", "a130", "a131", "a132", "a133", "a134", "a135", "a136", "a137", "a138", "a139", "a140", "a141", "a142", "a143", "a144", "a145", "a146", "a147", "a148", "a149", "a150", "a151", "a152", "a153", "a154", "a155", "a156", "a157", "a158", "a159", "a160", "a161", "a162", "a163", "a164", "a165", "a166", "a167", "a168", "a169", "a170", "a171", "a172", "a173", "a174", "a175", "a176", "a177", "a178", "a179", "a180", "a181", "a182", "a183", "a184", "a185", "a186", "a187", "a188", "a189", "a190", "a191", "a192", "a193", "a194", "a195", "a196", "a197", "a198", "a199", "a200", "a201", "a202", "a203", "a204", "a205", "a206", "a207", "a208", "a209", "a210", "a211", "a212", "a213", "a214", "a215", "a216", "a217", "a218", "a219", "a220", "a221", "a222", "a223", "a224", "a225", "a226", "a227", "a228", "a229", "a230", "a231", "a232", "a233", "a234", "a235", "a236", "a237", "a238", "a239", "a240", "a241", "a242", "a243", "a244", "a245", "a246", "a247", "a248", "a249", "a250", "a251", "a252", "a253", "a254", "a255", "s3", "s4", "s5", "s6", "s7", "s8", "s9", "s10", "s11", "s12", "s13", "s14", "s15", "s16", "s17", "s18", "s19", "s20", "s21", "s22", "s23", "s24", "s25", "s26", "s27", "s28", "s29", "s30", "s31", "s34", "s35", "s36", "s37", "s38", "s39", "s40", "s41", "s42", "s43", "s44", "s45", "s46", "s47", "s48", "s49", "s50", "s51", "s52", "s53", "s54", "s55", "s56", "s57", "s58", "s59", "s60", "s61", "s62", "s63", "s64", "s65", "s66", "s67", "s68", "s69", "s70", "s71", "s72", "s73", "s74", "s75", "s76", "s77", "s78", "s79", "s80", "s81", "s82", "s83", "s84", "s85", "s86", "s87", "s88", "s89", "s90", "s91", "s92", "s93", "s94", "s95", "s96", "s97", "s98", "s99", "s100", "s101", "vcc", "scc", "m0", "memory");
}

extern "C" void kernel_launch(void* const* d_in, const int* in_sizes, int n_in, void* d_out, int out_size, void* d_ws, size_t ws_size, hipStream_t stream) {
  hipFuncSetAttribute((const void*)attn_fused, hipFuncAttributeMaxDynamicSharedMemorySize, 163840);
  attn_fused<<<256, 256, 163840, stream>>>((const char*)d_in[0], (const char*)d_in[1], (const char*)d_in[2], (const char*)d_in[3], (const char*)d_in[4], (char*)d_ws, (char*)d_out);
}
```
